```python
import torch

def _softmax(x, axis=-1):
    return torch.softmax(x, dim=axis)
def _gelu(x):
    return torch.nn.functional.gelu(x, approximate="tanh")
def _silu(x):
    return torch.nn.functional.silu(x)
def _sigmoid(x):
    return torch.sigmoid(x)
def _layernorm(x, axis=-1, eps=1e-5):
    m = x.mean(dim=axis, keepdim=True)
    v = x.var(dim=axis, keepdim=True, unbiased=False)
    return (x - m) / torch.sqrt(v + eps)
def _scatter_add(tgt, idx, src, axis=0):
    return tgt.clone().index_add_(axis, idx, src)
def _segment_sum(src, seg, n):
    out = torch.zeros((n,) + src.shape[1:], dtype=src.dtype)
    return out.index_add_(0, seg, src)
def _pad(x, pw):
    flat = tuple(v for pair in reversed(pw) for v in pair)
    return torch.nn.functional.pad(x, flat)

kernel_name = 'sequential_preLN__ssm_conv_h3__geglu_ffn__prompt'

def setup_inputs():

    _w = (lambda a: (lambda t: t.to(torch.bfloat16) if t.is_floating_point() else t)(torch.as_tensor(a)))
    def _A_setup():
        B, S, D = 1, 2048, 448
        ks = 4
        return {
            "q": _w((lambda *s: torch.randn(*s, dtype=torch.bfloat16) * 0.05)(B, S, D)),
            "k": _w((lambda *s: torch.randn(*s, dtype=torch.bfloat16) * 0.4)(B, S, D)),
            "v": _w((lambda *s: torch.randn(*s, dtype=torch.bfloat16) * 0.4)(B, S, D)),
            "shift_k": _w((lambda *s: torch.randn(*s, dtype=torch.bfloat16) * 0.4)(D, ks) * 0.5),
            "ssm_k": _w((lambda *s: torch.randn(*s, dtype=torch.bfloat16) * 0.4)(D, S) * (1.0 / S ** 0.5)),
            "ssm_D": _w((lambda *s: torch.randn(*s, dtype=torch.bfloat16) * 0.4)(D)),
        }
    _A_in = _A_setup()

    def _B_setup():
        B, S, D, F = 1, 2048, 448, 1792
        return {
            "x": _w((lambda *s: torch.randn(*s, dtype=torch.bfloat16) * 0.05)(B, S, D)),
            "w_gate": _w((lambda *s: torch.randn(*s, dtype=torch.bfloat16) * 0.05)(D, F)),
            "w_up": _w((lambda *s: torch.randn(*s, dtype=torch.bfloat16) * 0.05)(D, F)),
            "w_down": _w((lambda *s: torch.randn(*s, dtype=torch.bfloat16) * 0.05)(F, D)),
        }
    _B_in = _B_setup()

    return {
        "A_q": _A_in["q"],
        "A_k": _A_in["k"],
        "A_v": _A_in["v"],
        "A_shift_k": _A_in["shift_k"],
        "A_ssm_k": _A_in["ssm_k"],
        "A_ssm_D": _A_in["ssm_D"],
        "B_x": _B_in["x"],
        "B_w_gate": _B_in["w_gate"],
        "B_w_up": _B_in["w_up"],
        "B_w_down": _B_in["w_down"],
    }

def reference(A_q, A_k, A_v, A_shift_k, A_ssm_k, A_ssm_D, B_x, B_w_gate, B_w_up, B_w_down):
    def _A_ref(q, k, v, shift_k, ssm_k, ssm_D):
        B, S, D = q.shape
        ks = shift_k.shape[-1]
        k_pad = _pad(k, ((0, 0), (ks - 1, 0), (0, 0)))
        k_shift = torch.zeros_like(k)
        for t in range(ks):
            k_shift = k_shift + k_pad[:, t:t + S, :] * shift_k[:, ks - 1 - t][None, None, :]
        kv = k_shift * v
        L = 2 * S
        kv_pad = _pad(kv, ((0, 0), (0, S), (0, 0)))
        kf_pad = _pad(ssm_k, ((0, 0), (0, S)))
        kv_f = (lambda x, axes=(-2,-1): torch.fft.rfft2(x.float(), dim=axes))(kv_pad[:, None, :, :], axes=(1, 2))
        kf_f = (lambda x, axes=(-2,-1): torch.fft.rfft2(x.float(), dim=axes))(kf_pad[:, None, :], axes=(1, 2))
        kf_f = (lambda x, *d: x.permute(*d))(kf_f, 1, 2, 0)
        y_f = kv_f * kf_f[None, :, :, :]
        y = (lambda x, s, axes=(-2,-1): torch.fft.irfft2(x, s=s, dim=axes).to(torch.bfloat16))(y_f, (1, L), axes=(1, 2))[:, 0, :S, :]
        y = y + kv * ssm_D[None, None, :]
        return q * y

    def _B_ref(x, w_gate, w_up, w_down):
        return (_gelu(x @ w_gate) * (x @ w_up)) @ w_down

    x = A_q
    h = x + _A_ref(_layernorm(x, axis=-1), A_k, A_v, A_shift_k, A_ssm_k, A_ssm_D)
    h2 = h + _B_ref(_layernorm(h, axis=-1), B_w_gate, B_w_up, B_w_down)
    return h2
```

```cpp
#include <hip/hip_runtime.h>
#include <hip/hip_bf16.h>
#include <stdint.h>

#define S_ 2048
#define D_ 448
#define F_ 1792

__device__ __forceinline__ float bf2f(uint16_t x){ return __uint_as_float(((uint32_t)x)<<16); }
__device__ __forceinline__ uint16_t f2bf(float f){ __hip_bfloat16 b = __float2bfloat16(f); return *(uint16_t*)&b; }
__device__ __forceinline__ float rbf(float f){ return bf2f(f2bf(f)); }

__global__ void k_kv(const uint16_t* k, const uint16_t* v, const uint16_t* sk, uint16_t* kvT){
  int i = blockIdx.x*256+threadIdx.x; int s=i/D_, d=i%D_;
  float acc=0.f;
  for(int t=0;t<4;t++){ int ss=s+t-3; float kk = ss>=0? bf2f(k[ss*D_+d]):0.f; acc = rbf(acc + rbf(kk*bf2f(sk[d*4+3-t]))); }
  kvT[d*S_+s]=f2bf(acc*bf2f(v[i]));
}
__global__ void k_conv(const uint16_t* kvT, const uint16_t* f, const uint16_t* Dv, uint16_t* y){
  int d=blockIdx.x;
  __shared__ float u[S_], ff[S_];
  for(int i=threadIdx.x;i<S_;i+=256){u[i]=bf2f(kvT[d*S_+i]); ff[i]=bf2f(f[d*S_+i]);}
  __syncthreads();
  for(int s=threadIdx.x;s<S_;s+=256){
    float a=0.f; for(int j=0;j<=s;j++) a+=u[s-j]*ff[j];
    float yy=rbf(a); float t=rbf(u[s]*bf2f(Dv[d]));
    y[s*D_+d]=f2bf(yy+t);
  }
}
__global__ void k_ln(const uint16_t* x, const uint16_t* y, uint16_t* h, uint16_t* xn){
  int s=blockIdx.x; int t=threadIdx.x;
  float xv[7], hv[7]; float sum=0;
  for(int i=0;i<7;i++){xv[i]=bf2f(x[s*D_+t+64*i]); sum+=xv[i];}
  for(int o=32;o>0;o>>=1) sum+=__shfl_xor(sum,o);
  float m=sum/D_; float q=0; for(int i=0;i<7;i++){q+=(xv[i]-m)*(xv[i]-m);} for(int o=32;o>0;o>>=1) q+=__shfl_xor(q,o);
  float r=rsqrtf(q/D_+1e-5f);
  sum=0;
  for(int i=0;i<7;i++){ float qq=rbf((xv[i]-m)*r); hv[i]=rbf(xv[i]+rbf(qq*bf2f(y[s*D_+t+64*i]))); h[s*D_+t+64*i]=f2bf(hv[i]); sum+=hv[i];}
  for(int o=32;o>0;o>>=1) sum+=__shfl_xor(sum,o);
  m=sum/D_; q=0; for(int i=0;i<7;i++){q+=(hv[i]-m)*(hv[i]-m);} for(int o=32;o>0;o>>=1) q+=__shfl_xor(q,o);
  r=rsqrtf(q/D_+1e-5f);
  for(int i=0;i<7;i++) xn[s*D_+t+64*i]=f2bf((hv[i]-m)*r);
}
__global__ void k_g1(const uint16_t* xn, const uint16_t* wg, const uint16_t* wu, uint16_t* H){
  int s=blockIdx.y; int f=blockIdx.x*256+threadIdx.x;
  float g=0,u=0; for(int d=0;d<D_;d++){float a=bf2f(xn[s*D_+d]); g+=a*bf2f(wg[d*F_+f]); u+=a*bf2f(wu[d*F_+f]);}
  g=rbf(g); u=rbf(u);
  float ge=0.5f*g*(1.f+tanhf(0.7978845608f*(g+0.044715f*g*g*g)));
  H[s*F_+f]=f2bf(rbf(ge)*u);
}
__global__ void k_g2(const uint16_t* H, const uint16_t* wd, const uint16_t* h, uint16_t* out){
  int s=blockIdx.y; int n=blockIdx.x*64+threadIdx.x;
  float a=0; for(int f=0;f<F_;f++) a+=bf2f(H[s*F_+f])*bf2f(wd[f*D_+n]);
  out[s*D_+n]=f2bf(bf2f(h[s*D_+n])+rbf(a));
}
extern "C" void kernel_launch(void* const* d_in, const int* in_sizes, int n_in,
                              void* d_out, int out_size, void* d_ws, size_t ws_size,
                              hipStream_t stream) {
  const uint16_t* q=(const uint16_t*)d_in[0]; const uint16_t* k=(const uint16_t*)d_in[1]; const uint16_t* v=(const uint16_t*)d_in[2];
  const uint16_t* sk=(const uint16_t*)d_in[3]; const uint16_t* f=(const uint16_t*)d_in[4]; const uint16_t* Dv=(const uint16_t*)d_in[5];
  const uint16_t* wg=(const uint16_t*)d_in[7]; const uint16_t* wu=(const uint16_t*)d_in[8]; const uint16_t* wd=(const uint16_t*)d_in[9];
  uint16_t* ws=(uint16_t*)d_ws; uint16_t* kvT=ws; uint16_t* y=ws+S_*D_; uint16_t* h=ws+2*S_*D_; uint16_t* xn=ws+3*S_*D_; uint16_t* H=ws+4*S_*D_;
  k_kv<<<S_*D_/256,256,0,stream>>>(k,v,sk,kvT);
  k_conv<<<D_,256,0,stream>>>(kvT,f,Dv,y);
  k_ln<<<S_,64,0,stream>>>(q,y,h,xn);
  k_g1<<<dim3(F_/256,S_),256,0,stream>>>(xn,wg,wu,H);
  k_g2<<<dim3(D_/64,S_),64,0,stream>>>(H,wd,h,(uint16_t*)d_out);
}
```

```cpp
#include <hip/hip_runtime.h>
#include <stdint.h>

namespace {

#define LAS __attribute__((address_space(3)))
#define GAS __attribute__((address_space(1)))
#define DI __device__ __forceinline__
#define AI __attribute__((always_inline))
typedef uint32_t u32;
typedef unsigned long long u64;
typedef u32 u32x2 __attribute__((ext_vector_type(2)));
typedef u32 u32x4 __attribute__((ext_vector_type(4)));
typedef short s16x4 __attribute__((ext_vector_type(4)));
typedef short s16x8 __attribute__((ext_vector_type(8)));
typedef float f32x4 __attribute__((ext_vector_type(4)));
typedef float f32x16 __attribute__((ext_vector_type(16)));
typedef __bf16 bf2 __attribute__((ext_vector_type(2)));
typedef __amdgpu_buffer_rsrc_t rsrc_t;
#define RLX_AGENT __ATOMIC_RELAXED, __HIP_MEMORY_SCOPE_AGENT

constexpr int S_ = 2048, D_ = 448, F_ = 1792;
constexpr u32 TB = 448 * 64;
constexpr u32 WS_E  = 0;
constexpr u32 WS_F1 = 128;
constexpr u32 WS_F3 = WS_F1 + 256 * 128;
constexpr u32 WS_F2 = WS_F3 + 896 * 128;
constexpr u32 WS_S2 = 256 * 1024 + 16384;
constexpr u32 WS_CS = 224 * 1024;
constexpr u32 WS_ST = 256 * 1024;
constexpr u32 WS_U  = WS_S2 + 32768;
constexpr u32 WS_X  = WS_U + 64 * TB;
constexpr u32 WS_H  = WS_X + 64 * TB;
constexpr u32 WS_G  = WS_H + 64 * TB;
constexpr u32 WS_END = WS_G + 64 * 112 * 1024;
constexpr u32 WS_DBG = 16u << 20;
constexpr int SC1 = 16;
constexpr u32 OOB = 0xFFFFFF00u;
constexpr u32 ONES = 0x3f803f80u;
constexpr u32 EMPTY = 0xffffffffu;

constexpr u32 L_K = 0, L_V = 9216, L_X = 17408, L_SK = 25600;
constexpr u32 L_RED = 0, L_FC = 32768, FCS = 4160, FCH = 4 * FCS;
constexpr u32 L_U = L_FC + 2 * FCH, UCH = 4608, L_RING = L_U + 2 * UCH;
constexpr u32 L_H0 = 32768, L_H1 = L_H0 + TB, L_WE = L_H1 + TB, L_H2 = 0, L_H3 = L_WE + 32768, L_SP = 155648;
constexpr u32 L_P = TB, L_Q = L_P + 32768, L_R = L_Q + 32768, L_R2 = L_H3 + TB;
constexpr u32 L_RES = 0;
constexpr u32 LDS_BYTES = 163840;

DI float bflo(u32 x) { return __uint_as_float(x << 16); }
DI float bfhi(u32 x) { return __uint_as_float(x & 0xffff0000u); }
DI u32 pk(float lo, float hi) { bf2 p; p.x = (__bf16)lo; p.y = (__bf16)hi; return __builtin_bit_cast(u32, p); }
DI rsrc_t mk(const void* p, u32 bytes) { return __builtin_amdgcn_make_buffer_rsrc((void*)p, 0, bytes, 0x00020000); }
DI u32x2 trd(const char* lds, u32 a) { return __builtin_bit_cast(u32x2, __builtin_amdgcn_ds_read_tr16_b64_v4i16((LAS s16x4*)(lds + a))); }
DI u32x4 cat(u32x2 a, u32x2 b) { return u32x4{a.x, a.y, b.x, b.y}; }
DI u32x4 srd(const void* p, u32 bytes) { const u64 a = (u64)p; return u32x4{(u32)a, (u32)(a >> 32) & 0xffffu, bytes, 0x00020000u}; }
template <int AUX = 0> DI void dma(u32x4 r, u32 ldsoff, u32 voff, u32 soff) {
  if (AUX) asm volatile("s_mov_b32 m0, %0\n\ts_nop 0\n\tbuffer_load_dwordx4 %1, %2, %3 offen sc1 lds" :: "s"(ldsoff), "v"(voff), "s"(r), "s"(soff) : "memory");
  else asm volatile("s_mov_b32 m0, %0\n\ts_nop 0\n\tbuffer_load_dwordx4 %1, %2, %3 offen lds" :: "s"(ldsoff), "v"(voff), "s"(r), "s"(soff) : "memory");
}
DI float fnma(float a, float b, float c) { float r; asm("v_fma_f32 %0, -%1, %2, %3" : "=v"(r) : "v"(a), "v"(b), "v"(c)); return r; }
DI float op(float x) { asm("" : "+v"(x)); return x; }
DI u32 opq(u32 x) { asm volatile("" : "+v"(x)); return x; }
#define PIN(x) asm volatile("" : "+v"(x))
DI f32x4 mfma16(u32x4 a, u32x4 b, f32x4 c) { return __builtin_amdgcn_mfma_f32_16x16x32_bf16(__builtin_bit_cast(s16x8, a), __builtin_bit_cast(s16x8, b), c, 0, 0, 0); }
DI f32x16 mfma32(u32x4 a, u32x4 b, f32x16 c) { return __builtin_amdgcn_mfma_f32_32x32x16_bf16(__builtin_bit_cast(s16x8, a), __builtin_bit_cast(s16x8, b), c, 0, 0, 0); }
DI float dot2(u32 a, u32 b, float c) { return __builtin_amdgcn_fdot2_f32_bf16(__builtin_bit_cast(bf2, a), __builtin_bit_cast(bf2, b), c, false); }
#define VMW(n) asm volatile("s_waitcnt vmcnt(" #n ")" ::: "memory")
#define LG0() asm volatile("s_waitcnt lgkmcnt(0)" ::: "memory")
#define SB() __builtin_amdgcn_sched_barrier(0)
DI void bar() { asm volatile("s_waitcnt lgkmcnt(0)\n\ts_barrier" ::: "memory"); }
#define RBAR() asm volatile("s_barrier" ::: "memory")
DI float wsum(float v) {
  v += __builtin_amdgcn_update_dpp(0.f, v, 0x111, 0xf, 0xf, true);
  v += __builtin_amdgcn_update_dpp(0.f, v, 0x112, 0xf, 0xf, true);
  v += __builtin_amdgcn_update_dpp(0.f, v, 0x114, 0xf, 0xf, true);
  v += __builtin_amdgcn_update_dpp(0.f, v, 0x118, 0xf, 0xf, true);
  v += __builtin_amdgcn_update_dpp(0.f, v, 0x142, 0xa, 0xf, true);
  v += __builtin_amdgcn_update_dpp(0.f, v, 0x143, 0xc, 0xf, true);
  return __uint_as_float(__builtin_amdgcn_readlane(__float_as_uint(v), 63));
}
DI float hsum(float v) {
  const auto r = __builtin_amdgcn_permlane32_swap(__float_as_uint(v), __float_as_uint(v), false, false);
  return __uint_as_float(r[0]) + __uint_as_float(r[1]);
}
template <int SLEEP = 1> DI void poll(char* ws, u32 off, bool act, u64 tag) {
  bool ok = !act;
#pragma unroll 1
  for (int it = 0; it < 20000; ++it) {
    u64 x = tag;
    if (!ok) x = __hip_atomic_load((GAS u64*)(ws + off), RLX_AGENT);
    ok = (x == tag);
    if (__all(ok)) break;
    __builtin_amdgcn_s_sleep(SLEEP);
  }
  asm volatile("" ::: "memory");
}
DI u32 max4(u32x4 v) { return max(max(v.x, v.y), max(v.z, v.w)); }
DI u64 flag_ld(u32x4 r, u32 voff) { u64 v; asm volatile("buffer_load_dwordx2 %0, %1, %2, 0 offen sc1" : "=v"(v) : "v"(voff), "s"(r) : "memory"); return v; }
DI u32x4 ldg(u32x4 r, u32 voff, u32 soff) { u32x4 v; asm volatile("buffer_load_dwordx4 %0, %1, %2, %3 offen sc1" : "=v"(v) : "v"(voff), "s"(r), "s"(soff) : "memory"); return v; }
template <int N> DI void vmw(u32x4& x) { asm volatile("s_waitcnt vmcnt(%1)" : "+v"(x) : "n"(N) : "memory"); }
#define VMWP(n, x) asm volatile("s_waitcnt vmcnt(" #n ")" : "+v"(x) :: "memory")
DI void setflag(char* ws, u32 off, u64 tag) { __hip_atomic_store((GAS u64*)(ws + off), tag, RLX_AGENT); }

#define STAMP(i)
#define STAMPC(i)
#define STAMP_OUT()

DI float xsum(float a, float c) {
  const auto r = __builtin_amdgcn_permlane32_swap(__float_as_uint(a), __float_as_uint(c), false, false);
  return __uint_as_float(r[0]) + __uint_as_float(r[1]);
}
DI f32x4 ln2_terms(float t) {
  const auto r = __builtin_amdgcn_permlane32_swap(__float_as_uint(t), __float_as_uint(t), false, false);
  const float mean = __uint_as_float(r[0]) * (1.f / D_);
  const float V = fmaxf(__uint_as_float(r[1]) * (1.f / D_) - mean * mean, 0.f) + 1e-5f;
  const float rs = __builtin_amdgcn_rsqf(V);
  const float K1 = -2.f * 0.7978845608028654f * 1.4426950408889634f * rs;
  const float lo = -mean - bflo(pk(-mean, 0.f));
  return f32x4{__uint_as_float(pk(-mean, lo)), V, K1, (K1 * rs) * (0.044715f * rs)};
}
template <int N> struct IC { static constexpr int value = N; };
template <int N, int I = 0, class F> DI void sfor(F&& f) { if constexpr (I < N) { f(IC<I>{}); sfor<N, I + 1>(f); } }
struct W1 { u32x4 rg, ru; u32 vg, vu, ra, lb; };
template <int T> DI void w1_g(const W1& c, u32 base) { dma(c.rg, c.lb + base, c.vg, T * (32 * F_ * 2)); }
template <int T> DI void w1_u(const W1& c, u32 base) { dma(c.ru, c.lb + base + 8192, c.vu, T * (32 * F_ * 2)); }
template <int T> DI void w1_issue(const W1& c, u32 base) { w1_g<T>(c, base); w1_u<T>(c, base); }
template <int T> DI void w1_read(const char* lds, u32 a, u32x4 (&W)[28]) {
#pragma unroll
  for (int kk = 0; kk < 2; ++kk) W[2 * T + kk] = cat(trd(lds, a + kk * 4096), trd(lds, a + kk * 4096 + 1024));
}
DI float x8sum(float v) {
  v += __builtin_amdgcn_update_dpp(0.f, v, 0xb1, 0xf, 0xf, true);
  v += __builtin_amdgcn_update_dpp(0.f, v, 0x4e, 0xf, 0xf, true);
  v += __builtin_amdgcn_update_dpp(0.f, v, 0x141, 0xf, 0xf, true);
  return v;
}

__global__ __launch_bounds__(512, 2) void h3_ffn(const char* __restrict__ gq, const char* __restrict__ gk, const char* __restrict__ gv,
                                                 const char* __restrict__ gsk, const char* __restrict__ gf, const char* __restrict__ gD,
                                                 const char* __restrict__ gwg, const char* __restrict__ gwu, const char* __restrict__ gwd,
                                                 char* __restrict__ gout, char* ws) {
  __shared__ __attribute__((aligned(16))) char lds[LDS_BYTES];
  const int tid = threadIdx.x, l = tid & 63, w = __builtin_amdgcn_readfirstlane(tid >> 6);
  const int b = blockIdx.x, xc = b & 7, sl = b >> 3;
  const int tb64 = 4 * xc + (sl >> 3), role = sl & 7;
  const int sc = (sl >> 3) * 7 + role;
  asm volatile("" :: "s"(gq), "s"(gk), "s"(gv), "s"(gsk), "s"(gf), "s"(gD), "s"(gwg), "s"(gwu), "s"(gwd), "s"(gout), "s"(ws));
  STAMP(0);
  const rsrc_t rw = mk(ws, WS_END);
  const int G = l >> 4, i16 = l & 15, q4 = i16 >> 2, p4 = l & 3, r8 = l >> 3;
  auto mktag = [&](u64 e0) AI { return ((u64)__builtin_amdgcn_readfirstlane((u32)e0) | ((u64)__builtin_amdgcn_readfirstlane((u32)(e0 >> 32)) << 32)) * 6364136223846793005ull + 1442695040888963407ull; };
  if (role == 7) {
    const rsrc_t rq = mk(gq, S_ * D_ * 2);
    const int row = 64 * tb64 + 8 * w + r8;
    u32x4 xs[7];
#pragma unroll
    for (int i = 0; i < 7; ++i) xs[i] = __builtin_amdgcn_raw_buffer_load_b128(rq, (u32)(row * 896 + (l & 7) * 16 + i * 128), 0, 0);
    const u64 e0 = __hip_atomic_load((GAS u64*)(ws + WS_E), RLX_AGENT);
    SB();
    SB();
    const u64 tag = mktag(e0);
    float s[4] = {};
#pragma unroll
    for (int i = 0; i < 7; ++i)
#pragma unroll
      for (int e = 0; e < 4; ++e) s[e] += bflo(xs[i][e]) + bfhi(xs[i][e]);
    const float mean = x8sum((s[0] + s[1]) + (s[2] + s[3])) * (1.f / D_);
    float v[4] = {};
#pragma unroll
    for (int i = 0; i < 7; ++i)
#pragma unroll
      for (int e = 0; e < 4; ++e) { const float d0 = bflo(xs[i][e]) - mean, d1 = bfhi(xs[i][e]) - mean; v[e] += d0 * d0; v[e] += d1 * d1; }
    const float rstd = __builtin_amdgcn_rsqf(x8sum((v[0] + v[1]) + (v[2] + v[3])) * (1.f / D_) + 1e-5f);
    const u32 so = WS_ST + (row >> 8) * 2048 + ((row >> 1) & 3) * 512 + ((row >> 5) & 7) * 64 + ((row >> 3) & 3) * 16 + (row & 1) * 8;
    __builtin_amdgcn_raw_buffer_store_b64(u32x2{__float_as_uint(mean), __float_as_uint(rstd)}, rw, (l & 7) == 0 ? so : OOB, 0, SC1);
    VMW(0); bar();
    if (tid == 0) setflag(ws, WS_F1 + (tb64 * 8 + 7) * 128, tag);
#pragma unroll
    for (int i = 0; i < 7; ++i) __builtin_amdgcn_raw_buffer_store_b128(u32x4{EMPTY, EMPTY, EMPTY, EMPTY}, rw, (u32)(tid * 16), WS_H + (u32)(tb64 * 2 * TB + i * 8192), SC1);
    __builtin_amdgcn_raw_buffer_store_b128(u32x4{EMPTY, EMPTY, EMPTY, EMPTY}, rw, tid < 64 ? WS_S2 + (u32)(tb64 * 1024 + tid * 16) : tid < 120 ? WS_CS + (u32)(tb64 * 896 + (tid - 64) * 16) : OOB, 0, SC1);
    VMW(0); bar();
    if (tid == 0) setflag(ws, WS_F2 + tb64 * 128, tag);
    STAMP(1);
    {
      const int mat = tb64 >= 14, j = tb64 < 28 ? tb64 - 14 * mat : 0;
      const rsrc_t rm = mk(mat ? gwu : gwg, D_ * F_ * 2);
      u32x4 wv[14];
#pragma unroll
      for (int i = 0; i < 14; ++i) wv[i] = __builtin_amdgcn_raw_buffer_load_b128(rm, tb64 < 28 ? (u32)(((4 * w + G + 32 * i) * F_ + 128 * j) * 2 + i16 * 16) : OOB, 0, 0);
      float c[8] = {};
#pragma unroll
      for (int i = 0; i < 14; ++i)
#pragma unroll
        for (int e = 0; e < 4; ++e) { c[2 * e] += bflo(wv[i][e]); c[2 * e + 1] += bfhi(wv[i][e]); }
      *(f32x4*)(lds + (4 * w + G) * 512 + i16 * 32) = f32x4{c[0], c[1], c[2], c[3]};
      *(f32x4*)(lds + (4 * w + G) * 512 + i16 * 32 + 16) = f32x4{c[4], c[5], c[6], c[7]};
      poll(ws, WS_F2 + (u32)(l & 31) * 128, true, tag);
      bar();
      if (tid < 128 && tb64 < 28) {
        float t = 0.f;
#pragma unroll
        for (int n = 0; n < 32; ++n) t += *(const float*)(lds + n * 512 + tid * 4);
        const u32 hi = pk(t, t), lo = pk(t - bflo(hi), 0.f);
        __builtin_amdgcn_raw_buffer_store_b64(u32x2{hi, lo}, rw, WS_CS + (u32)(((j * 8 + (tid >> 4)) * 32 + mat * 16 + (tid & 15)) * 8), 0, SC1);
      }
    }
    STAMP(2);
    const u32x4 dw = srd(ws, WS_END);
    const u32 lb = (u32)(size_t)(LAS char*)lds;
#pragma unroll 1
    for (int half = (tb64 & 1) ^ 1; half < 2; ++half) {
      const int tb = 32 * half + tb64;
#pragma unroll 1
      for (int it = 0; it < 4000; ++it) {
#pragma unroll
        for (int k = 0; k < 4; ++k) if (k < 3 || l < 32) dma<SC1>(dw, lb + 32768 + w * 3584 + k * 1024, l * 16, WS_H + (u32)(tb * TB + w * 3584 + k * 1024));
        VMW(0);
        u32 m = 0;
#pragma unroll
        for (int k = 0; k < 4; ++k) m = max(m, max4(*(const u32x4*)(lds + 32768 + w * 3584 + (k < 3 ? k * 1024 + l * 16 : 3072 + (l & 31) * 16))));
        if (!__any(m == EMPTY)) break;
        __builtin_amdgcn_s_sleep(6);
      }
      float s1 = 0.f, s2 = 0.f;
#pragma unroll
      for (int i = 0; i < 7; ++i) {
        const u32x2 t = trd(lds, 32768 + (u32)((56 * w + 4 * (G >> 1) + q4) * 64 + (G & 1) * 32 + p4 * 8) + i * 512);
        s1 = dot2(t.x, ONES, s1); s2 = dot2(t.x, t.x, s2); s1 = dot2(t.y, ONES, s1); s2 = dot2(t.y, t.y, s2);
      }
      *(float*)(lds + 20480 + half * 2048 + l * 32 + w * 4) = xsum(s1, s2);
      bar();
      if (w == 0) {
        const f32x4 a = *(const f32x4*)(lds + 20480 + half * 2048 + l * 32), c = *(const f32x4*)(lds + 20480 + half * 2048 + l * 32 + 16);
        __builtin_amdgcn_raw_buffer_store_b128(__builtin_bit_cast(u32x4, ln2_terms(((a.x + a.y) + (a.z + a.w)) + ((c.x + c.y) + (c.z + c.w)))), rw, l < 32 ? WS_S2 + (u32)(tb * 512 + l * 16) : OOB, 0, SC1);
      }
    }
    STAMP(3);
    STAMP_OUT();
    return;
  }
  const int cp = xc * 28 + sc, cch = w & 1, vp = w >> 1;
  u32x4 fhi; u32x2 flo; u32 Dh; u64 e0;
  const u32 lb = (u32)(size_t)(LAS char*)lds;
  const u32x4 dw = srd(ws, WS_END);
  {
    const u32x4 rk = srd(gk, S_ * D_ * 2), rv = srd(gv, S_ * D_ * 2);
    const int c8 = l & 7, s0 = 64 * tb64;
    const int swv = ((r8 >> 1) & 1) | ((w & 1) << 1), swk = swv ^ 2;
    const u32 g0 = (u32)((s0 + 8 * w + r8) * 896 + role * 128);
    if (w == 0) dma(rk, lb + L_K, g0 - 8 * 896 + ((c8 ^ (swv << 1)) << 4), 0);
    if (w == 1) dma(srd(gsk, D_ * 8), lb + L_SK, (u32)(512 * role + 16 * l), 0);
    dma(rk, lb + L_K + 1024 + w * 1024, g0 + ((c8 ^ (swk << 1)) << 4), 0);
    dma(rv, lb + L_V + w * 1024, g0 + ((c8 ^ (swv << 1)) << 4), 0);
    dma(srd(gq, S_ * D_ * 2), lb + L_X + w * 1024, g0 + ((c8 ^ (swv << 1)) << 4), 0);
    const rsrc_t rf = mk(gf, S_ * D_ * 2);
    const int fq = vp * 64 + l;
    const u32 fo = (u32)((2 * cp + cch) * 4096 + 16 * fq);
    fhi = __builtin_amdgcn_raw_buffer_load_b128(rf, fo, 0, 0);
    flo = __builtin_amdgcn_raw_buffer_load_b64(rf, fq > 0 ? fo - 8 : OOB, 0, 0);
    Dh = __builtin_amdgcn_raw_buffer_load_b16(mk(gD, D_ * 2), (u32)((2 * cp + ((l >> 2) & 1)) * 2), 0, 0);
    e0 = __hip_atomic_load((GAS u64*)(ws + WS_E), RLX_AGENT);
    SB();
  }
  const int gi = 4 * (xc >> 1) + sc / 7, gj = 7 * (xc & 1) + sc % 7;
  W1 w1;
  {
    const u32 v0 = (u32)(((4 * w + G) * F_ + 128 * gj) * 2);
    w1.rg = srd(gwg, D_ * F_ * 2); w1.ru = srd(gwu, D_ * F_ * 2); w1.lb = lb + w * 1024;
    w1.vg = v0 + ((i16 ^ (4 * G)) << 4); w1.vu = v0 + ((i16 ^ (4 * G + 2)) << 4);
    w1.ra = (u32)((G & 1) * 8192 + (8 * (G >> 1) + q4) * 256 + ((w ^ (2 * q4 + (G & 1))) * 32) + p4 * 8);
  }
  VMW(5); bar();
  STAMP(1);
  {
    const int tbl = w >> 2, chg = w & 3;
    const u32 rb = (u32)((32 * tbl + 8 * G + q4) * 128 + p4 * 8);
    const u32 sv = (u32)((chg ^ ((q4 >> 1) | ((G & 1) << 1))) * 32), sk2 = sv ^ 64;
    const u32x2 km = trd(lds, L_K + rb + 512 + sv), k0 = trd(lds, L_K + rb + 1024 + sk2), k1 = trd(lds, L_K + rb + 1536 + sk2);
    const u32x2 v0 = trd(lds, L_V + rb + sv), v1 = trd(lds, L_V + rb + 512 + sv);
    const u32x2 skv = *(const u32x2*)(lds + L_SK + (16 * chg + i16) * 8);
    const float kk[11] = {bfhi(km.x), bflo(km.y), bfhi(km.y), bflo(k0.x), bfhi(k0.x), bflo(k0.y), bfhi(k0.y), bflo(k1.x), bfhi(k1.x), bflo(k1.y), bfhi(k1.y)};
    const float vv[8] = {bflo(v0.x), bfhi(v0.x), bflo(v0.y), bfhi(v0.y), bflo(v1.x), bfhi(v1.x), bflo(v1.y), bfhi(v1.y)};
    const float s0 = bflo(skv.x), s1 = bfhi(skv.x), s2 = bflo(skv.y), s3 = bfhi(skv.y);
    float u[8];
#pragma unroll
    for (int e = 0; e < 8; ++e) u[e] = (kk[e + 3] * s0 + kk[e + 2] * s1 + kk[e + 1] * s2 + kk[e] * s3) * vv[e];
    const u32 o = (u32)((2 * tb64 + tbl) * TB + (64 * role + 16 * chg + i16) * 64 + G * 16);
    __builtin_amdgcn_raw_buffer_store_b128(u32x4{pk(u[0], u[1]), pk(u[2], u[3]), pk(u[4], u[5]), pk(u[6], u[7])}, rw, WS_U + o, 0, SC1);
    SB();
    STAMP(2);
    VMW(5); bar();
    const u32x2 x0 = trd(lds, L_X + rb + sv), x1 = trd(lds, L_X + rb + 512 + sv);
    __builtin_amdgcn_raw_buffer_store_b128(cat(x0, x1), rw, WS_X + o, 0, SC1);
    PIN(fhi); PIN(flo); PIN(Dh); PIN(e0);
    SB();
  }
  STAMP(3);
  {
    const u32 a0 = __builtin_amdgcn_alignbit(flo.y, flo.x, 16), a1 = __builtin_amdgcn_alignbit(fhi.x, flo.y, 16), a2 = __builtin_amdgcn_alignbit(fhi.y, fhi.x, 16),
              a3 = __builtin_amdgcn_alignbit(fhi.z, fhi.y, 16), a4 = __builtin_amdgcn_alignbit(fhi.w, fhi.z, 16);
    char* fc = lds + L_FC + cch * FCH + (vp * 64 + l) * 16 + 32;
    *(u32x4*)(fc) = fhi;
    *(u32x4*)(fc + FCS) = u32x4{a1, a2, a3, a4};
    *(u32x4*)(fc + 2 * FCS) = u32x4{flo.y, fhi.x, fhi.y, fhi.z};
    *(u32x4*)(fc + 3 * FCS) = u32x4{a0, a1, a2, a3};
    if (tid >= 128 && tid < 144) *(u32x4*)(lds + L_FC + (tid & 7) * FCS + ((tid >> 3) & 1) * 16) = u32x4{0, 0, 0, 0};
    if (tid >= 192 && tid < 252) { const int z = tid - 192; *(u32x4*)(lds + L_U + (z / 30) * UCH + (z % 30) * 16) = u32x4{0, 0, 0, 0}; }
  }
  const u64 tag = mktag(e0);
  const float Dv = bflo(Dh);
  SB();
  w1_issue<0>(w1, L_RING); w1_issue<1>(w1, L_RING + 16384);
  STAMP(4);
  VMW(4); bar();
  if (tid == 0) setflag(ws, WS_F1 + (tb64 * 8 + role) * 128, tag);
  STAMP(5);
  const int hh = l >> 5, tk = l & 31;
  const u32 gso = WS_G + (u32)(((2 * gi * 2 + (tk >> 4)) * 56 + 4 * gj + (w >> 1)) * 1024 + (2 * (w & 1) + hh) * 256 + i16 * 16);
  auto gofs = [](int g) AI { return (u32)((32 * (g >> 1) + (g & 1)) * 2 * 56 * 1024); };
  u32x4 W[28];
  w1_issue<2>(w1, L_RING + 32768); w1_issue<3>(w1, L_RING + 49152); w1_issue<4>(w1, L_RING + 65536); w1_issue<5>(w1, L_RED); w1_issue<6>(w1, L_RED + 16384);
  SB();

  const int ech = (l >> 2) & 1;
  const u32 eo = (u32)((8 * w + r8) * TB + (2 * cp + ech) * 64 + p4 * 16);
  STAMP(6);
  poll(ws, WS_F1 + (u32)(32 * w + 8 * (l & 3) + ((l & 4) ? 7 : (cp >> 5))) * 128, l < 8, tag);
  STAMP(7);
  u32x4 uu = ldg(dw, eo, WS_U), ex = ldg(dw, eo, WS_X);
  u32x4 est[4];
#pragma unroll
  for (int i = 0; i < 4; ++i) est[i] = ldg(dw, (u32)(r8 * 64 + p4 * 16), WS_ST + (u32)(w * 2048 + i * 512));
  u64 fl = flag_ld(dw, WS_F2 + (u32)(4 * w + (l & 3)) * 128);
  bar();
  const u32 ra0 = opq(w1.ra), ra1 = opq(w1.ra + L_RING), ra2 = opq(w1.ra + L_RING + 49152);
  w1_read<5>(lds, ra0 + L_RED, W); w1_read<6>(lds, ra0 + L_RED + 16384, W);
  w1_read<0>(lds, ra1, W); w1_read<1>(lds, ra1 + 16384, W); w1_read<2>(lds, ra1 + 32768, W); w1_read<3>(lds, ra2, W); w1_read<4>(lds, ra2 + 16384, W);
  const u32 aA = opq(L_FC + cch * FCH + q4 * FCS + 64 - 16 * G + 8 * p4 - 8 + 128 * vp);
  const u32 aB = opq(L_U + cch * UCH + 32 * i16 + 16 * G + 448 - 128 * vp - 64);
  const u32 rwr = opq(L_RED + w * 4096 + G * 256 + ((i16 ^ ((G >> 1) << 3)) << 4));
  const u32 rr = opq(L_RED + ech * 4096 + (w & 3) * 1024 + (p4 & 1) * 512 + (((2 * r8 + (p4 >> 1)) ^ ((p4 & 1) << 3)) << 4));
  SB();
  vmw<6>(uu);
  *(u32x4*)(lds + L_U + ech * UCH + 480 + (8 * w + r8) * 64 + p4 * 16) = uu;
  bar();
  STAMP(8);
  auto rdA = [&](int v, int m) AI { const u32 o = 64 * v + 512 * m; return cat(trd(lds, aA + o + 8), trd(lds, aA + o)); };
  auto rdB = [&](int v, int m) AI { return *(const u32x4*)(lds + aB + 64 * (1 - v) + 512 * m); };
  float ex8[8], eq8[8], eu8[8];
  auto terms = [&]() AI {
#pragma unroll
    for (int i = 0; i < 4; ++i) {
      ex8[2 * i] = bflo(ex[i]); ex8[2 * i + 1] = bfhi(ex[i]);
      eu8[2 * i] = bflo(uu[i]) * Dv; eu8[2 * i + 1] = op(bfhi(uu[i]) * Dv);
      eq8[2 * i] = (ex8[2 * i] - __uint_as_float(est[i].x)) * __uint_as_float(est[i].y);
      eq8[2 * i + 1] = op(op(ex8[2 * i + 1] - __uint_as_float(est[i].z)) * __uint_as_float(est[i].w));
    }
#pragma unroll
    for (int i = 0; i < 8; ++i) { PIN(ex8[i]); PIN(eq8[i]); PIN(eu8[i]); }
  };
  auto epilogue = [&]() AI {
    f32x4 p[8];
#pragma unroll
    for (int n = 0; n < 4; ++n) { p[2 * n] = *(const f32x4*)(lds + rr + n * 8192); p[2 * n + 1] = *(const f32x4*)(lds + rr + n * 8192 + 256); }
    float h[8];
#pragma unroll
    for (int i = 0; i < 8; ++i) {
      const float y = op(op(eu8[i] + p[i >> 2][i & 3]) + p[2 + (i >> 2)][i & 3]) + op(p[4 + (i >> 2)][i & 3] + p[6 + (i >> 2)][i & 3]);
      h[i] = op(ex8[i] + eq8[i] * op(y));
    }
    __builtin_amdgcn_raw_buffer_store_b128(u32x4{pk(h[0], h[1]), pk(h[2], h[3]), pk(h[4], h[5]), pk(h[6], h[7])}, rw, WS_H + eo, 0, SC1);
  };
  {
    f32x4 acc[4];
    u32x4 A[2][4], B[2][4];
#pragma unroll
    for (int v = 0; v < 2; ++v) {
      A[v][0] = rdA(v, 0); SB();
#pragma unroll
      for (int m = 0; m < 4; ++m) { B[v][m] = rdB(v, m); SB(); }
#pragma unroll
      for (int m = 1; m < 4; ++m) { A[v][m] = rdA(v, m); SB(); }
      if (v == 0) {
        vmw<0>(ex); PIN(est[0]); PIN(est[1]); PIN(est[2]); PIN(est[3]); PIN(fl);
        if (!__all(fl == tag)) poll<2>(ws, WS_F2 + (u32)(4 * w + (l & 3)) * 128, true, tag);
        if (w < 4) terms();
        SB();
      }
    }
#pragma unroll
    for (int v = 0; v < 2; ++v)
#pragma unroll
      for (int m = 0; m < 4; ++m) {
#pragma unroll
        for (int c = m; c < 4; ++c) acc[c] = mfma16(A[v][m], B[v][c - m], (v == 0 && m == 0) ? f32x4{} : acc[c]);
        SB();
        if (v == 0 && m == 0) w1_g<7>(w1, L_RING);
        if (v == 0 && m == 2) w1_u<7>(w1, L_RING);
        if (v == 1 && m == 0) w1_g<8>(w1, L_RING + 16384);
        SB();
      }
#pragma unroll
    for (int c = 0; c < 4; ++c) *(f32x4*)(lds + rwr + c * 1024) = acc[c];
  }
  bar();
  STAMP(9);
  if (w < 4) epilogue();
  SB();
  STAMP(10);
  {
    f32x4 acc[4];
    u32x4 A[2][8], B[2][8];
    auto rd = [&](int v, int m) AI {
      if (m == 0) {
        A[v][0] = rdA(v, 0);
#pragma unroll
        for (int d = 4; d < 8; ++d) B[v][d] = rdB(v, d);
      } else { A[v][m] = rdA(v, m); if (m <= 4) B[v][4 - m] = rdB(v, 4 - m); }
      SB();
    };
    rd(0, 0); rd(0, 1); rd(0, 2); rd(0, 3);
#pragma unroll
    for (int v = 0; v < 2; ++v) {
#pragma unroll
      for (int m = 0; m < 8; ++m) {
#pragma unroll
        for (int c = 4; c < 8; ++c) if (m <= c) acc[c - 4] = mfma16(A[v][m], B[v][c - m], (v == 0 && m == 0) ? f32x4{} : acc[c - 4]);
        SB();
        if (m < 4) rd(v, m + 4); else if (v == 0) rd(1, m - 4);
        if (v == 0) {
          if (m == 0) w1_u<8>(w1, L_RING + 16384);
          if (m == 1) w1_g<9>(w1, L_RING + 32768);
          if (m == 2) w1_u<9>(w1, L_RING + 32768);
          if (m == 3) w1_g<10>(w1, L_RING + 49152);
          if (m == 4) w1_u<10>(w1, L_RING + 49152);
          if (m == 6) w1_g<11>(w1, L_RING + 65536);
          if (m == 7) w1_u<11>(w1, L_RING + 65536);
        }
        SB();
      }
    }
    if (w >= 4) terms();
    STAMP(11);
    VMW(0); bar();
#pragma unroll
    for (int c = 0; c < 4; ++c) *(f32x4*)(lds + rwr + c * 1024) = acc[c];
  }
  const u32 lo16 = l * 16;
  if (w < 4) {
#pragma unroll
    for (int k = 0; k < 7; ++k) dma<SC1>(dw, lb + L_H0 + (7 * w + k) * 1024, lo16, WS_H + (u32)(2 * gi * TB + (7 * w + k) * 1024));
  }
  w1_read<7>(lds, ra1, W); w1_read<8>(lds, ra1 + 16384, W); w1_read<9>(lds, ra1 + 32768, W); w1_read<10>(lds, ra2, W); w1_read<11>(lds, ra2 + 16384, W);
  bar();
  STAMP(12);
  if (w >= 4) {
    epilogue();
    SB();
#pragma unroll
    for (int i = 0; i < 2; ++i) {
      W1 c = w1; c.lb -= i * 4096; c.vg -= i * (16 * F_ * 2); c.vu -= i * (16 * F_ * 2);
      w1_issue<12>(c, L_WE); w1_issue<13>(c, L_WE + 16384);
    }
  } else {
    const u32 ha = opq(L_H0 + 7 * w * 1024 + lo16);
#pragma unroll 1
    for (int it = 0; it < 4000; ++it) {
      VMW(0);
      u32 m = 0;
#pragma unroll
      for (int k = 0; k < 7; ++k) { const u32x4 v = *(const u32x4*)(lds + ha + k * 1024); m = max(m, max4(v)); }
      if (!__any(m == EMPTY)) break;
#pragma unroll
      for (int k = 0; k < 7; ++k) dma<SC1>(dw, lb + L_H0 + (7 * w + k) * 1024, lo16, WS_H + (u32)(2 * gi * TB + (7 * w + k) * 1024));
    }
    VMW(0);
    STAMP(13);
  }
  bar();
  STAMP(14);

  u64 csv;
  const u32 bta = (u32)((8 * (G >> 1) + q4) * 64 + (G & 1) * 32 + p4 * 8);
  const u32 sta = (u32)((56 * w + 4 * (G >> 1) + q4) * 64 + (G & 1) * 32 + p4 * 8);
  auto hbase = [](int g) AI { return g == 0 ? L_H0 : g == 1 ? L_H1 : g == 2 ? L_H2 : L_H3; };
  const u32 spa = opq(L_SP + l * 32);
  float s1a, s1b, s2a, s2b, xs; u32x2 st[2];
  auto stats_step = [&](u32 b0, int k) AI {
    if (k > 0) {
      if (k & 1) { s1a = k == 1 ? xs : s1a + xs; s2a = k == 1 ? xs * xs : s2a + xs * xs; PIN(s1a); PIN(s2a); }
      else       { s1b = k == 2 ? xs : s1b + xs; s2b = k == 2 ? xs * xs : s2b + xs * xs; PIN(s1b); PIN(s2b); }
    }
    if (k < 28) {
      const int u = k >> 2;
      const u32 d = (k & 2) ? st[u & 1].y : st[u & 1].x;
      xs = (k & 1) ? bfhi(d) : bflo(d); PIN(xs);
      if ((k & 3) == 3 && u + 2 < 7) st[u & 1] = trd(lds, b0 + (u + 2) * 512);
    } else *(float*)(lds + spa + w * 4) = xsum(s1a + s1b, s2a + s2b);
  };
  f32x4 sp0, sp1, tm; float gu[8], gt[8]; u32 od[4];
  auto center = [&](f32x16& acc, float m) AI {
    const u32 d = __float_as_uint(m);
    acc = mfma32(u32x4{(u32)csv, (u32)(csv >> 32), 0, 0}, u32x4{d, d & 0xffffu, 0, 0}, acc);
  };
  auto geglu_step = [&](int g, f32x16& acc, int i) AI {
    if (g == 0) {
      if (i == 1) { sp0 = *(const f32x4*)(lds + spa); sp1 = *(const f32x4*)(lds + spa + 16); }
      if (i == 3) { sp0.x = op(sp0.x + sp0.y); sp0.z = op(sp0.z + sp0.w); sp1.x = op(sp1.x + sp1.y); sp1.z = op(sp1.z + sp1.w); PIN(sp0); PIN(sp1); }
      if (i == 4) { tm = ln2_terms((sp0.x + sp0.z) + (sp1.x + sp1.z)); PIN(tm); }
      if (i == 5) center(acc, tm.x);
    }
#pragma unroll
    for (int e = 0; e < 8; ++e) {
      const int s = i - (g == 0 ? 7 + (13 * e) / 7 : 2 + (18 * e) / 7);
      if (s == 0) { gt[e] = acc[e] * acc[e]; gu[e] = op(acc[e] * acc[e + 8]); PIN(gt[e]); PIN(gu[e]); }
      if (s == 1) { gt[e] = tm.w * gt[e] + tm.z; PIN(gt[e]); }
      if (s == 2) { gt[e] = acc[e] * gt[e]; PIN(gt[e]); }
      if (s == 3) { gt[e] = __builtin_amdgcn_exp2f(gt[e]); PIN(gt[e]); }
      if (s == 4) { gt[e] = gt[e] * tm.y + tm.y; PIN(gt[e]); }
      if (s == 5) { gt[e] = __builtin_amdgcn_rcpf(gt[e]); PIN(gt[e]); }
      if (s == 6) { gu[e] = gu[e] * gt[e]; PIN(gu[e]); }
      if (s == 7 && (e & 1)) { od[e >> 1] = pk(gu[e - 1], gu[e]); PIN(od[e >> 1]); }
    }
    if (i == 28) {
      __builtin_amdgcn_raw_buffer_store_b128(u32x4{od[0], od[1], od[2], od[3]}, rw, gso, gofs(g), SC1);
    }
  };
  constexpr int PF = 4;
  u32x4 B[PF];
  u32 ba = opq(hbase(0) + bta);
  auto chain = [&](int g, f32x16& acc, auto side) AI {
    const u32 bn = opq(hbase(g < 3 ? g + 1 : g) + bta);
    sfor<28>([&](auto kc) AI {
      constexpr int k = decltype(kc)::value;
      acc = mfma32(W[k], B[k % PF], k == 0 ? f32x16{} : acc);
      SB();
      side(k);
      if (k + PF < 28) B[k % PF] = cat(trd(lds, ba + (k + PF) * 1024), trd(lds, ba + (k + PF) * 1024 + 256));
      else if (g < 3) B[k % PF] = cat(trd(lds, bn + (k + PF - 28) * 1024), trd(lds, bn + (k + PF - 28) * 1024 + 256));
      SB();
    });
    ba = bn;
  };
  auto flag3 = [&](int g) AI { setflag(ws, WS_F3 + (u32)(((gi * 4 + g) * 14) + gj) * 128, tag); };
  const int ri = 2 * xc + sc / 14, rq2 = (sc / 7) & 1, n2 = sc % 7;
  const u32x4 rd = srd(gwd, D_ * F_ * 2);
  const u32 wdv = (u32)(((224 * w + r8) * D_ + 64 * n2) * 2 + (((l & 7) ^ (((r8 >> 1) & 3) << 1)) << 4));
  const u32 blkR = w < 7 ? L_R + w * 4096 : L_R2;
  u32 wda[4];
#pragma unroll
  for (int nt = 0; nt < 4; ++nt) wda[nt] = (16 * (G >> 1) + 4 * (G & 1) + q4) * 128 + ((nt ^ (2 * (G & 1) + (q4 >> 1))) * 32) + p4 * 8;
  u32x4 Wd[7][4];
  auto wd_blk = [&](int k) AI { return k % 3 == 1 ? blkR : (k % 3 == 2 ? L_Q : L_P) + w * 4096; };
  auto wd_dma = [&](int k, int i) AI { dma(rd, lb + wd_blk(k) + i * 1024, wdv, (4 * k + i) * (8 * D_ * 2)); };
  auto wd_read = [&](int k) AI {
#pragma unroll
    for (int nt = 0; nt < 4; ++nt) { const u32 a = opq(wda[nt] + wd_blk(k)); Wd[k][nt] = cat(trd(lds, a), trd(lds, a + 1024)); }
  };
  const int j0 = (14 * w) >> 3, jn = ((14 * w + 13) >> 3) - j0;
  const u32 f3o = WS_F3 + (u32)(ri * 56 + j0 + ((l & 3) < jn ? (l & 3) : jn)) * 128;

  __builtin_amdgcn_s_setprio(1);
  f32x16 accA, accB;
  u32x4 tm1, tm2, tm3;
  const u32 s2o = WS_S2 + (u32)(2 * gi * 512 + tk * 16);
  auto h_dma = [&](int g, int k) AI {
    if (k < 3 || l < 32) dma<SC1>(dw, lb + hbase(g) + w * 3584 + k * 1024, lo16, WS_H + (u32)((32 * (g >> 1) + 2 * gi + (g & 1)) * TB + w * 3584 + k * 1024));
  };
  const u32 cso = l < 32 ? WS_CS + (u32)(((gj * 8 + w) * 32 + l) * 8) : OOB;
  u32x4 cv[4];
  auto h_rd = [&](int g) AI {
    const u32 a = opq(hbase(g) + w * 3584 + lo16), a3 = opq(hbase(g) + w * 3584 + 3072 + tk * 16);
    cv[0] = *(const u32x4*)(lds + a); cv[1] = *(const u32x4*)(lds + a + 1024); cv[2] = *(const u32x4*)(lds + a + 2048); cv[3] = *(const u32x4*)(lds + a3);
  };
  auto h_fix = [&](int g) AI {
    u32 cm = g == 1 ? max((u32)csv, (u32)(csv >> 32)) : g == 2 ? max4(tm1) : max(max4(tm2), max4(tm3));
    cm = max(max(cm, max(max4(cv[0]), max4(cv[1]))), max(max4(cv[2]), max4(cv[3])));
#pragma unroll 1
    for (int it = 0; it < 4000 && __any(cm == EMPTY); ++it) {
      h_dma(g, 0); h_dma(g, 1); h_dma(g, 2); h_dma(g, 3);
      if (g == 1) { csv = flag_ld(dw, cso); VMWP(0, csv); }
      if (g == 2) { tm1 = ldg(dw, s2o, 512); vmw<0>(tm1); }
      if (g == 3) { tm2 = ldg(dw, s2o, 32 * 512); tm3 = ldg(dw, s2o, 33 * 512); vmw<0>(tm2); PIN(tm3); }
      h_rd(g);
      cm = g == 1 ? max((u32)csv, (u32)(csv >> 32)) : g == 2 ? max4(tm1) : max(max4(tm2), max4(tm3));
      cm = max(max(cm, max(max4(cv[0]), max4(cv[1]))), max(max4(cv[2]), max4(cv[3])));
    }
  };
  SB();
  {
    const u32 b0 = opq(hbase(0) + sta);
#pragma unroll
    for (int k = 0; k < PF; ++k) B[k] = cat(trd(lds, ba + k * 1024), trd(lds, ba + k * 1024 + 256));
    st[0] = trd(lds, b0); st[1] = trd(lds, b0 + 512);
    SB();
    chain(0, accA, [&](int k) AI {
      stats_step(b0, k);
      if (k <= 6 && !(k & 1)) h_dma(1, k >> 1);
      if (k == 7) csv = flag_ld(dw, cso);
      if (k == 10 || k == 13) __builtin_amdgcn_raw_buffer_store_b128(u32x4{EMPTY, EMPTY, EMPTY, EMPTY}, rw, gso, gofs(k == 10 ? 2 : 3), SC1);
      if (k == 14) { VMW(7); RBAR(); const u32 ra3 = opq(w1.ra + L_WE); w1_read<12>(lds, ra3, W); w1_read<13>(lds, ra3 + 16384, W); }
      if (k == 19) { VMWP(2, csv); h_rd(1); }
      if (k == 22) { h_fix(1); RBAR(); }
      if (k >= 23 && (k & 1)) h_dma(2, (k - 23) >> 1);
    });
    stats_step(b0, 28);
  }
  STAMP(16);
  chain(1, accB, [&](int k) AI {
    if (k == 1) { bar(); if (tid == 0 && b == 0) setflag(ws, WS_E, tag); h_dma(2, 3); }
    geglu_step(0, accA, k);
    if (k == 3) tm1 = ldg(dw, s2o, 512);
    if (k >= 5 && k <= 14 && k % 3 == 2) wd_dma(0, (k - 5) / 3);
    if (k >= 16 && k <= 25 && k % 3 == 1) h_dma(3, (k - 16) / 3);
    if (k == 20) { vmw<6>(tm1); h_rd(2); }
    if (k == 23) { h_fix(2); RBAR(); }
  });
  center(accB, __uint_as_float(tm1.x));
  geglu_step(0, accA, 28);
  tm = __builtin_bit_cast(f32x4, tm1);
  SB();
  STAMP(18);
  chain(2, accA, [&](int k) AI {
    if (k == 1) RBAR();
    geglu_step(1, accB, k);
    if (k <= 6 && !(k & 1)) wd_dma(1, k >> 1);
    if (k == 3) { VMW(7); wd_read(0); }
    if (k == 8) { tm2 = ldg(dw, s2o, 32 * 512); tm3 = ldg(dw, s2o, 33 * 512); }
    if (k >= 10 && k <= 24 && !(k & 1)) wd_dma(2 + ((k - 10) >> 3), ((k - 10) >> 1) & 3);
    if (k == 19) { VMW(11); h_rd(3); }
    if (k == 23) { vmw<7>(tm2); PIN(tm3); h_fix(3); RBAR(); if (tid == 0) flag3(0); }
  });
  center(accA, __uint_as_float(tm2.x));
  geglu_step(1, accB, 28);
  tm = __builtin_bit_cast(f32x4, tm2);
  SB();
  STAMP(20);
  chain(3, accB, [&](int k) AI {
    geglu_step(2, accA, k);
    if (k == 12) fl = flag_ld(dw, f3o);
    if (k == 15) { VMW(1); RBAR(); if (tid == 0) flag3(1); }
    if (k >= 15 && k < 18) wd_read(k - 14);
    if (k == 19) LG0();
    if (k >= 19 && k < 27 && (k & 1)) wd_dma(4, (k - 19) >> 1);
  });
  center(accB, __uint_as_float(tm3.x));
  geglu_step(2, accA, 28);
  tm = __builtin_bit_cast(f32x4, tm3);
  STAMP(21);
#pragma unroll
  for (int e = 0; e < 8; ++e) {
    const float a = accB[e], u = accB[e + 8];
    gu[e] = (a * u) * __builtin_amdgcn_rcpf(__builtin_amdgcn_exp2f(a * (tm.w * (a * a) + tm.z)) * tm.y + tm.y);
  }
#pragma unroll
  for (int e = 0; e < 4; ++e) od[e] = pk(gu[2 * e], gu[2 * e + 1]);
  geglu_step(3, accB, 28);
  __builtin_amdgcn_s_setprio(0);
  SB();
  STAMP(22);
  const u32 gb = WS_G + (u32)((2 * ri * 2 + rq2) * 56 + 7 * w) * 1024;
  u32x4 Hf[3][7];
  auto h_load = [&](int g, int k0, int k1) AI {
#pragma unroll
    for (int k = k0; k < k1; ++k) Hf[g % 3][k] = ldg(dw, lo16, gb + gofs(g) + k * 1024);
  };
  const rsrc_t ro = mk(gout, S_ * D_ * 2);
  const u32 pw = (u32)(i16 * 256), tf = 4 * (w & 3) + G;
  f32x4 acc[4];
  u32x2 res[4];
  auto step = [&](auto gc, auto kc, auto nc) AI {
    constexpr int g = decltype(gc)::value, k = decltype(kc)::value;
    vmw<decltype(nc)::value>(Hf[g % 3][k]);
#pragma unroll
    for (int nt = 0; nt < 4; ++nt) acc[nt] = mfma16(Wd[k][nt], Hf[g % 3][k], k == 0 ? f32x4{} : acc[nt]);
    SB();
  };
  auto put = [&](int g) AI {
    const u32 rb = opq(((g & 1) ? L_Q : L_P) + w * 4096 + pw);
#pragma unroll
    for (int nt = 0; nt < 4; ++nt) *(f32x4*)(lds + rb + (((4 * nt + G) ^ (i16 & 7)) << 4)) = acc[nt];
    *(f32x4*)(lds + L_RES + (g & 1) * 4096 + pw + (((2 * w + (G & 1)) ^ (i16 & 7)) << 4)) = f32x4{bflo(res[g].x), bfhi(res[g].x), bflo(res[g].y), bfhi(res[g].y)};
  };
  auto fin = [&](int g) AI {
    if ((w >> 2) == (g & 1)) {
      const u32 a0 = tf * 256 + ((i16 ^ (tf & 7)) << 4), a = opq(((g & 1) ? L_Q : L_P) + a0);
      f32x4 sum = *(const f32x4*)(lds + L_RES + (g & 1) * 4096 + a0);
#pragma unroll
      for (int n = 0; n < 8; ++n) sum += *(const f32x4*)(lds + a + n * 4096);
      const int tok = 32 * (32 * (g >> 1) + (g & 1) + 2 * ri) + 16 * rq2 + tf;
      __builtin_amdgcn_raw_buffer_store_b64(u32x2{pk(sum.x, sum.y), pk(sum.z, sum.w)}, ro, (u32)((tok * D_ + 64 * n2 + 4 * i16) * 2), 0, 0);
    }
  };
  u32 chk;
  auto stepc = [&](auto gc, auto kc, auto nc) AI {
    constexpr int g = decltype(gc)::value, k = decltype(kc)::value;
    step(gc, kc, nc);
    const u32x4 v = Hf[g % 3][k];
    chk = k == 0 ? max4(v) : max(chk, max4(v));
    PIN(chk);
  };
  auto redo = [&](auto gc) AI {
    constexpr int g = decltype(gc)::value;
#pragma unroll 1
    for (int it = 0; it < 4000 && __any(chk == EMPTY); ++it) {
      h_load(g, 0, 7);
      stepc(gc, IC<0>{}, IC<6>{}); stepc(gc, IC<1>{}, IC<5>{}); stepc(gc, IC<2>{}, IC<4>{}); stepc(gc, IC<3>{}, IC<3>{});
      stepc(gc, IC<4>{}, IC<2>{}); stepc(gc, IC<5>{}, IC<1>{}); stepc(gc, IC<6>{}, IC<0>{});
    }
  };
  VMWP(6, fl);
  if (!__all(fl == tag)) poll(ws, f3o, true, tag);
  wd_dma(5, 0); wd_dma(5, 1); wd_dma(5, 2); wd_dma(5, 3);
  h_load(0, 0, 3);
  fl = flag_ld(dw, f3o + 14 * 128);
  h_load(0, 3, 7);
  const u32 hro = WS_H + (u32)((32 * (G >> 1) + (G & 1) + 2 * ri) * TB + (64 * n2 + 8 * w + (i16 >> 1)) * 64 + rq2 * 32 + (i16 & 1) * 16);
  u32x4 hr = ldg(dw, hro, 0);
  wd_dma(6, 0); wd_dma(6, 1); wd_dma(6, 2); wd_dma(6, 3);
  VMWP(9, fl);
  if (!__all(fl == tag)) poll(ws, f3o + 14 * 128, true, tag);
  wd_read(4); wd_read(5);
  SB();
  STAMP(23);
  h_load(1, 0, 1); step(IC<0>{}, IC<0>{}, IC<10>{});
  h_load(1, 1, 2); step(IC<0>{}, IC<1>{}, IC<11>{});
  h_load(1, 2, 3); step(IC<0>{}, IC<2>{}, IC<12>{});
  h_load(1, 3, 4); step(IC<0>{}, IC<3>{}, IC<12>{});
  h_load(1, 4, 5); step(IC<0>{}, IC<4>{}, IC<12>{});
  h_load(1, 5, 6); step(IC<0>{}, IC<5>{}, IC<12>{});
  h_load(1, 6, 7);
  vmw<7>(hr);
#pragma unroll 1
  for (int it = 0; it < 4000 && __any(max4(hr) == EMPTY); ++it) { hr = ldg(dw, hro, 0); vmw<0>(hr); }
  wd_read(6);
  *(u32x4*)(lds + blkR + l * 16) = hr;
#pragma unroll
  for (int g = 0; g < 4; ++g) res[g] = trd(lds, blkR + g * 256 + (4 * (G & 1) + q4) * 32 + p4 * 8);
  h_load(2, 0, 2);
  step(IC<0>{}, IC<6>{}, IC<9>{});
  put(0);
  STAMP(24);
  h_load(2, 2, 3); step(IC<1>{}, IC<0>{}, IC<9>{});
  h_load(2, 3, 5); step(IC<1>{}, IC<1>{}, IC<10>{});
  h_load(2, 5, 7); step(IC<1>{}, IC<2>{}, IC<11>{});
  h_load(3, 0, 2); step(IC<1>{}, IC<3>{}, IC<12>{});
  h_load(3, 2, 4); step(IC<1>{}, IC<4>{}, IC<13>{});
  h_load(3, 4, 6); step(IC<1>{}, IC<5>{}, IC<14>{});
  h_load(3, 6, 7); step(IC<1>{}, IC<6>{}, IC<14>{});
  put(1);
  STAMP(25);
  bar();
  fin(0); fin(1);
  SB();
  stepc(IC<2>{}, IC<0>{}, IC<13>{}); stepc(IC<2>{}, IC<1>{}, IC<12>{}); stepc(IC<2>{}, IC<2>{}, IC<11>{}); stepc(IC<2>{}, IC<3>{}, IC<10>{});
  stepc(IC<2>{}, IC<4>{}, IC<9>{}); stepc(IC<2>{}, IC<5>{}, IC<8>{}); stepc(IC<2>{}, IC<6>{}, IC<7>{});
  redo(IC<2>{});
  bar();
  put(2);
  SB();
  STAMP(26);
  stepc(IC<3>{}, IC<0>{}, IC<6>{}); stepc(IC<3>{}, IC<1>{}, IC<5>{}); stepc(IC<3>{}, IC<2>{}, IC<4>{}); stepc(IC<3>{}, IC<3>{}, IC<3>{});
  stepc(IC<3>{}, IC<4>{}, IC<2>{}); stepc(IC<3>{}, IC<5>{}, IC<1>{}); stepc(IC<3>{}, IC<6>{}, IC<0>{});
  redo(IC<3>{});
  put(3);
  bar();
  STAMP(27);
  fin(2); fin(3);
  STAMP(28);
  STAMP_OUT();
}

}

extern "C" void kernel_launch(void* const* d_in, const int* in_sizes, int n_in, void* d_out, int out_size, void* d_ws, size_t ws_size,
                              hipStream_t stream) {
  h3_ffn<<<256, 512, 0, stream>>>((const char*)d_in[0], (const char*)d_in[1], (const char*)d_in[2], (const char*)d_in[3], (const char*)d_in[4],
                                  (const char*)d_in[5], (const char*)d_in[7], (const char*)d_in[8], (const char*)d_in[9], (char*)d_out, (char*)d_ws);
}
```

```cpp
#include <hip/hip_runtime.h>
#include <stdint.h>

namespace {

#define LAS __attribute__((address_space(3)))
#define GAS __attribute__((address_space(1)))
#define DI __device__ __forceinline__
#define AI __attribute__((always_inline))
typedef uint32_t u32;
typedef unsigned long long u64;
typedef u32 u32x2 __attribute__((ext_vector_type(2)));
typedef u32 u32x4 __attribute__((ext_vector_type(4)));
typedef short s16x4 __attribute__((ext_vector_type(4)));
typedef short s16x8 __attribute__((ext_vector_type(8)));
typedef float f32x4 __attribute__((ext_vector_type(4)));
typedef float f32x16 __attribute__((ext_vector_type(16)));
typedef __bf16 bf2 __attribute__((ext_vector_type(2)));
typedef __amdgpu_buffer_rsrc_t rsrc_t;
#define RLX_AGENT __ATOMIC_RELAXED, __HIP_MEMORY_SCOPE_AGENT

constexpr int S_ = 2048, D_ = 448, F_ = 1792;
constexpr u32 TB = 448 * 64;
constexpr u32 WS_E  = 0;
constexpr u32 WS_F1 = 128;
constexpr u32 WS_F3 = WS_F1 + 256 * 128;
constexpr u32 WS_F2 = WS_F3 + 896 * 128;
constexpr u32 WS_S2 = 256 * 1024 + 16384;
constexpr u32 WS_CS = 224 * 1024;
constexpr u32 WS_ST = 256 * 1024;
constexpr u32 WS_U  = WS_S2 + 32768;
constexpr u32 WS_X  = WS_U + 64 * TB;
constexpr u32 WS_H  = WS_X + 64 * TB;
constexpr u32 WS_G  = WS_H + 64 * TB;
constexpr u32 WS_END = WS_G + 64 * 112 * 1024;
constexpr u32 WS_DBG = 16u << 20;
constexpr int SC1 = 16;
constexpr u32 OOB = 0xFFFFFF00u;
constexpr u32 ONES = 0x3f803f80u;
constexpr u32 EMPTY = 0xffffffffu;

constexpr u32 L_K = 0, L_V = 9216, L_X = 17408, L_SK = 25600;
constexpr u32 L_RED = 0, L_FC = 32768, FCS = 4160, FCH = 4 * FCS;
constexpr u32 L_U = L_FC + 2 * FCH, UCH = 4608, L_RING = L_U + 2 * UCH;
constexpr u32 L_H0 = 32768, L_H1 = L_H0 + TB, L_WE = L_H1 + TB, L_H2 = 0, L_H3 = L_WE + 32768, L_SP = 155648;
constexpr u32 L_P = TB, L_Q = L_P + 32768, L_R = L_Q + 32768, L_R2 = L_H3 + TB;
constexpr u32 L_RES = 0;
constexpr u32 LDS_BYTES = 163840;

DI float bflo(u32 x) { return __uint_as_float(x << 16); }
DI float bfhi(u32 x) { return __uint_as_float(x & 0xffff0000u); }
DI u32 pk(float lo, float hi) { bf2 p; p.x = (__bf16)lo; p.y = (__bf16)hi; return __builtin_bit_cast(u32, p); }
DI rsrc_t mk(const void* p, u32 bytes) { return __builtin_amdgcn_make_buffer_rsrc((void*)p, 0, bytes, 0x00020000); }
DI u32x2 trd(const char* lds, u32 a) { return __builtin_bit_cast(u32x2, __builtin_amdgcn_ds_read_tr16_b64_v4i16((LAS s16x4*)(lds + a))); }
DI u32x4 cat(u32x2 a, u32x2 b) { return u32x4{a.x, a.y, b.x, b.y}; }
DI u32x4 srd(const void* p, u32 bytes) { const u64 a = (u64)p; return u32x4{(u32)a, (u32)(a >> 32) & 0xffffu, bytes, 0x00020000u}; }
template <int AUX = 0> DI void dma(u32x4 r, u32 ldsoff, u32 voff, u32 soff) {
  if (AUX) asm volatile("s_mov_b32 m0, %0\n\ts_nop 0\n\tbuffer_load_dwordx4 %1, %2, %3 offen sc1 lds" :: "s"(ldsoff), "v"(voff), "s"(r), "s"(soff) : "memory");
  else asm volatile("s_mov_b32 m0, %0\n\ts_nop 0\n\tbuffer_load_dwordx4 %1, %2, %3 offen lds" :: "s"(ldsoff), "v"(voff), "s"(r), "s"(soff) : "memory");
}
DI float fnma(float a, float b, float c) { float r; asm("v_fma_f32 %0, -%1, %2, %3" : "=v"(r) : "v"(a), "v"(b), "v"(c)); return r; }
DI float op(float x) { asm("" : "+v"(x)); return x; }
DI u32 opq(u32 x) { asm volatile("" : "+v"(x)); return x; }
#define PIN(x) asm volatile("" : "+v"(x))
DI f32x4 mfma16(u32x4 a, u32x4 b, f32x4 c) { return __builtin_amdgcn_mfma_f32_16x16x32_bf16(__builtin_bit_cast(s16x8, a), __builtin_bit_cast(s16x8, b), c, 0, 0, 0); }
DI f32x16 mfma32(u32x4 a, u32x4 b, f32x16 c) { return __builtin_amdgcn_mfma_f32_32x32x16_bf16(__builtin_bit_cast(s16x8, a), __builtin_bit_cast(s16x8, b), c, 0, 0, 0); }
DI float dot2(u32 a, u32 b, float c) { return __builtin_amdgcn_fdot2_f32_bf16(__builtin_bit_cast(bf2, a), __builtin_bit_cast(bf2, b), c, false); }
#define VMW(n) asm volatile("s_waitcnt vmcnt(" #n ")" ::: "memory")
#define LG0() asm volatile("s_waitcnt lgkmcnt(0)" ::: "memory")
#define SB() __builtin_amdgcn_sched_barrier(0)
DI void bar() { asm volatile("s_waitcnt lgkmcnt(0)\n\ts_barrier" ::: "memory"); }
#define RBAR() asm volatile("s_barrier" ::: "memory")
DI float wsum(float v) {
  v += __builtin_amdgcn_update_dpp(0.f, v, 0x111, 0xf, 0xf, true);
  v += __builtin_amdgcn_update_dpp(0.f, v, 0x112, 0xf, 0xf, true);
  v += __builtin_amdgcn_update_dpp(0.f, v, 0x114, 0xf, 0xf, true);
  v += __builtin_amdgcn_update_dpp(0.f, v, 0x118, 0xf, 0xf, true);
  v += __builtin_amdgcn_update_dpp(0.f, v, 0x142, 0xa, 0xf, true);
  v += __builtin_amdgcn_update_dpp(0.f, v, 0x143, 0xc, 0xf, true);
  return __uint_as_float(__builtin_amdgcn_readlane(__float_as_uint(v), 63));
}
DI float hsum(float v) {
  const auto r = __builtin_amdgcn_permlane32_swap(__float_as_uint(v), __float_as_uint(v), false, false);
  return __uint_as_float(r[0]) + __uint_as_float(r[1]);
}
template <int SLEEP = 1> DI void poll(char* ws, u32 off, bool act, u64 tag) {
  bool ok = !act;
#pragma unroll 1
  for (int it = 0; it < 20000; ++it) {
    u64 x = tag;
    if (!ok) x = __hip_atomic_load((GAS u64*)(ws + off), RLX_AGENT);
    ok = (x == tag);
    if (__all(ok)) break;
    __builtin_amdgcn_s_sleep(SLEEP);
  }
  asm volatile("" ::: "memory");
}
DI u32 max4(u32x4 v) { return max(max(v.x, v.y), max(v.z, v.w)); }
DI u64 flag_ld(u32x4 r, u32 voff) { u64 v; asm volatile("buffer_load_dwordx2 %0, %1, %2, 0 offen sc1" : "=v"(v) : "v"(voff), "s"(r) : "memory"); return v; }
DI u32x4 ldg(u32x4 r, u32 voff, u32 soff) { u32x4 v; asm volatile("buffer_load_dwordx4 %0, %1, %2, %3 offen sc1" : "=v"(v) : "v"(voff), "s"(r), "s"(soff) : "memory"); return v; }
template <int N> DI void vmw(u32x4& x) { asm volatile("s_waitcnt vmcnt(%1)" : "+v"(x) : "n"(N) : "memory"); }
#define VMWP(n, x) asm volatile("s_waitcnt vmcnt(" #n ")" : "+v"(x) :: "memory")
DI void setflag(char* ws, u32 off, u64 tag) { __hip_atomic_store((GAS u64*)(ws + off), tag, RLX_AGENT); }

#define STAMP(i)
#define STAMPC(i)
#define STAMP_OUT()

DI float xsum(float a, float c) {
  const auto r = __builtin_amdgcn_permlane32_swap(__float_as_uint(a), __float_as_uint(c), false, false);
  return __uint_as_float(r[0]) + __uint_as_float(r[1]);
}
DI f32x4 ln2_terms(float t) {
  const auto r = __builtin_amdgcn_permlane32_swap(__float_as_uint(t), __float_as_uint(t), false, false);
  const float mean = __uint_as_float(r[0]) * (1.f / D_);
  const float V = fmaxf(__uint_as_float(r[1]) * (1.f / D_) - mean * mean, 0.f) + 1e-5f;
  const float rs = __builtin_amdgcn_rsqf(V);
  const float K1 = -2.f * 0.7978845608028654f * 1.4426950408889634f * rs;
  const float lo = -mean - bflo(pk(-mean, 0.f));
  return f32x4{__uint_as_float(pk(-mean, lo)), V, K1, (K1 * rs) * (0.044715f * rs)};
}
template <int N> struct IC { static constexpr int value = N; };
template <int N, int I = 0, class F> DI void sfor(F&& f) { if constexpr (I < N) { f(IC<I>{}); sfor<N, I + 1>(f); } }
struct W1 { u32x4 rg, ru; u32 vg, vu, ra, lb; };
template <int T> DI void w1_g(const W1& c, u32 base) { dma(c.rg, c.lb + base, c.vg, T * (32 * F_ * 2)); }
template <int T> DI void w1_u(const W1& c, u32 base) { dma(c.ru, c.lb + base + 8192, c.vu, T * (32 * F_ * 2)); }
template <int T> DI void w1_issue(const W1& c, u32 base) { w1_g<T>(c, base); w1_u<T>(c, base); }
template <int T> DI void w1_read(const char* lds, u32 a, u32x4 (&W)[28]) {
#pragma unroll
  for (int kk = 0; kk < 2; ++kk) W[2 * T + kk] = cat(trd(lds, a + kk * 4096), trd(lds, a + kk * 4096 + 1024));
}
DI float x8sum(float v) {
  v += __builtin_amdgcn_update_dpp(0.f, v, 0xb1, 0xf, 0xf, true);
  v += __builtin_amdgcn_update_dpp(0.f, v, 0x4e, 0xf, 0xf, true);
  v += __builtin_amdgcn_update_dpp(0.f, v, 0x141, 0xf, 0xf, true);
  return v;
}

__global__ __launch_bounds__(512, 2) void h3_ffn(const char* __restrict__ gq, const char* __restrict__ gk, const char* __restrict__ gv,
                                                 const char* __restrict__ gsk, const char* __restrict__ gf, const char* __restrict__ gD,
                                                 const char* __restrict__ gwg, const char* __restrict__ gwu, const char* __restrict__ gwd,
                                                 char* __restrict__ gout, char* ws) {
  __shared__ __attribute__((aligned(16))) char lds[LDS_BYTES];
  const int tid = threadIdx.x, l = tid & 63, w = __builtin_amdgcn_readfirstlane(tid >> 6);
  const int b = blockIdx.x, xc = b & 7, sl = b >> 3;
  const int tb64 = 4 * xc + (sl >> 3), role = sl & 7;
  const int sc = (sl >> 3) * 7 + role;
  asm volatile("" :: "s"(gq), "s"(gk), "s"(gv), "s"(gsk), "s"(gf), "s"(gD), "s"(gwg), "s"(gwu), "s"(gwd), "s"(gout), "s"(ws));
  STAMP(0);
  const rsrc_t rw = mk(ws, WS_END);
  const int G = l >> 4, i16 = l & 15, q4 = i16 >> 2, p4 = l & 3, r8 = l >> 3;
  auto mktag = [&](u64 e0) AI { return ((u64)__builtin_amdgcn_readfirstlane((u32)e0) | ((u64)__builtin_amdgcn_readfirstlane((u32)(e0 >> 32)) << 32)) * 6364136223846793005ull + 1442695040888963407ull; };
  if (role == 7) {
    {
      u32 acc_t = 0;
      const char* bufs[9] = {ws + WS_U, gk, gv, gf, gwg, gwu, gwd, gout, gq};
      const u32 lens[9] = {WS_END - WS_U, S_ * D_ * 2, S_ * D_ * 2, S_ * D_ * 2, D_ * F_ * 2, D_ * F_ * 2, D_ * F_ * 2, S_ * D_ * 2, S_ * D_ * 2};
#pragma unroll
      for (int i = 0; i < 9; ++i) {
        const rsrc_t rt = mk(bufs[i], lens[i]);
        for (u32 o = (u32)(tid) * 65536 + 64; o < lens[i]; o += 33554432) acc_t ^= __builtin_amdgcn_raw_buffer_load_b32(rt, o, 0, 0);
      }
      asm volatile("" :: "v"(acc_t));
    }
    const rsrc_t rq = mk(gq, S_ * D_ * 2);
    const int row = 64 * tb64 + 8 * w + r8;
    u32x4 xs[7];
#pragma unroll
    for (int i = 0; i < 7; ++i) xs[i] = __builtin_amdgcn_raw_buffer_load_b128(rq, (u32)(row * 896 + (l & 7) * 16 + i * 128), 0, 0);
    const u64 e0 = __hip_atomic_load((GAS u64*)(ws + WS_E), RLX_AGENT);
    SB();
    SB();
    const u64 tag = mktag(e0);
    float s[4] = {};
#pragma unroll
    for (int i = 0; i < 7; ++i)
#pragma unroll
      for (int e = 0; e < 4; ++e) s[e] += bflo(xs[i][e]) + bfhi(xs[i][e]);
    const float mean = x8sum((s[0] + s[1]) + (s[2] + s[3])) * (1.f / D_);
    float v[4] = {};
#pragma unroll
    for (int i = 0; i < 7; ++i)
#pragma unroll
      for (int e = 0; e < 4; ++e) { const float d0 = bflo(xs[i][e]) - mean, d1 = bfhi(xs[i][e]) - mean; v[e] += d0 * d0; v[e] += d1 * d1; }
    const float rstd = __builtin_amdgcn_rsqf(x8sum((v[0] + v[1]) + (v[2] + v[3])) * (1.f / D_) + 1e-5f);
    const u32 so = WS_ST + (row >> 8) * 2048 + ((row >> 1) & 3) * 512 + ((row >> 5) & 7) * 64 + ((row >> 3) & 3) * 16 + (row & 1) * 8;
    __builtin_amdgcn_raw_buffer_store_b64(u32x2{__float_as_uint(mean), __float_as_uint(rstd)}, rw, (l & 7) == 0 ? so : OOB, 0, SC1);
    VMW(0); bar();
    if (tid == 0) setflag(ws, WS_F1 + (tb64 * 8 + 7) * 128, tag);
#pragma unroll
    for (int i = 0; i < 7; ++i) __builtin_amdgcn_raw_buffer_store_b128(u32x4{EMPTY, EMPTY, EMPTY, EMPTY}, rw, (u32)(tid * 16), WS_H + (u32)(tb64 * 2 * TB + i * 8192), SC1);
    __builtin_amdgcn_raw_buffer_store_b128(u32x4{EMPTY, EMPTY, EMPTY, EMPTY}, rw, tid < 64 ? WS_S2 + (u32)(tb64 * 1024 + tid * 16) : tid < 120 ? WS_CS + (u32)(tb64 * 896 + (tid - 64) * 16) : OOB, 0, SC1);
    VMW(0); bar();
    if (tid == 0) setflag(ws, WS_F2 + tb64 * 128, tag);
    STAMP(1);
    {
      const int mat = tb64 >= 14, j = tb64 < 28 ? tb64 - 14 * mat : 0;
      const rsrc_t rm = mk(mat ? gwu : gwg, D_ * F_ * 2);
      u32x4 wv[14];
#pragma unroll
      for (int i = 0; i < 14; ++i) wv[i] = __builtin_amdgcn_raw_buffer_load_b128(rm, tb64 < 28 ? (u32)(((4 * w + G + 32 * i) * F_ + 128 * j) * 2 + i16 * 16) : OOB, 0, 0);
      float c[8] = {};
#pragma unroll
      for (int i = 0; i < 14; ++i)
#pragma unroll
        for (int e = 0; e < 4; ++e) { c[2 * e] += bflo(wv[i][e]); c[2 * e + 1] += bfhi(wv[i][e]); }
      *(f32x4*)(lds + (4 * w + G) * 512 + i16 * 32) = f32x4{c[0], c[1], c[2], c[3]};
      *(f32x4*)(lds + (4 * w + G) * 512 + i16 * 32 + 16) = f32x4{c[4], c[5], c[6], c[7]};
      poll(ws, WS_F2 + (u32)(l & 31) * 128, true, tag);
      bar();
      if (tid < 128 && tb64 < 28) {
        float t = 0.f;
#pragma unroll
        for (int n = 0; n < 32; ++n) t += *(const float*)(lds + n * 512 + tid * 4);
        const u32 hi = pk(t, t), lo = pk(t - bflo(hi), 0.f);
        __builtin_amdgcn_raw_buffer_store_b64(u32x2{hi, lo}, rw, WS_CS + (u32)(((j * 8 + (tid >> 4)) * 32 + mat * 16 + (tid & 15)) * 8), 0, SC1);
      }
    }
    STAMP(2);
    const u32x4 dw = srd(ws, WS_END);
    const u32 lb = (u32)(size_t)(LAS char*)lds;
#pragma unroll 1
    for (int half = (tb64 & 1) ^ 1; half < 2; ++half) {
      const int tb = 32 * half + tb64;
#pragma unroll 1
      for (int it = 0; it < 4000; ++it) {
#pragma unroll
        for (int k = 0; k < 4; ++k) if (k < 3 || l < 32) dma<SC1>(dw, lb + 32768 + w * 3584 + k * 1024, l * 16, WS_H + (u32)(tb * TB + w * 3584 + k * 1024));
        VMW(0);
        u32 m = 0;
#pragma unroll
        for (int k = 0; k < 4; ++k) m = max(m, max4(*(const u32x4*)(lds + 32768 + w * 3584 + (k < 3 ? k * 1024 + l * 16 : 3072 + (l & 31) * 16))));
        if (!__any(m == EMPTY)) break;
        __builtin_amdgcn_s_sleep(6);
      }
      float s1 = 0.f, s2 = 0.f;
#pragma unroll
      for (int i = 0; i < 7; ++i) {
        const u32x2 t = trd(lds, 32768 + (u32)((56 * w + 4 * (G >> 1) + q4) * 64 + (G & 1) * 32 + p4 * 8) + i * 512);
        s1 = dot2(t.x, ONES, s1); s2 = dot2(t.x, t.x, s2); s1 = dot2(t.y, ONES, s1); s2 = dot2(t.y, t.y, s2);
      }
      *(float*)(lds + 20480 + half * 2048 + l * 32 + w * 4) = xsum(s1, s2);
      bar();
      if (w == 0) {
        const f32x4 a = *(const f32x4*)(lds + 20480 + half * 2048 + l * 32), c = *(const f32x4*)(lds + 20480 + half * 2048 + l * 32 + 16);
        __builtin_amdgcn_raw_buffer_store_b128(__builtin_bit_cast(u32x4, ln2_terms(((a.x + a.y) + (a.z + a.w)) + ((c.x + c.y) + (c.z + c.w)))), rw, l < 32 ? WS_S2 + (u32)(tb * 512 + l * 16) : OOB, 0, SC1);
      }
    }
    STAMP(3);
    STAMP_OUT();
    return;
  }
  const int cp = xc * 28 + sc, cch = w & 1, vp = w >> 1;
  u32x4 fhi; u32x2 flo; u32 Dh; u64 e0;
  const u32 lb = (u32)(size_t)(LAS char*)lds;
  const u32x4 dw = srd(ws, WS_END);
  {
    const u32x4 rk = srd(gk, S_ * D_ * 2), rv = srd(gv, S_ * D_ * 2);
    const int c8 = l & 7, s0 = 64 * tb64;
    const int swv = ((r8 >> 1) & 1) | ((w & 1) << 1), swk = swv ^ 2;
    const u32 g0 = (u32)((s0 + 8 * w + r8) * 896 + role * 128);
    if (w == 0) dma(rk, lb + L_K, g0 - 8 * 896 + ((c8 ^ (swv << 1)) << 4), 0);
    if (w == 1) dma(srd(gsk, D_ * 8), lb + L_SK, (u32)(512 * role + 16 * l), 0);
    dma(rk, lb + L_K + 1024 + w * 1024, g0 + ((c8 ^ (swk << 1)) << 4), 0);
    dma(rv, lb + L_V + w * 1024, g0 + ((c8 ^ (swv << 1)) << 4), 0);
    dma(srd(gq, S_ * D_ * 2), lb + L_X + w * 1024, g0 + ((c8 ^ (swv << 1)) << 4), 0);
    const rsrc_t rf = mk(gf, S_ * D_ * 2);
    const int fq = vp * 64 + l;
    const u32 fo = (u32)((2 * cp + cch) * 4096 + 16 * fq);
    fhi = __builtin_amdgcn_raw_buffer_load_b128(rf, fo, 0, 0);
    flo = __builtin_amdgcn_raw_buffer_load_b64(rf, fq > 0 ? fo - 8 : OOB, 0, 0);
    Dh = __builtin_amdgcn_raw_buffer_load_b16(mk(gD, D_ * 2), (u32)((2 * cp + ((l >> 2) & 1)) * 2), 0, 0);
    e0 = __hip_atomic_load((GAS u64*)(ws + WS_E), RLX_AGENT);
    SB();
  }
  const int gi = 4 * (xc >> 1) + sc / 7, gj = 7 * (xc & 1) + sc % 7;
  W1 w1;
  {
    const u32 v0 = (u32)(((4 * w + G) * F_ + 128 * gj) * 2);
    w1.rg = srd(gwg, D_ * F_ * 2); w1.ru = srd(gwu, D_ * F_ * 2); w1.lb = lb + w * 1024;
    w1.vg = v0 + ((i16 ^ (4 * G)) << 4); w1.vu = v0 + ((i16 ^ (4 * G + 2)) << 4);
    w1.ra = (u32)((G & 1) * 8192 + (8 * (G >> 1) + q4) * 256 + ((w ^ (2 * q4 + (G & 1))) * 32) + p4 * 8);
  }
  VMW(5); bar();
  STAMP(1);
  {
    const int tbl = w >> 2, chg = w & 3;
    const u32 rb = (u32)((32 * tbl + 8 * G + q4) * 128 + p4 * 8);
    const u32 sv = (u32)((chg ^ ((q4 >> 1) | ((G & 1) << 1))) * 32), sk2 = sv ^ 64;
    const u32x2 km = trd(lds, L_K + rb + 512 + sv), k0 = trd(lds, L_K + rb + 1024 + sk2), k1 = trd(lds, L_K + rb + 1536 + sk2);
    const u32x2 v0 = trd(lds, L_V + rb + sv), v1 = trd(lds, L_V + rb + 512 + sv);
    const u32x2 skv = *(const u32x2*)(lds + L_SK + (16 * chg + i16) * 8);
    const float kk[11] = {bfhi(km.x), bflo(km.y), bfhi(km.y), bflo(k0.x), bfhi(k0.x), bflo(k0.y), bfhi(k0.y), bflo(k1.x), bfhi(k1.x), bflo(k1.y), bfhi(k1.y)};
    const float vv[8] = {bflo(v0.x), bfhi(v0.x), bflo(v0.y), bfhi(v0.y), bflo(v1.x), bfhi(v1.x), bflo(v1.y), bfhi(v1.y)};
    const float s0 = bflo(skv.x), s1 = bfhi(skv.x), s2 = bflo(skv.y), s3 = bfhi(skv.y);
    float u[8];
#pragma unroll
    for (int e = 0; e < 8; ++e) u[e] = (kk[e + 3] * s0 + kk[e + 2] * s1 + kk[e + 1] * s2 + kk[e] * s3) * vv[e];
    const u32 o = (u32)((2 * tb64 + tbl) * TB + (64 * role + 16 * chg + i16) * 64 + G * 16);
    __builtin_amdgcn_raw_buffer_store_b128(u32x4{pk(u[0], u[1]), pk(u[2], u[3]), pk(u[4], u[5]), pk(u[6], u[7])}, rw, WS_U + o, 0, SC1);
    SB();
    STAMP(2);
    VMW(5); bar();
    const u32x2 x0 = trd(lds, L_X + rb + sv), x1 = trd(lds, L_X + rb + 512 + sv);
    __builtin_amdgcn_raw_buffer_store_b128(cat(x0, x1), rw, WS_X + o, 0, SC1);
    PIN(fhi); PIN(flo); PIN(Dh); PIN(e0);
    SB();
  }
  STAMP(3);
  {
    const u32 a0 = __builtin_amdgcn_alignbit(flo.y, flo.x, 16), a1 = __builtin_amdgcn_alignbit(fhi.x, flo.y, 16), a2 = __builtin_amdgcn_alignbit(fhi.y, fhi.x, 16),
              a3 = __builtin_amdgcn_alignbit(fhi.z, fhi.y, 16), a4 = __builtin_amdgcn_alignbit(fhi.w, fhi.z, 16);
    char* fc = lds + L_FC + cch * FCH + (vp * 64 + l) * 16 + 32;
    *(u32x4*)(fc) = fhi;
    *(u32x4*)(fc + FCS) = u32x4{a1, a2, a3, a4};
    *(u32x4*)(fc + 2 * FCS) = u32x4{flo.y, fhi.x, fhi.y, fhi.z};
    *(u32x4*)(fc + 3 * FCS) = u32x4{a0, a1, a2, a3};
    if (tid >= 128 && tid < 144) *(u32x4*)(lds + L_FC + (tid & 7) * FCS + ((tid >> 3) & 1) * 16) = u32x4{0, 0, 0, 0};
    if (tid >= 192 && tid < 252) { const int z = tid - 192; *(u32x4*)(lds + L_U + (z / 30) * UCH + (z % 30) * 16) = u32x4{0, 0, 0, 0}; }
  }
  const u64 tag = mktag(e0);
  const float Dv = bflo(Dh);
  SB();
  w1_issue<0>(w1, L_RING); w1_issue<1>(w1, L_RING + 16384);
  STAMP(4);
  VMW(4); bar();
  if (tid == 0) setflag(ws, WS_F1 + (tb64 * 8 + role) * 128, tag);
  STAMP(5);
  const int hh = l >> 5, tk = l & 31;
  const u32 gso = WS_G + (u32)(((2 * gi * 2 + (tk >> 4)) * 56 + 4 * gj + (w >> 1)) * 1024 + (2 * (w & 1) + hh) * 256 + i16 * 16);
  auto gofs = [](int g) AI { return (u32)((32 * (g >> 1) + (g & 1)) * 2 * 56 * 1024); };
  u32x4 W[28];
  w1_issue<2>(w1, L_RING + 32768); w1_issue<3>(w1, L_RING + 49152); w1_issue<4>(w1, L_RING + 65536); w1_issue<5>(w1, L_RED); w1_issue<6>(w1, L_RED + 16384);
  SB();

  const int ech = (l >> 2) & 1;
  const u32 eo = (u32)((8 * w + r8) * TB + (2 * cp + ech) * 64 + p4 * 16);
  STAMP(6);
  poll(ws, WS_F1 + (u32)(32 * w + 8 * (l & 3) + ((l & 4) ? 7 : (cp >> 5))) * 128, l < 8, tag);
  STAMP(7);
  u32x4 uu = ldg(dw, eo, WS_U), ex = ldg(dw, eo, WS_X);
  u32x4 est[4];
#pragma unroll
  for (int i = 0; i < 4; ++i) est[i] = ldg(dw, (u32)(r8 * 64 + p4 * 16), WS_ST + (u32)(w * 2048 + i * 512));
  u64 fl = flag_ld(dw, WS_F2 + (u32)(4 * w + (l & 3)) * 128);
  bar();
  const u32 ra0 = opq(w1.ra), ra1 = opq(w1.ra + L_RING), ra2 = opq(w1.ra + L_RING + 49152);
  w1_read<5>(lds, ra0 + L_RED, W); w1_read<6>(lds, ra0 + L_RED + 16384, W);
  w1_read<0>(lds, ra1, W); w1_read<1>(lds, ra1 + 16384, W); w1_read<2>(lds, ra1 + 32768, W); w1_read<3>(lds, ra2, W); w1_read<4>(lds, ra2 + 16384, W);
  const u32 aA = opq(L_FC + cch * FCH + q4 * FCS + 64 - 16 * G + 8 * p4 - 8 + 128 * vp);
  const u32 aB = opq(L_U + cch * UCH + 32 * i16 + 16 * G + 448 - 128 * vp - 64);
  const u32 rwr = opq(L_RED + w * 4096 + G * 256 + ((i16 ^ ((G >> 1) << 3)) << 4));
  const u32 rr = opq(L_RED + ech * 4096 + (w & 3) * 1024 + (p4 & 1) * 512 + (((2 * r8 + (p4 >> 1)) ^ ((p4 & 1) << 3)) << 4));
  SB();
  vmw<6>(uu);
  *(u32x4*)(lds + L_U + ech * UCH + 480 + (8 * w + r8) * 64 + p4 * 16) = uu;
  bar();
  STAMP(8);
  auto rdA = [&](int v, int m) AI { const u32 o = 64 * v + 512 * m; return cat(trd(lds, aA + o + 8), trd(lds, aA + o)); };
  auto rdB = [&](int v, int m) AI { return *(const u32x4*)(lds + aB + 64 * (1 - v) + 512 * m); };
  float ex8[8], eq8[8], eu8[8];
  auto terms = [&]() AI {
#pragma unroll
    for (int i = 0; i < 4; ++i) {
      ex8[2 * i] = bflo(ex[i]); ex8[2 * i + 1] = bfhi(ex[i]);
      eu8[2 * i] = bflo(uu[i]) * Dv; eu8[2 * i + 1] = op(bfhi(uu[i]) * Dv);
      eq8[2 * i] = (ex8[2 * i] - __uint_as_float(est[i].x)) * __uint_as_float(est[i].y);
      eq8[2 * i + 1] = op(op(ex8[2 * i + 1] - __uint_as_float(est[i].z)) * __uint_as_float(est[i].w));
    }
#pragma unroll
    for (int i = 0; i < 8; ++i) { PIN(ex8[i]); PIN(eq8[i]); PIN(eu8[i]); }
  };
  auto epilogue = [&]() AI {
    f32x4 p[8];
#pragma unroll
    for (int n = 0; n < 4; ++n) { p[2 * n] = *(const f32x4*)(lds + rr + n * 8192); p[2 * n + 1] = *(const f32x4*)(lds + rr + n * 8192 + 256); }
    float h[8];
#pragma unroll
    for (int i = 0; i < 8; ++i) {
      const float y = op(op(eu8[i] + p[i >> 2][i & 3]) + p[2 + (i >> 2)][i & 3]) + op(p[4 + (i >> 2)][i & 3] + p[6 + (i >> 2)][i & 3]);
      h[i] = op(ex8[i] + eq8[i] * op(y));
    }
    __builtin_amdgcn_raw_buffer_store_b128(u32x4{pk(h[0], h[1]), pk(h[2], h[3]), pk(h[4], h[5]), pk(h[6], h[7])}, rw, WS_H + eo, 0, SC1);
  };
  {
    f32x4 acc[4];
    u32x4 A[2][4], B[2][4];
#pragma unroll
    for (int v = 0; v < 2; ++v) {
      A[v][0] = rdA(v, 0); SB();
#pragma unroll
      for (int m = 0; m < 4; ++m) { B[v][m] = rdB(v, m); SB(); }
#pragma unroll
      for (int m = 1; m < 4; ++m) { A[v][m] = rdA(v, m); SB(); }
      if (v == 0) {
        vmw<0>(ex); PIN(est[0]); PIN(est[1]); PIN(est[2]); PIN(est[3]); PIN(fl);
        if (!__all(fl == tag)) poll<2>(ws, WS_F2 + (u32)(4 * w + (l & 3)) * 128, true, tag);
        if (w < 4) terms();
        SB();
      }
    }
#pragma unroll
    for (int v = 0; v < 2; ++v)
#pragma unroll
      for (int m = 0; m < 4; ++m) {
#pragma unroll
        for (int c = m; c < 4; ++c) acc[c] = mfma16(A[v][m], B[v][c - m], (v == 0 && m == 0) ? f32x4{} : acc[c]);
        SB();
        if (v == 0 && m == 0) w1_g<7>(w1, L_RING);
        if (v == 0 && m == 2) w1_u<7>(w1, L_RING);
        if (v == 1 && m == 0) w1_g<8>(w1, L_RING + 16384);
        SB();
      }
#pragma unroll
    for (int c = 0; c < 4; ++c) *(f32x4*)(lds + rwr + c * 1024) = acc[c];
  }
  bar();
  STAMP(9);
  if (w < 4) epilogue();
  SB();
  STAMP(10);
  {
    f32x4 acc[4];
    u32x4 A[2][8], B[2][8];
    auto rd = [&](int v, int m) AI {
      if (m == 0) {
        A[v][0] = rdA(v, 0);
#pragma unroll
        for (int d = 4; d < 8; ++d) B[v][d] = rdB(v, d);
      } else { A[v][m] = rdA(v, m); if (m <= 4) B[v][4 - m] = rdB(v, 4 - m); }
      SB();
    };
    rd(0, 0); rd(0, 1); rd(0, 2); rd(0, 3);
#pragma unroll
    for (int v = 0; v < 2; ++v) {
#pragma unroll
      for (int m = 0; m < 8; ++m) {
#pragma unroll
        for (int c = 4; c < 8; ++c) if (m <= c) acc[c - 4] = mfma16(A[v][m], B[v][c - m], (v == 0 && m == 0) ? f32x4{} : acc[c - 4]);
        SB();
        if (m < 4) rd(v, m + 4); else if (v == 0) rd(1, m - 4);
        if (v == 0) {
          if (m == 0) w1_u<8>(w1, L_RING + 16384);
          if (m == 1) w1_g<9>(w1, L_RING + 32768);
          if (m == 2) w1_u<9>(w1, L_RING + 32768);
          if (m == 3) w1_g<10>(w1, L_RING + 49152);
          if (m == 4) w1_u<10>(w1, L_RING + 49152);
          if (m == 6) w1_g<11>(w1, L_RING + 65536);
          if (m == 7) w1_u<11>(w1, L_RING + 65536);
        }
        SB();
      }
    }
    if (w >= 4) terms();
    STAMP(11);
    VMW(0); bar();
#pragma unroll
    for (int c = 0; c < 4; ++c) *(f32x4*)(lds + rwr + c * 1024) = acc[c];
  }
  const u32 lo16 = l * 16;
  if (w < 4) {
#pragma unroll
    for (int k = 0; k < 7; ++k) dma<SC1>(dw, lb + L_H0 + (7 * w + k) * 1024, lo16, WS_H + (u32)(2 * gi * TB + (7 * w + k) * 1024));
  }
  w1_read<7>(lds, ra1, W); w1_read<8>(lds, ra1 + 16384, W); w1_read<9>(lds, ra1 + 32768, W); w1_read<10>(lds, ra2, W); w1_read<11>(lds, ra2 + 16384, W);
  bar();
  STAMP(12);
  if (w >= 4) {
    epilogue();
    SB();
#pragma unroll
    for (int i = 0; i < 2; ++i) {
      W1 c = w1; c.lb -= i * 4096; c.vg -= i * (16 * F_ * 2); c.vu -= i * (16 * F_ * 2);
      w1_issue<12>(c, L_WE); w1_issue<13>(c, L_WE + 16384);
    }
  } else {
    const u32 ha = opq(L_H0 + 7 * w * 1024 + lo16);
#pragma unroll 1
    for (int it = 0; it < 4000; ++it) {
      VMW(0);
      u32 m = 0;
#pragma unroll
      for (int k = 0; k < 7; ++k) { const u32x4 v = *(const u32x4*)(lds + ha + k * 1024); m = max(m, max4(v)); }
      if (!__any(m == EMPTY)) break;
#pragma unroll
      for (int k = 0; k < 7; ++k) dma<SC1>(dw, lb + L_H0 + (7 * w + k) * 1024, lo16, WS_H + (u32)(2 * gi * TB + (7 * w + k) * 1024));
    }
    VMW(0);
    STAMP(13);
  }
  bar();
  STAMP(14);

  u64 csv;
  const u32 bta = (u32)((8 * (G >> 1) + q4) * 64 + (G & 1) * 32 + p4 * 8);
  const u32 sta = (u32)((56 * w + 4 * (G >> 1) + q4) * 64 + (G & 1) * 32 + p4 * 8);
  auto hbase = [](int g) AI { return g == 0 ? L_H0 : g == 1 ? L_H1 : g == 2 ? L_H2 : L_H3; };
  const u32 spa = opq(L_SP + l * 32);
  float s1a, s1b, s2a, s2b, xs; u32x2 st[2];
  auto stats_step = [&](u32 b0, int k) AI {
    if (k > 0) {
      if (k & 1) { s1a = k == 1 ? xs : s1a + xs; s2a = k == 1 ? xs * xs : s2a + xs * xs; PIN(s1a); PIN(s2a); }
      else       { s1b = k == 2 ? xs : s1b + xs; s2b = k == 2 ? xs * xs : s2b + xs * xs; PIN(s1b); PIN(s2b); }
    }
    if (k < 28) {
      const int u = k >> 2;
      const u32 d = (k & 2) ? st[u & 1].y : st[u & 1].x;
      xs = (k & 1) ? bfhi(d) : bflo(d); PIN(xs);
      if ((k & 3) == 3 && u + 2 < 7) st[u & 1] = trd(lds, b0 + (u + 2) * 512);
    } else *(float*)(lds + spa + w * 4) = xsum(s1a + s1b, s2a + s2b);
  };
  f32x4 sp0, sp1, tm; float gu[8], gt[8]; u32 od[4];
  auto center = [&](f32x16& acc, float m) AI {
    const u32 d = __float_as_uint(m);
    acc = mfma32(u32x4{(u32)csv, (u32)(csv >> 32), 0, 0}, u32x4{d, d & 0xffffu, 0, 0}, acc);
  };
  auto geglu_step = [&](int g, f32x16& acc, int i) AI {
    if (g == 0) {
      if (i == 1) { sp0 = *(const f32x4*)(lds + spa); sp1 = *(const f32x4*)(lds + spa + 16); }
      if (i == 3) { sp0.x = op(sp0.x + sp0.y); sp0.z = op(sp0.z + sp0.w); sp1.x = op(sp1.x + sp1.y); sp1.z = op(sp1.z + sp1.w); PIN(sp0); PIN(sp1); }
      if (i == 4) { tm = ln2_terms((sp0.x + sp0.z) + (sp1.x + sp1.z)); PIN(tm); }
      if (i == 5) center(acc, tm.x);
    }
#pragma unroll
    for (int e = 0; e < 8; ++e) {
      const int s = i - (g == 0 ? 7 + (13 * e) / 7 : 2 + (18 * e) / 7);
      if (s == 0) { gt[e] = acc[e] * acc[e]; gu[e] = op(acc[e] * acc[e + 8]); PIN(gt[e]); PIN(gu[e]); }
      if (s == 1) { gt[e] = tm.w * gt[e] + tm.z; PIN(gt[e]); }
      if (s == 2) { gt[e] = acc[e] * gt[e]; PIN(gt[e]); }
      if (s == 3) { gt[e] = __builtin_amdgcn_exp2f(gt[e]); PIN(gt[e]); }
      if (s == 4) { gt[e] = gt[e] * tm.y + tm.y; PIN(gt[e]); }
      if (s == 5) { gt[e] = __builtin_amdgcn_rcpf(gt[e]); PIN(gt[e]); }
      if (s == 6) { gu[e] = gu[e] * gt[e]; PIN(gu[e]); }
      if (s == 7 && (e & 1)) { od[e >> 1] = pk(gu[e - 1], gu[e]); PIN(od[e >> 1]); }
    }
    if (i == 28) {
      __builtin_amdgcn_raw_buffer_store_b128(u32x4{od[0], od[1], od[2], od[3]}, rw, gso, gofs(g), SC1);
    }
  };
  constexpr int PF = 4;
  u32x4 B[PF];
  u32 ba = opq(hbase(0) + bta);
  auto chain = [&](int g, f32x16& acc, auto side) AI {
    const u32 bn = opq(hbase(g < 3 ? g + 1 : g) + bta);
    sfor<28>([&](auto kc) AI {
      constexpr int k = decltype(kc)::value;
      acc = mfma32(W[k], B[k % PF], k == 0 ? f32x16{} : acc);
      SB();
      side(k);
      if (k + PF < 28) B[k % PF] = cat(trd(lds, ba + (k + PF) * 1024), trd(lds, ba + (k + PF) * 1024 + 256));
      else if (g < 3) B[k % PF] = cat(trd(lds, bn + (k + PF - 28) * 1024), trd(lds, bn + (k + PF - 28) * 1024 + 256));
      SB();
    });
    ba = bn;
  };
  auto flag3 = [&](int g) AI { setflag(ws, WS_F3 + (u32)(((gi * 4 + g) * 14) + gj) * 128, tag); };
  const int ri = 2 * xc + sc / 14, rq2 = (sc / 7) & 1, n2 = sc % 7;
  const u32x4 rd = srd(gwd, D_ * F_ * 2);
  const u32 wdv = (u32)(((224 * w + r8) * D_ + 64 * n2) * 2 + (((l & 7) ^ (((r8 >> 1) & 3) << 1)) << 4));
  const u32 blkR = w < 7 ? L_R + w * 4096 : L_R2;
  u32 wda[4];
#pragma unroll
  for (int nt = 0; nt < 4; ++nt) wda[nt] = (16 * (G >> 1) + 4 * (G & 1) + q4) * 128 + ((nt ^ (2 * (G & 1) + (q4 >> 1))) * 32) + p4 * 8;
  u32x4 Wd[7][4];
  auto wd_blk = [&](int k) AI { return k % 3 == 1 ? blkR : (k % 3 == 2 ? L_Q : L_P) + w * 4096; };
  auto wd_dma = [&](int k, int i) AI { dma(rd, lb + wd_blk(k) + i * 1024, wdv, (4 * k + i) * (8 * D_ * 2)); };
  auto wd_read = [&](int k) AI {
#pragma unroll
    for (int nt = 0; nt < 4; ++nt) { const u32 a = opq(wda[nt] + wd_blk(k)); Wd[k][nt] = cat(trd(lds, a), trd(lds, a + 1024)); }
  };
  const int j0 = (14 * w) >> 3, jn = ((14 * w + 13) >> 3) - j0;
  const u32 f3o = WS_F3 + (u32)(ri * 56 + j0 + ((l & 3) < jn ? (l & 3) : jn)) * 128;

  __builtin_amdgcn_s_setprio(1);
  f32x16 accA, accB;
  u32x4 tm1, tm2, tm3;
  const u32 s2o = WS_S2 + (u32)(2 * gi * 512 + tk * 16);
  auto h_dma = [&](int g, int k) AI {
    if (k < 3 || l < 32) dma<SC1>(dw, lb + hbase(g) + w * 3584 + k * 1024, lo16, WS_H + (u32)((32 * (g >> 1) + 2 * gi + (g & 1)) * TB + w * 3584 + k * 1024));
  };
  const u32 cso = l < 32 ? WS_CS + (u32)(((gj * 8 + w) * 32 + l) * 8) : OOB;
  u32x4 cv[4];
  auto h_rd = [&](int g) AI {
    const u32 a = opq(hbase(g) + w * 3584 + lo16), a3 = opq(hbase(g) + w * 3584 + 3072 + tk * 16);
    cv[0] = *(const u32x4*)(lds + a); cv[1] = *(const u32x4*)(lds + a + 1024); cv[2] = *(const u32x4*)(lds + a + 2048); cv[3] = *(const u32x4*)(lds + a3);
  };
  auto h_fix = [&](int g) AI {
    u32 cm = g == 1 ? max((u32)csv, (u32)(csv >> 32)) : g == 2 ? max4(tm1) : max(max4(tm2), max4(tm3));
    cm = max(max(cm, max(max4(cv[0]), max4(cv[1]))), max(max4(cv[2]), max4(cv[3])));
#pragma unroll 1
    for (int it = 0; it < 4000 && __any(cm == EMPTY); ++it) {
      h_dma(g, 0); h_dma(g, 1); h_dma(g, 2); h_dma(g, 3);
      if (g == 1) { csv = flag_ld(dw, cso); VMWP(0, csv); }
      if (g == 2) { tm1 = ldg(dw, s2o, 512); vmw<0>(tm1); }
      if (g == 3) { tm2 = ldg(dw, s2o, 32 * 512); tm3 = ldg(dw, s2o, 33 * 512); vmw<0>(tm2); PIN(tm3); }
      h_rd(g);
      cm = g == 1 ? max((u32)csv, (u32)(csv >> 32)) : g == 2 ? max4(tm1) : max(max4(tm2), max4(tm3));
      cm = max(max(cm, max(max4(cv[0]), max4(cv[1]))), max(max4(cv[2]), max4(cv[3])));
    }
  };
  SB();
  {
    const u32 b0 = opq(hbase(0) + sta);
#pragma unroll
    for (int k = 0; k < PF; ++k) B[k] = cat(trd(lds, ba + k * 1024), trd(lds, ba + k * 1024 + 256));
    st[0] = trd(lds, b0); st[1] = trd(lds, b0 + 512);
    SB();
    chain(0, accA, [&](int k) AI {
      stats_step(b0, k);
      if (k <= 6 && !(k & 1)) h_dma(1, k >> 1);
      if (k == 7) csv = flag_ld(dw, cso);
      if (k == 10 || k == 13) __builtin_amdgcn_raw_buffer_store_b128(u32x4{EMPTY, EMPTY, EMPTY, EMPTY}, rw, gso, gofs(k == 10 ? 2 : 3), SC1);
      if (k == 14) { VMW(7); RBAR(); const u32 ra3 = opq(w1.ra + L_WE); w1_read<12>(lds, ra3, W); w1_read<13>(lds, ra3 + 16384, W); }
      if (k == 19) { VMWP(2, csv); h_rd(1); }
      if (k == 22) { h_fix(1); RBAR(); }
      if (k >= 23 && (k & 1)) h_dma(2, (k - 23) >> 1);
    });
    stats_step(b0, 28);
  }
  STAMP(16);
  chain(1, accB, [&](int k) AI {
    if (k == 1) { bar(); if (tid == 0 && b == 0) setflag(ws, WS_E, tag); h_dma(2, 3); }
    geglu_step(0, accA, k);
    if (k == 3) tm1 = ldg(dw, s2o, 512);
    if (k >= 5 && k <= 14 && k % 3 == 2) wd_dma(0, (k - 5) / 3);
    if (k >= 16 && k <= 25 && k % 3 == 1) h_dma(3, (k - 16) / 3);
    if (k == 20) { vmw<6>(tm1); h_rd(2); }
    if (k == 23) { h_fix(2); RBAR(); }
  });
  center(accB, __uint_as_float(tm1.x));
  geglu_step(0, accA, 28);
  tm = __builtin_bit_cast(f32x4, tm1);
  SB();
  STAMP(18);
  chain(2, accA, [&](int k) AI {
    if (k == 1) RBAR();
    geglu_step(1, accB, k);
    if (k <= 6 && !(k & 1)) wd_dma(1, k >> 1);
    if (k == 3) { VMW(7); wd_read(0); }
    if (k == 8) { tm2 = ldg(dw, s2o, 32 * 512); tm3 = ldg(dw, s2o, 33 * 512); }
    if (k >= 10 && k <= 24 && !(k & 1)) wd_dma(2 + ((k - 10) >> 3), ((k - 10) >> 1) & 3);
    if (k == 19) { VMW(11); h_rd(3); }
    if (k == 23) { vmw<7>(tm2); PIN(tm3); h_fix(3); RBAR(); if (tid == 0) flag3(0); }
  });
  center(accA, __uint_as_float(tm2.x));
  geglu_step(1, accB, 28);
  tm = __builtin_bit_cast(f32x4, tm2);
  SB();
  STAMP(20);
  chain(3, accB, [&](int k) AI {
    geglu_step(2, accA, k);
    if (k == 12) fl = flag_ld(dw, f3o);
    if (k == 15) { VMW(1); RBAR(); if (tid == 0) flag3(1); }
    if (k >= 15 && k < 18) wd_read(k - 14);
    if (k == 19) LG0();
    if (k >= 19 && k < 27 && (k & 1)) wd_dma(4, (k - 19) >> 1);
  });
  center(accB, __uint_as_float(tm3.x));
  geglu_step(2, accA, 28);
  tm = __builtin_bit_cast(f32x4, tm3);
  STAMP(21);
#pragma unroll
  for (int e = 0; e < 8; ++e) {
    const float a = accB[e], u = accB[e + 8];
    gu[e] = (a * u) * __builtin_amdgcn_rcpf(__builtin_amdgcn_exp2f(a * (tm.w * (a * a) + tm.z)) * tm.y + tm.y);
  }
#pragma unroll
  for (int e = 0; e < 4; ++e) od[e] = pk(gu[2 * e], gu[2 * e + 1]);
  geglu_step(3, accB, 28);
  __builtin_amdgcn_s_setprio(0);
  SB();
  STAMP(22);
  const u32 gb = WS_G + (u32)((2 * ri * 2 + rq2) * 56 + 7 * w) * 1024;
  u32x4 Hf[3][7];
  auto h_load = [&](int g, int k0, int k1) AI {
#pragma unroll
    for (int k = k0; k < k1; ++k) Hf[g % 3][k] = ldg(dw, lo16, gb + gofs(g) + k * 1024);
  };
  const rsrc_t ro = mk(gout, S_ * D_ * 2);
  const u32 pw = (u32)(i16 * 256), tf = 4 * (w & 3) + G;
  f32x4 acc[4];
  u32x2 res[4];
  auto step = [&](auto gc, auto kc, auto nc) AI {
    constexpr int g = decltype(gc)::value, k = decltype(kc)::value;
    vmw<decltype(nc)::value>(Hf[g % 3][k]);
#pragma unroll
    for (int nt = 0; nt < 4; ++nt) acc[nt] = mfma16(Wd[k][nt], Hf[g % 3][k], k == 0 ? f32x4{} : acc[nt]);
    SB();
  };
  auto put = [&](int g) AI {
    const u32 rb = opq(((g & 1) ? L_Q : L_P) + w * 4096 + pw);
#pragma unroll
    for (int nt = 0; nt < 4; ++nt) *(f32x4*)(lds + rb + (((4 * nt + G) ^ (i16 & 7)) << 4)) = acc[nt];
    *(f32x4*)(lds + L_RES + (g & 1) * 4096 + pw + (((2 * w + (G & 1)) ^ (i16 & 7)) << 4)) = f32x4{bflo(res[g].x), bfhi(res[g].x), bflo(res[g].y), bfhi(res[g].y)};
  };
  auto fin = [&](int g) AI {
    if ((w >> 2) == (g & 1)) {
      const u32 a0 = tf * 256 + ((i16 ^ (tf & 7)) << 4), a = opq(((g & 1) ? L_Q : L_P) + a0);
      f32x4 sum = *(const f32x4*)(lds + L_RES + (g & 1) * 4096 + a0);
#pragma unroll
      for (int n = 0; n < 8; ++n) sum += *(const f32x4*)(lds + a + n * 4096);
      const int tok = 32 * (32 * (g >> 1) + (g & 1) + 2 * ri) + 16 * rq2 + tf;
      __builtin_amdgcn_raw_buffer_store_b64(u32x2{pk(sum.x, sum.y), pk(sum.z, sum.w)}, ro, (u32)((tok * D_ + 64 * n2 + 4 * i16) * 2), 0, 0);
    }
  };
  u32 chk;
  auto stepc = [&](auto gc, auto kc, auto nc) AI {
    constexpr int g = decltype(gc)::value, k = decltype(kc)::value;
    step(gc, kc, nc);
    const u32x4 v = Hf[g % 3][k];
    chk = k == 0 ? max4(v) : max(chk, max4(v));
    PIN(chk);
  };
  auto redo = [&](auto gc) AI {
    constexpr int g = decltype(gc)::value;
#pragma unroll 1
    for (int it = 0; it < 4000 && __any(chk == EMPTY); ++it) {
      h_load(g, 0, 7);
      stepc(gc, IC<0>{}, IC<6>{}); stepc(gc, IC<1>{}, IC<5>{}); stepc(gc, IC<2>{}, IC<4>{}); stepc(gc, IC<3>{}, IC<3>{});
      stepc(gc, IC<4>{}, IC<2>{}); stepc(gc, IC<5>{}, IC<1>{}); stepc(gc, IC<6>{}, IC<0>{});
    }
  };
  VMWP(6, fl);
  if (!__all(fl == tag)) poll(ws, f3o, true, tag);
  wd_dma(5, 0); wd_dma(5, 1); wd_dma(5, 2); wd_dma(5, 3);
  h_load(0, 0, 3);
  fl = flag_ld(dw, f3o + 14 * 128);
  h_load(0, 3, 7);
  const u32 hro = WS_H + (u32)((32 * (G >> 1) + (G & 1) + 2 * ri) * TB + (64 * n2 + 8 * w + (i16 >> 1)) * 64 + rq2 * 32 + (i16 & 1) * 16);
  u32x4 hr = ldg(dw, hro, 0);
  wd_dma(6, 0); wd_dma(6, 1); wd_dma(6, 2); wd_dma(6, 3);
  VMWP(9, fl);
  if (!__all(fl == tag)) poll(ws, f3o + 14 * 128, true, tag);
  wd_read(4); wd_read(5);
  SB();
  STAMP(23);
  h_load(1, 0, 1); step(IC<0>{}, IC<0>{}, IC<10>{});
  h_load(1, 1, 2); step(IC<0>{}, IC<1>{}, IC<11>{});
  h_load(1, 2, 3); step(IC<0>{}, IC<2>{}, IC<12>{});
  h_load(1, 3, 4); step(IC<0>{}, IC<3>{}, IC<12>{});
  h_load(1, 4, 5); step(IC<0>{}, IC<4>{}, IC<12>{});
  h_load(1, 5, 6); step(IC<0>{}, IC<5>{}, IC<12>{});
  h_load(1, 6, 7);
  vmw<7>(hr);
#pragma unroll 1
  for (int it = 0; it < 4000 && __any(max4(hr) == EMPTY); ++it) { hr = ldg(dw, hro, 0); vmw<0>(hr); }
  wd_read(6);
  *(u32x4*)(lds + blkR + l * 16) = hr;
#pragma unroll
  for (int g = 0; g < 4; ++g) res[g] = trd(lds, blkR + g * 256 + (4 * (G & 1) + q4) * 32 + p4 * 8);
  h_load(2, 0, 2);
  step(IC<0>{}, IC<6>{}, IC<9>{});
  put(0);
  STAMP(24);
  h_load(2, 2, 3); step(IC<1>{}, IC<0>{}, IC<9>{});
  h_load(2, 3, 5); step(IC<1>{}, IC<1>{}, IC<10>{});
  h_load(2, 5, 7); step(IC<1>{}, IC<2>{}, IC<11>{});
  h_load(3, 0, 2); step(IC<1>{}, IC<3>{}, IC<12>{});
  h_load(3, 2, 4); step(IC<1>{}, IC<4>{}, IC<13>{});
  h_load(3, 4, 6); step(IC<1>{}, IC<5>{}, IC<14>{});
  h_load(3, 6, 7); step(IC<1>{}, IC<6>{}, IC<14>{});
  put(1);
  STAMP(25);
  bar();
  fin(0); fin(1);
  SB();
  stepc(IC<2>{}, IC<0>{}, IC<13>{}); stepc(IC<2>{}, IC<1>{}, IC<12>{}); stepc(IC<2>{}, IC<2>{}, IC<11>{}); stepc(IC<2>{}, IC<3>{}, IC<10>{});
  stepc(IC<2>{}, IC<4>{}, IC<9>{}); stepc(IC<2>{}, IC<5>{}, IC<8>{}); stepc(IC<2>{}, IC<6>{}, IC<7>{});
  redo(IC<2>{});
  bar();
  put(2);
  SB();
  STAMP(26);
  stepc(IC<3>{}, IC<0>{}, IC<6>{}); stepc(IC<3>{}, IC<1>{}, IC<5>{}); stepc(IC<3>{}, IC<2>{}, IC<4>{}); stepc(IC<3>{}, IC<3>{}, IC<3>{});
  stepc(IC<3>{}, IC<4>{}, IC<2>{}); stepc(IC<3>{}, IC<5>{}, IC<1>{}); stepc(IC<3>{}, IC<6>{}, IC<0>{});
  redo(IC<3>{});
  put(3);
  bar();
  STAMP(27);
  fin(2); fin(3);
  STAMP(28);
  STAMP_OUT();
}

}

extern "C" void kernel_launch(void* const* d_in, const int* in_sizes, int n_in, void* d_out, int out_size, void* d_ws, size_t ws_size,
                              hipStream_t stream) {
  h3_ffn<<<256, 512, 0, stream>>>((const char*)d_in[0], (const char*)d_in[1], (const char*)d_in[2], (const char*)d_in[3], (const char*)d_in[4],
                                  (const char*)d_in[5], (const char*)d_in[7], (const char*)d_in[8], (const char*)d_in[9], (char*)d_out, (char*)d_ws);
}
```

```cpp
#include <hip/hip_runtime.h>
#include <stdint.h>

namespace {

#define LAS __attribute__((address_space(3)))
#define GAS __attribute__((address_space(1)))
#define DI __device__ __forceinline__
#define AI __attribute__((always_inline))
typedef uint32_t u32;
typedef unsigned long long u64;
typedef u32 u32x2 __attribute__((ext_vector_type(2)));
typedef u32 u32x4 __attribute__((ext_vector_type(4)));
typedef short s16x4 __attribute__((ext_vector_type(4)));
typedef short s16x8 __attribute__((ext_vector_type(8)));
typedef float f32x4 __attribute__((ext_vector_type(4)));
typedef float f32x16 __attribute__((ext_vector_type(16)));
typedef __bf16 bf2 __attribute__((ext_vector_type(2)));
typedef __amdgpu_buffer_rsrc_t rsrc_t;
#define RLX_AGENT __ATOMIC_RELAXED, __HIP_MEMORY_SCOPE_AGENT

constexpr int S_ = 2048, D_ = 448, F_ = 1792;
constexpr u32 TB = 448 * 64;
constexpr u32 WS_E  = 0;
constexpr u32 WS_F1 = 128;
constexpr u32 WS_F3 = WS_F1 + 256 * 128;
constexpr u32 WS_F2 = WS_F3 + 896 * 128;
constexpr u32 WS_S2 = 256 * 1024 + 16384;
constexpr u32 WS_CS = 224 * 1024;
constexpr u32 WS_ST = 256 * 1024;
constexpr u32 WS_U  = WS_S2 + 32768;
constexpr u32 WS_X  = WS_U + 64 * TB;
constexpr u32 WS_H  = WS_X + 64 * TB;
constexpr u32 WS_G  = WS_H + 64 * TB;
constexpr u32 WS_END = WS_G + 64 * 112 * 1024;
constexpr u32 WS_DBG = 16u << 20;
constexpr int SC1 = 16;
constexpr u32 OOB = 0xFFFFFF00u;
constexpr u32 ONES = 0x3f803f80u;
constexpr u32 EMPTY = 0xffffffffu;

constexpr u32 L_K = 0, L_V = 9216, L_X = 17408, L_SK = 25600;
constexpr u32 L_RED = 0, L_FC = 32768, FCS = 4160, FCH = 4 * FCS;
constexpr u32 L_U = L_FC + 2 * FCH, UCH = 4608, L_RING = L_U + 2 * UCH;
constexpr u32 L_H0 = 32768, L_H1 = L_H0 + TB, L_WE = L_H1 + TB, L_H2 = 0, L_H3 = L_WE + 32768, L_SP = 155648;
constexpr u32 L_P = TB, L_Q = L_P + 32768, L_R = L_Q + 32768, L_R2 = L_H3 + TB;
constexpr u32 L_RES = 0;
constexpr u32 LDS_BYTES = 163840;

DI float bflo(u32 x) { return __uint_as_float(x << 16); }
DI float bfhi(u32 x) { return __uint_as_float(x & 0xffff0000u); }
DI u32 pk(float lo, float hi) { bf2 p; p.x = (__bf16)lo; p.y = (__bf16)hi; return __builtin_bit_cast(u32, p); }
DI rsrc_t mk(const void* p, u32 bytes) { return __builtin_amdgcn_make_buffer_rsrc((void*)p, 0, bytes, 0x00020000); }
DI u32x2 trd(const char* lds, u32 a) { return __builtin_bit_cast(u32x2, __builtin_amdgcn_ds_read_tr16_b64_v4i16((LAS s16x4*)(lds + a))); }
DI u32x4 cat(u32x2 a, u32x2 b) { return u32x4{a.x, a.y, b.x, b.y}; }
DI u32x4 srd(const void* p, u32 bytes) { const u64 a = (u64)p; return u32x4{(u32)a, (u32)(a >> 32) & 0xffffu, bytes, 0x00020000u}; }
template <int AUX = 0> DI void dma(u32x4 r, u32 ldsoff, u32 voff, u32 soff) {
  if (AUX) asm volatile("s_mov_b32 m0, %0\n\ts_nop 0\n\tbuffer_load_dwordx4 %1, %2, %3 offen sc1 lds" :: "s"(ldsoff), "v"(voff), "s"(r), "s"(soff) : "memory");
  else asm volatile("s_mov_b32 m0, %0\n\ts_nop 0\n\tbuffer_load_dwordx4 %1, %2, %3 offen lds" :: "s"(ldsoff), "v"(voff), "s"(r), "s"(soff) : "memory");
}
DI float fnma(float a, float b, float c) { float r; asm("v_fma_f32 %0, -%1, %2, %3" : "=v"(r) : "v"(a), "v"(b), "v"(c)); return r; }
DI float op(float x) { asm("" : "+v"(x)); return x; }
DI u32 opq(u32 x) { asm volatile("" : "+v"(x)); return x; }
#define PIN(x) asm volatile("" : "+v"(x))
DI f32x4 mfma16(u32x4 a, u32x4 b, f32x4 c) { return __builtin_amdgcn_mfma_f32_16x16x32_bf16(__builtin_bit_cast(s16x8, a), __builtin_bit_cast(s16x8, b), c, 0, 0, 0); }
DI f32x16 mfma32(u32x4 a, u32x4 b, f32x16 c) { return __builtin_amdgcn_mfma_f32_32x32x16_bf16(__builtin_bit_cast(s16x8, a), __builtin_bit_cast(s16x8, b), c, 0, 0, 0); }
DI float dot2(u32 a, u32 b, float c) { return __builtin_amdgcn_fdot2_f32_bf16(__builtin_bit_cast(bf2, a), __builtin_bit_cast(bf2, b), c, false); }
#define VMW(n) asm volatile("s_waitcnt vmcnt(" #n ")" ::: "memory")
#define LG0() asm volatile("s_waitcnt lgkmcnt(0)" ::: "memory")
#define SB() __builtin_amdgcn_sched_barrier(0)
DI void bar() { asm volatile("s_waitcnt lgkmcnt(0)\n\ts_barrier" ::: "memory"); }
#define RBAR() asm volatile("s_barrier" ::: "memory")
DI float wsum(float v) {
  v += __builtin_amdgcn_update_dpp(0.f, v, 0x111, 0xf, 0xf, true);
  v += __builtin_amdgcn_update_dpp(0.f, v, 0x112, 0xf, 0xf, true);
  v += __builtin_amdgcn_update_dpp(0.f, v, 0x114, 0xf, 0xf, true);
  v += __builtin_amdgcn_update_dpp(0.f, v, 0x118, 0xf, 0xf, true);
  v += __builtin_amdgcn_update_dpp(0.f, v, 0x142, 0xa, 0xf, true);
  v += __builtin_amdgcn_update_dpp(0.f, v, 0x143, 0xc, 0xf, true);
  return __uint_as_float(__builtin_amdgcn_readlane(__float_as_uint(v), 63));
}
DI float hsum(float v) {
  const auto r = __builtin_amdgcn_permlane32_swap(__float_as_uint(v), __float_as_uint(v), false, false);
  return __uint_as_float(r[0]) + __uint_as_float(r[1]);
}
template <int SLEEP = 1> DI void poll(char* ws, u32 off, bool act, u64 tag) {
  bool ok = !act;
#pragma unroll 1
  for (int it = 0; it < 20000; ++it) {
    u64 x = tag;
    if (!ok) x = __hip_atomic_load((GAS u64*)(ws + off), RLX_AGENT);
    ok = (x == tag);
    if (__all(ok)) break;
    __builtin_amdgcn_s_sleep(SLEEP);
  }
  asm volatile("" ::: "memory");
}
DI u32 max4(u32x4 v) { return max(max(v.x, v.y), max(v.z, v.w)); }
DI u64 flag_ld(u32x4 r, u32 voff) { u64 v; asm volatile("buffer_load_dwordx2 %0, %1, %2, 0 offen sc1" : "=v"(v) : "v"(voff), "s"(r) : "memory"); return v; }
DI u32x4 ldg(u32x4 r, u32 voff, u32 soff) { u32x4 v; asm volatile("buffer_load_dwordx4 %0, %1, %2, %3 offen sc1" : "=v"(v) : "v"(voff), "s"(r), "s"(soff) : "memory"); return v; }
template <int N> DI void vmw(u32x4& x) { asm volatile("s_waitcnt vmcnt(%1)" : "+v"(x) : "n"(N) : "memory"); }
#define VMWP(n, x) asm volatile("s_waitcnt vmcnt(" #n ")" : "+v"(x) :: "memory")
DI void setflag(char* ws, u32 off, u64 tag) { __hip_atomic_store((GAS u64*)(ws + off), tag, RLX_AGENT); }

#define STAMP(i) do { if ((i) == 0) t_0 = __builtin_amdgcn_s_memrealtime(); if ((i) == 1) t_j = __builtin_amdgcn_s_memrealtime(); } while (0)
#define STAMPC(i)
#define STAMP_OUT() do { if (b == 9 && w == 0) { const u64 te = __builtin_amdgcn_s_memrealtime() + 8 * (t_j - t_0); while (__builtin_amdgcn_s_memrealtime() < te) __builtin_amdgcn_s_sleep(1); } } while (0)

DI float xsum(float a, float c) {
  const auto r = __builtin_amdgcn_permlane32_swap(__float_as_uint(a), __float_as_uint(c), false, false);
  return __uint_as_float(r[0]) + __uint_as_float(r[1]);
}
DI f32x4 ln2_terms(float t) {
  const auto r = __builtin_amdgcn_permlane32_swap(__float_as_uint(t), __float_as_uint(t), false, false);
  const float mean = __uint_as_float(r[0]) * (1.f / D_);
  const float V = fmaxf(__uint_as_float(r[1]) * (1.f / D_) - mean * mean, 0.f) + 1e-5f;
  const float rs = __builtin_amdgcn_rsqf(V);
  const float K1 = -2.f * 0.7978845608028654f * 1.4426950408889634f * rs;
  const float lo = -mean - bflo(pk(-mean, 0.f));
  return f32x4{__uint_as_float(pk(-mean, lo)), V, K1, (K1 * rs) * (0.044715f * rs)};
}
template <int N> struct IC { static constexpr int value = N; };
template <int N, int I = 0, class F> DI void sfor(F&& f) { if constexpr (I < N) { f(IC<I>{}); sfor<N, I + 1>(f); } }
struct W1 { u32x4 rg, ru; u32 vg, vu, ra, lb; };
template <int T> DI void w1_g(const W1& c, u32 base) { dma(c.rg, c.lb + base, c.vg, T * (32 * F_ * 2)); }
template <int T> DI void w1_u(const W1& c, u32 base) { dma(c.ru, c.lb + base + 8192, c.vu, T * (32 * F_ * 2)); }
template <int T> DI void w1_issue(const W1& c, u32 base) { w1_g<T>(c, base); w1_u<T>(c, base); }
template <int T> DI void w1_read(const char* lds, u32 a, u32x4 (&W)[28]) {
#pragma unroll
  for (int kk = 0; kk < 2; ++kk) W[2 * T + kk] = cat(trd(lds, a + kk * 4096), trd(lds, a + kk * 4096 + 1024));
}
DI float x8sum(float v) {
  v += __builtin_amdgcn_update_dpp(0.f, v, 0xb1, 0xf, 0xf, true);
  v += __builtin_amdgcn_update_dpp(0.f, v, 0x4e, 0xf, 0xf, true);
  v += __builtin_amdgcn_update_dpp(0.f, v, 0x141, 0xf, 0xf, true);
  return v;
}

__global__ __launch_bounds__(512, 2) void h3_ffn(const char* __restrict__ gq, const char* __restrict__ gk, const char* __restrict__ gv,
                                                 const char* __restrict__ gsk, const char* __restrict__ gf, const char* __restrict__ gD,
                                                 const char* __restrict__ gwg, const char* __restrict__ gwu, const char* __restrict__ gwd,
                                                 char* __restrict__ gout, char* ws) {
  __shared__ __attribute__((aligned(16))) char lds[LDS_BYTES];
  const int tid = threadIdx.x, l = tid & 63, w = __builtin_amdgcn_readfirstlane(tid >> 6);
  const int b = blockIdx.x, xc = b & 7, sl = b >> 3;
  const int tb64 = 4 * xc + (sl >> 3), role = sl & 7;
  const int sc = (sl >> 3) * 7 + role;
  u64 t_0 = 0, t_j = 0;
  asm volatile("" :: "s"(gq), "s"(gk), "s"(gv), "s"(gsk), "s"(gf), "s"(gD), "s"(gwg), "s"(gwu), "s"(gwd), "s"(gout), "s"(ws));
  STAMP(0);
  const rsrc_t rw = mk(ws, WS_END);
  const int G = l >> 4, i16 = l & 15, q4 = i16 >> 2, p4 = l & 3, r8 = l >> 3;
  auto mktag = [&](u64 e0) AI { return ((u64)__builtin_amdgcn_readfirstlane((u32)e0) | ((u64)__builtin_amdgcn_readfirstlane((u32)(e0 >> 32)) << 32)) * 6364136223846793005ull + 1442695040888963407ull; };
  if (role == 7) {
    const rsrc_t rq = mk(gq, S_ * D_ * 2);
    const int row = 64 * tb64 + 8 * w + r8;
    u32x4 xs[7];
#pragma unroll
    for (int i = 0; i < 7; ++i) xs[i] = __builtin_amdgcn_raw_buffer_load_b128(rq, (u32)(row * 896 + (l & 7) * 16 + i * 128), 0, 0);
    const u64 e0 = __hip_atomic_load((GAS u64*)(ws + WS_E), RLX_AGENT);
    SB();
    SB();
    const u64 tag = mktag(e0);
    float s[4] = {};
#pragma unroll
    for (int i = 0; i < 7; ++i)
#pragma unroll
      for (int e = 0; e < 4; ++e) s[e] += bflo(xs[i][e]) + bfhi(xs[i][e]);
    const float mean = x8sum((s[0] + s[1]) + (s[2] + s[3])) * (1.f / D_);
    float v[4] = {};
#pragma unroll
    for (int i = 0; i < 7; ++i)
#pragma unroll
      for (int e = 0; e < 4; ++e) { const float d0 = bflo(xs[i][e]) - mean, d1 = bfhi(xs[i][e]) - mean; v[e] += d0 * d0; v[e] += d1 * d1; }
    const float rstd = __builtin_amdgcn_rsqf(x8sum((v[0] + v[1]) + (v[2] + v[3])) * (1.f / D_) + 1e-5f);
    const u32 so = WS_ST + (row >> 8) * 2048 + ((row >> 1) & 3) * 512 + ((row >> 5) & 7) * 64 + ((row >> 3) & 3) * 16 + (row & 1) * 8;
    __builtin_amdgcn_raw_buffer_store_b64(u32x2{__float_as_uint(mean), __float_as_uint(rstd)}, rw, (l & 7) == 0 ? so : OOB, 0, SC1);
    VMW(0); bar();
    if (tid == 0) setflag(ws, WS_F1 + (tb64 * 8 + 7) * 128, tag);
#pragma unroll
    for (int i = 0; i < 7; ++i) __builtin_amdgcn_raw_buffer_store_b128(u32x4{EMPTY, EMPTY, EMPTY, EMPTY}, rw, (u32)(tid * 16), WS_H + (u32)(tb64 * 2 * TB + i * 8192), SC1);
    __builtin_amdgcn_raw_buffer_store_b128(u32x4{EMPTY, EMPTY, EMPTY, EMPTY}, rw, tid < 64 ? WS_S2 + (u32)(tb64 * 1024 + tid * 16) : tid < 120 ? WS_CS + (u32)(tb64 * 896 + (tid - 64) * 16) : OOB, 0, SC1);
    VMW(0); bar();
    if (tid == 0) setflag(ws, WS_F2 + tb64 * 128, tag);
    STAMP(1);
    {
      const int mat = tb64 >= 14, j = tb64 < 28 ? tb64 - 14 * mat : 0;
      const rsrc_t rm = mk(mat ? gwu : gwg, D_ * F_ * 2);
      u32x4 wv[14];
#pragma unroll
      for (int i = 0; i < 14; ++i) wv[i] = __builtin_amdgcn_raw_buffer_load_b128(rm, tb64 < 28 ? (u32)(((4 * w + G + 32 * i) * F_ + 128 * j) * 2 + i16 * 16) : OOB, 0, 0);
      float c[8] = {};
#pragma unroll
      for (int i = 0; i < 14; ++i)
#pragma unroll
        for (int e = 0; e < 4; ++e) { c[2 * e] += bflo(wv[i][e]); c[2 * e + 1] += bfhi(wv[i][e]); }
      *(f32x4*)(lds + (4 * w + G) * 512 + i16 * 32) = f32x4{c[0], c[1], c[2], c[3]};
      *(f32x4*)(lds + (4 * w + G) * 512 + i16 * 32 + 16) = f32x4{c[4], c[5], c[6], c[7]};
      poll(ws, WS_F2 + (u32)(l & 31) * 128, true, tag);
      bar();
      if (tid < 128 && tb64 < 28) {
        float t = 0.f;
#pragma unroll
        for (int n = 0; n < 32; ++n) t += *(const float*)(lds + n * 512 + tid * 4);
        const u32 hi = pk(t, t), lo = pk(t - bflo(hi), 0.f);
        __builtin_amdgcn_raw_buffer_store_b64(u32x2{hi, lo}, rw, WS_CS + (u32)(((j * 8 + (tid >> 4)) * 32 + mat * 16 + (tid & 15)) * 8), 0, SC1);
      }
    }
    STAMP(2);
    const u32x4 dw = srd(ws, WS_END);
    const u32 lb = (u32)(size_t)(LAS char*)lds;
#pragma unroll 1
    for (int half = (tb64 & 1) ^ 1; half < 2; ++half) {
      const int tb = 32 * half + tb64;
#pragma unroll 1
      for (int it = 0; it < 4000; ++it) {
#pragma unroll
        for (int k = 0; k < 4; ++k) if (k < 3 || l < 32) dma<SC1>(dw, lb + 32768 + w * 3584 + k * 1024, l * 16, WS_H + (u32)(tb * TB + w * 3584 + k * 1024));
        VMW(0);
        u32 m = 0;
#pragma unroll
        for (int k = 0; k < 4; ++k) m = max(m, max4(*(const u32x4*)(lds + 32768 + w * 3584 + (k < 3 ? k * 1024 + l * 16 : 3072 + (l & 31) * 16))));
        if (!__any(m == EMPTY)) break;
        __builtin_amdgcn_s_sleep(6);
      }
      float s1 = 0.f, s2 = 0.f;
#pragma unroll
      for (int i = 0; i < 7; ++i) {
        const u32x2 t = trd(lds, 32768 + (u32)((56 * w + 4 * (G >> 1) + q4) * 64 + (G & 1) * 32 + p4 * 8) + i * 512);
        s1 = dot2(t.x, ONES, s1); s2 = dot2(t.x, t.x, s2); s1 = dot2(t.y, ONES, s1); s2 = dot2(t.y, t.y, s2);
      }
      *(float*)(lds + 20480 + half * 2048 + l * 32 + w * 4) = xsum(s1, s2);
      bar();
      if (w == 0) {
        const f32x4 a = *(const f32x4*)(lds + 20480 + half * 2048 + l * 32), c = *(const f32x4*)(lds + 20480 + half * 2048 + l * 32 + 16);
        __builtin_amdgcn_raw_buffer_store_b128(__builtin_bit_cast(u32x4, ln2_terms(((a.x + a.y) + (a.z + a.w)) + ((c.x + c.y) + (c.z + c.w)))), rw, l < 32 ? WS_S2 + (u32)(tb * 512 + l * 16) : OOB, 0, SC1);
      }
    }
    STAMP(3);
    STAMP_OUT();
    return;
  }
  const int cp = xc * 28 + sc, cch = w & 1, vp = w >> 1;
  u32x4 fhi; u32x2 flo; u32 Dh; u64 e0;
  const u32 lb = (u32)(size_t)(LAS char*)lds;
  const u32x4 dw = srd(ws, WS_END);
  {
    const u32x4 rk = srd(gk, S_ * D_ * 2), rv = srd(gv, S_ * D_ * 2);
    const int c8 = l & 7, s0 = 64 * tb64;
    const int swv = ((r8 >> 1) & 1) | ((w & 1) << 1), swk = swv ^ 2;
    const u32 g0 = (u32)((s0 + 8 * w + r8) * 896 + role * 128);
    if (w == 0) dma(rk, lb + L_K, g0 - 8 * 896 + ((c8 ^ (swv << 1)) << 4), 0);
    if (w == 1) dma(srd(gsk, D_ * 8), lb + L_SK, (u32)(512 * role + 16 * l), 0);
    dma(rk, lb + L_K + 1024 + w * 1024, g0 + ((c8 ^ (swk << 1)) << 4), 0);
    dma(rv, lb + L_V + w * 1024, g0 + ((c8 ^ (swv << 1)) << 4), 0);
    dma(srd(gq, S_ * D_ * 2), lb + L_X + w * 1024, g0 + ((c8 ^ (swv << 1)) << 4), 0);
    const rsrc_t rf = mk(gf, S_ * D_ * 2);
    const int fq = vp * 64 + l;
    const u32 fo = (u32)((2 * cp + cch) * 4096 + 16 * fq);
    fhi = __builtin_amdgcn_raw_buffer_load_b128(rf, fo, 0, 0);
    flo = __builtin_amdgcn_raw_buffer_load_b64(rf, fq > 0 ? fo - 8 : OOB, 0, 0);
    Dh = __builtin_amdgcn_raw_buffer_load_b16(mk(gD, D_ * 2), (u32)((2 * cp + ((l >> 2) & 1)) * 2), 0, 0);
    e0 = __hip_atomic_load((GAS u64*)(ws + WS_E), RLX_AGENT);
    SB();
  }
  const int gi = 4 * (xc >> 1) + sc / 7, gj = 7 * (xc & 1) + sc % 7;
  W1 w1;
  {
    const u32 v0 = (u32)(((4 * w + G) * F_ + 128 * gj) * 2);
    w1.rg = srd(gwg, D_ * F_ * 2); w1.ru = srd(gwu, D_ * F_ * 2); w1.lb = lb + w * 1024;
    w1.vg = v0 + ((i16 ^ (4 * G)) << 4); w1.vu = v0 + ((i16 ^ (4 * G + 2)) << 4);
    w1.ra = (u32)((G & 1) * 8192 + (8 * (G >> 1) + q4) * 256 + ((w ^ (2 * q4 + (G & 1))) * 32) + p4 * 8);
  }
  VMW(5); bar();
  STAMP(1);
  {
    const int tbl = w >> 2, chg = w & 3;
    const u32 rb = (u32)((32 * tbl + 8 * G + q4) * 128 + p4 * 8);
    const u32 sv = (u32)((chg ^ ((q4 >> 1) | ((G & 1) << 1))) * 32), sk2 = sv ^ 64;
    const u32x2 km = trd(lds, L_K + rb + 512 + sv), k0 = trd(lds, L_K + rb + 1024 + sk2), k1 = trd(lds, L_K + rb + 1536 + sk2);
    const u32x2 v0 = trd(lds, L_V + rb + sv), v1 = trd(lds, L_V + rb + 512 + sv);
    const u32x2 skv = *(const u32x2*)(lds + L_SK + (16 * chg + i16) * 8);
    const float kk[11] = {bfhi(km.x), bflo(km.y), bfhi(km.y), bflo(k0.x), bfhi(k0.x), bflo(k0.y), bfhi(k0.y), bflo(k1.x), bfhi(k1.x), bflo(k1.y), bfhi(k1.y)};
    const float vv[8] = {bflo(v0.x), bfhi(v0.x), bflo(v0.y), bfhi(v0.y), bflo(v1.x), bfhi(v1.x), bflo(v1.y), bfhi(v1.y)};
    const float s0 = bflo(skv.x), s1 = bfhi(skv.x), s2 = bflo(skv.y), s3 = bfhi(skv.y);
    float u[8];
#pragma unroll
    for (int e = 0; e < 8; ++e) u[e] = (kk[e + 3] * s0 + kk[e + 2] * s1 + kk[e + 1] * s2 + kk[e] * s3) * vv[e];
    const u32 o = (u32)((2 * tb64 + tbl) * TB + (64 * role + 16 * chg + i16) * 64 + G * 16);
    __builtin_amdgcn_raw_buffer_store_b128(u32x4{pk(u[0], u[1]), pk(u[2], u[3]), pk(u[4], u[5]), pk(u[6], u[7])}, rw, WS_U + o, 0, SC1);
    SB();
    STAMP(2);
    VMW(5); bar();
    const u32x2 x0 = trd(lds, L_X + rb + sv), x1 = trd(lds, L_X + rb + 512 + sv);
    __builtin_amdgcn_raw_buffer_store_b128(cat(x0, x1), rw, WS_X + o, 0, SC1);
    PIN(fhi); PIN(flo); PIN(Dh); PIN(e0);
    SB();
  }
  STAMP(3);
  {
    const u32 a0 = __builtin_amdgcn_alignbit(flo.y, flo.x, 16), a1 = __builtin_amdgcn_alignbit(fhi.x, flo.y, 16), a2 = __builtin_amdgcn_alignbit(fhi.y, fhi.x, 16),
              a3 = __builtin_amdgcn_alignbit(fhi.z, fhi.y, 16), a4 = __builtin_amdgcn_alignbit(fhi.w, fhi.z, 16);
    char* fc = lds + L_FC + cch * FCH + (vp * 64 + l) * 16 + 32;
    *(u32x4*)(fc) = fhi;
    *(u32x4*)(fc + FCS) = u32x4{a1, a2, a3, a4};
    *(u32x4*)(fc + 2 * FCS) = u32x4{flo.y, fhi.x, fhi.y, fhi.z};
    *(u32x4*)(fc + 3 * FCS) = u32x4{a0, a1, a2, a3};
    if (tid >= 128 && tid < 144) *(u32x4*)(lds + L_FC + (tid & 7) * FCS + ((tid >> 3) & 1) * 16) = u32x4{0, 0, 0, 0};
    if (tid >= 192 && tid < 252) { const int z = tid - 192; *(u32x4*)(lds + L_U + (z / 30) * UCH + (z % 30) * 16) = u32x4{0, 0, 0, 0}; }
  }
  const u64 tag = mktag(e0);
  const float Dv = bflo(Dh);
  SB();
  w1_issue<0>(w1, L_RING); w1_issue<1>(w1, L_RING + 16384);
  STAMP(4);
  VMW(4); bar();
  if (tid == 0) setflag(ws, WS_F1 + (tb64 * 8 + role) * 128, tag);
  STAMP(5);
  const int hh = l >> 5, tk = l & 31;
  const u32 gso = WS_G + (u32)(((2 * gi * 2 + (tk >> 4)) * 56 + 4 * gj + (w >> 1)) * 1024 + (2 * (w & 1) + hh) * 256 + i16 * 16);
  auto gofs = [](int g) AI { return (u32)((32 * (g >> 1) + (g & 1)) * 2 * 56 * 1024); };
  u32x4 W[28];
  w1_issue<2>(w1, L_RING + 32768); w1_issue<3>(w1, L_RING + 49152); w1_issue<4>(w1, L_RING + 65536); w1_issue<5>(w1, L_RED); w1_issue<6>(w1, L_RED + 16384);
  SB();

  const int ech = (l >> 2) & 1;
  const u32 eo = (u32)((8 * w + r8) * TB + (2 * cp + ech) * 64 + p4 * 16);
  STAMP(6);
  poll(ws, WS_F1 + (u32)(32 * w + 8 * (l & 3) + ((l & 4) ? 7 : (cp >> 5))) * 128, l < 8, tag);
  STAMP(7);
  u32x4 uu = ldg(dw, eo, WS_U), ex = ldg(dw, eo, WS_X);
  u32x4 est[4];
#pragma unroll
  for (int i = 0; i < 4; ++i) est[i] = ldg(dw, (u32)(r8 * 64 + p4 * 16), WS_ST + (u32)(w * 2048 + i * 512));
  u64 fl = flag_ld(dw, WS_F2 + (u32)(4 * w + (l & 3)) * 128);
  bar();
  const u32 ra0 = opq(w1.ra), ra1 = opq(w1.ra + L_RING), ra2 = opq(w1.ra + L_RING + 49152);
  w1_read<5>(lds, ra0 + L_RED, W); w1_read<6>(lds, ra0 + L_RED + 16384, W);
  w1_read<0>(lds, ra1, W); w1_read<1>(lds, ra1 + 16384, W); w1_read<2>(lds, ra1 + 32768, W); w1_read<3>(lds, ra2, W); w1_read<4>(lds, ra2 + 16384, W);
  const u32 aA = opq(L_FC + cch * FCH + q4 * FCS + 64 - 16 * G + 8 * p4 - 8 + 128 * vp);
  const u32 aB = opq(L_U + cch * UCH + 32 * i16 + 16 * G + 448 - 128 * vp - 64);
  const u32 rwr = opq(L_RED + w * 4096 + G * 256 + ((i16 ^ ((G >> 1) << 3)) << 4));
  const u32 rr = opq(L_RED + ech * 4096 + (w & 3) * 1024 + (p4 & 1) * 512 + (((2 * r8 + (p4 >> 1)) ^ ((p4 & 1) << 3)) << 4));
  SB();
  vmw<6>(uu);
  *(u32x4*)(lds + L_U + ech * UCH + 480 + (8 * w + r8) * 64 + p4 * 16) = uu;
  bar();
  STAMP(8);
  auto rdA = [&](int v, int m) AI { const u32 o = 64 * v + 512 * m; return cat(trd(lds, aA + o + 8), trd(lds, aA + o)); };
  auto rdB = [&](int v, int m) AI { return *(const u32x4*)(lds + aB + 64 * (1 - v) + 512 * m); };
  float ex8[8], eq8[8], eu8[8];
  auto terms = [&]() AI {
#pragma unroll
    for (int i = 0; i < 4; ++i) {
      ex8[2 * i] = bflo(ex[i]); ex8[2 * i + 1] = bfhi(ex[i]);
      eu8[2 * i] = bflo(uu[i]) * Dv; eu8[2 * i + 1] = op(bfhi(uu[i]) * Dv);
      eq8[2 * i] = (ex8[2 * i] - __uint_as_float(est[i].x)) * __uint_as_float(est[i].y);
      eq8[2 * i + 1] = op(op(ex8[2 * i + 1] - __uint_as_float(est[i].z)) * __uint_as_float(est[i].w));
    }
#pragma unroll
    for (int i = 0; i < 8; ++i) { PIN(ex8[i]); PIN(eq8[i]); PIN(eu8[i]); }
  };
  auto epilogue = [&]() AI {
    f32x4 p[8];
#pragma unroll
    for (int n = 0; n < 4; ++n) { p[2 * n] = *(const f32x4*)(lds + rr + n * 8192); p[2 * n + 1] = *(const f32x4*)(lds + rr + n * 8192 + 256); }
    float h[8];
#pragma unroll
    for (int i = 0; i < 8; ++i) {
      const float y = op(op(eu8[i] + p[i >> 2][i & 3]) + p[2 + (i >> 2)][i & 3]) + op(p[4 + (i >> 2)][i & 3] + p[6 + (i >> 2)][i & 3]);
      h[i] = op(ex8[i] + eq8[i] * op(y));
    }
    __builtin_amdgcn_raw_buffer_store_b128(u32x4{pk(h[0], h[1]), pk(h[2], h[3]), pk(h[4], h[5]), pk(h[6], h[7])}, rw, WS_H + eo, 0, SC1);
  };
  {
    f32x4 acc[4];
    u32x4 A[2][4], B[2][4];
#pragma unroll
    for (int v = 0; v < 2; ++v) {
      A[v][0] = rdA(v, 0); SB();
#pragma unroll
      for (int m = 0; m < 4; ++m) { B[v][m] = rdB(v, m); SB(); }
#pragma unroll
      for (int m = 1; m < 4; ++m) { A[v][m] = rdA(v, m); SB(); }
      if (v == 0) {
        vmw<0>(ex); PIN(est[0]); PIN(est[1]); PIN(est[2]); PIN(est[3]); PIN(fl);
        if (!__all(fl == tag)) poll<2>(ws, WS_F2 + (u32)(4 * w + (l & 3)) * 128, true, tag);
        if (w < 4) terms();
        SB();
      }
    }
#pragma unroll
    for (int v = 0; v < 2; ++v)
#pragma unroll
      for (int m = 0; m < 4; ++m) {
#pragma unroll
        for (int c = m; c < 4; ++c) acc[c] = mfma16(A[v][m], B[v][c - m], (v == 0 && m == 0) ? f32x4{} : acc[c]);
        SB();
        if (v == 0 && m == 0) w1_g<7>(w1, L_RING);
        if (v == 0 && m == 2) w1_u<7>(w1, L_RING);
        if (v == 1 && m == 0) w1_g<8>(w1, L_RING + 16384);
        SB();
      }
#pragma unroll
    for (int c = 0; c < 4; ++c) *(f32x4*)(lds + rwr + c * 1024) = acc[c];
  }
  bar();
  STAMP(9);
  if (w < 4) epilogue();
  SB();
  STAMP(10);
  {
    f32x4 acc[4];
    u32x4 A[2][8], B[2][8];
    auto rd = [&](int v, int m) AI {
      if (m == 0) {
        A[v][0] = rdA(v, 0);
#pragma unroll
        for (int d = 4; d < 8; ++d) B[v][d] = rdB(v, d);
      } else { A[v][m] = rdA(v, m); if (m <= 4) B[v][4 - m] = rdB(v, 4 - m); }
      SB();
    };
    rd(0, 0); rd(0, 1); rd(0, 2); rd(0, 3);
#pragma unroll
    for (int v = 0; v < 2; ++v) {
#pragma unroll
      for (int m = 0; m < 8; ++m) {
#pragma unroll
        for (int c = 4; c < 8; ++c) if (m <= c) acc[c - 4] = mfma16(A[v][m], B[v][c - m], (v == 0 && m == 0) ? f32x4{} : acc[c - 4]);
        SB();
        if (m < 4) rd(v, m + 4); else if (v == 0) rd(1, m - 4);
        if (v == 0) {
          if (m == 0) w1_u<8>(w1, L_RING + 16384);
          if (m == 1) w1_g<9>(w1, L_RING + 32768);
          if (m == 2) w1_u<9>(w1, L_RING + 32768);
          if (m == 3) w1_g<10>(w1, L_RING + 49152);
          if (m == 4) w1_u<10>(w1, L_RING + 49152);
          if (m == 6) w1_g<11>(w1, L_RING + 65536);
          if (m == 7) w1_u<11>(w1, L_RING + 65536);
        }
        SB();
      }
    }
    if (w >= 4) terms();
    STAMP(11);
    VMW(0); bar();
#pragma unroll
    for (int c = 0; c < 4; ++c) *(f32x4*)(lds + rwr + c * 1024) = acc[c];
  }
  const u32 lo16 = l * 16;
  if (w < 4) {
#pragma unroll
    for (int k = 0; k < 7; ++k) dma<SC1>(dw, lb + L_H0 + (7 * w + k) * 1024, lo16, WS_H + (u32)(2 * gi * TB + (7 * w + k) * 1024));
  }
  w1_read<7>(lds, ra1, W); w1_read<8>(lds, ra1 + 16384, W); w1_read<9>(lds, ra1 + 32768, W); w1_read<10>(lds, ra2, W); w1_read<11>(lds, ra2 + 16384, W);
  bar();
  STAMP(12);
  if (w >= 4) {
    epilogue();
    SB();
#pragma unroll
    for (int i = 0; i < 2; ++i) {
      W1 c = w1; c.lb -= i * 4096; c.vg -= i * (16 * F_ * 2); c.vu -= i * (16 * F_ * 2);
      w1_issue<12>(c, L_WE); w1_issue<13>(c, L_WE + 16384);
    }
  } else {
    const u32 ha = opq(L_H0 + 7 * w * 1024 + lo16);
#pragma unroll 1
    for (int it = 0; it < 4000; ++it) {
      VMW(0);
      u32 m = 0;
#pragma unroll
      for (int k = 0; k < 7; ++k) { const u32x4 v = *(const u32x4*)(lds + ha + k * 1024); m = max(m, max4(v)); }
      if (!__any(m == EMPTY)) break;
#pragma unroll
      for (int k = 0; k < 7; ++k) dma<SC1>(dw, lb + L_H0 + (7 * w + k) * 1024, lo16, WS_H + (u32)(2 * gi * TB + (7 * w + k) * 1024));
    }
    VMW(0);
    STAMP(13);
  }
  bar();
  STAMP(14);

  u64 csv;
  const u32 bta = (u32)((8 * (G >> 1) + q4) * 64 + (G & 1) * 32 + p4 * 8);
  const u32 sta = (u32)((56 * w + 4 * (G >> 1) + q4) * 64 + (G & 1) * 32 + p4 * 8);
  auto hbase = [](int g) AI { return g == 0 ? L_H0 : g == 1 ? L_H1 : g == 2 ? L_H2 : L_H3; };
  const u32 spa = opq(L_SP + l * 32);
  float s1a, s1b, s2a, s2b, xs; u32x2 st[2];
  auto stats_step = [&](u32 b0, int k) AI {
    if (k > 0) {
      if (k & 1) { s1a = k == 1 ? xs : s1a + xs; s2a = k == 1 ? xs * xs : s2a + xs * xs; PIN(s1a); PIN(s2a); }
      else       { s1b = k == 2 ? xs : s1b + xs; s2b = k == 2 ? xs * xs : s2b + xs * xs; PIN(s1b); PIN(s2b); }
    }
    if (k < 28) {
      const int u = k >> 2;
      const u32 d = (k & 2) ? st[u & 1].y : st[u & 1].x;
      xs = (k & 1) ? bfhi(d) : bflo(d); PIN(xs);
      if ((k & 3) == 3 && u + 2 < 7) st[u & 1] = trd(lds, b0 + (u + 2) * 512);
    } else *(float*)(lds + spa + w * 4) = xsum(s1a + s1b, s2a + s2b);
  };
  f32x4 sp0, sp1, tm; float gu[8], gt[8]; u32 od[4];
  auto center = [&](f32x16& acc, float m) AI {
    const u32 d = __float_as_uint(m);
    acc = mfma32(u32x4{(u32)csv, (u32)(csv >> 32), 0, 0}, u32x4{d, d & 0xffffu, 0, 0}, acc);
  };
  auto geglu_step = [&](int g, f32x16& acc, int i) AI {
    if (g == 0) {
      if (i == 1) { sp0 = *(const f32x4*)(lds + spa); sp1 = *(const f32x4*)(lds + spa + 16); }
      if (i == 3) { sp0.x = op(sp0.x + sp0.y); sp0.z = op(sp0.z + sp0.w); sp1.x = op(sp1.x + sp1.y); sp1.z = op(sp1.z + sp1.w); PIN(sp0); PIN(sp1); }
      if (i == 4) { tm = ln2_terms((sp0.x + sp0.z) + (sp1.x + sp1.z)); PIN(tm); }
      if (i == 5) center(acc, tm.x);
    }
#pragma unroll
    for (int e = 0; e < 8; ++e) {
      const int s = i - (g == 0 ? 7 + (13 * e) / 7 : 2 + (18 * e) / 7);
      if (s == 0) { gt[e] = acc[e] * acc[e]; gu[e] = op(acc[e] * acc[e + 8]); PIN(gt[e]); PIN(gu[e]); }
      if (s == 1) { gt[e] = tm.w * gt[e] + tm.z; PIN(gt[e]); }
      if (s == 2) { gt[e] = acc[e] * gt[e]; PIN(gt[e]); }
      if (s == 3) { gt[e] = __builtin_amdgcn_exp2f(gt[e]); PIN(gt[e]); }
      if (s == 4) { gt[e] = gt[e] * tm.y + tm.y; PIN(gt[e]); }
      if (s == 5) { gt[e] = __builtin_amdgcn_rcpf(gt[e]); PIN(gt[e]); }
      if (s == 6) { gu[e] = gu[e] * gt[e]; PIN(gu[e]); }
      if (s == 7 && (e & 1)) { od[e >> 1] = pk(gu[e - 1], gu[e]); PIN(od[e >> 1]); }
    }
    if (i == 28) {
      __builtin_amdgcn_raw_buffer_store_b128(u32x4{od[0], od[1], od[2], od[3]}, rw, gso, gofs(g), SC1);
    }
  };
  constexpr int PF = 4;
  u32x4 B[PF];
  u32 ba = opq(hbase(0) + bta);
  auto chain = [&](int g, f32x16& acc, auto side) AI {
    const u32 bn = opq(hbase(g < 3 ? g + 1 : g) + bta);
    sfor<28>([&](auto kc) AI {
      constexpr int k = decltype(kc)::value;
      acc = mfma32(W[k], B[k % PF], k == 0 ? f32x16{} : acc);
      SB();
      side(k);
      if (k + PF < 28) B[k % PF] = cat(trd(lds, ba + (k + PF) * 1024), trd(lds, ba + (k + PF) * 1024 + 256));
      else if (g < 3) B[k % PF] = cat(trd(lds, bn + (k + PF - 28) * 1024), trd(lds, bn + (k + PF - 28) * 1024 + 256));
      SB();
    });
    ba = bn;
  };
  auto flag3 = [&](int g) AI { setflag(ws, WS_F3 + (u32)(((gi * 4 + g) * 14) + gj) * 128, tag); };
  const int ri = 2 * xc + sc / 14, rq2 = (sc / 7) & 1, n2 = sc % 7;
  const u32x4 rd = srd(gwd, D_ * F_ * 2);
  const u32 wdv = (u32)(((224 * w + r8) * D_ + 64 * n2) * 2 + (((l & 7) ^ (((r8 >> 1) & 3) << 1)) << 4));
  const u32 blkR = w < 7 ? L_R + w * 4096 : L_R2;
  u32 wda[4];
#pragma unroll
  for (int nt = 0; nt < 4; ++nt) wda[nt] = (16 * (G >> 1) + 4 * (G & 1) + q4) * 128 + ((nt ^ (2 * (G & 1) + (q4 >> 1))) * 32) + p4 * 8;
  u32x4 Wd[7][4];
  auto wd_blk = [&](int k) AI { return k % 3 == 1 ? blkR : (k % 3 == 2 ? L_Q : L_P) + w * 4096; };
  auto wd_dma = [&](int k, int i) AI { dma(rd, lb + wd_blk(k) + i * 1024, wdv, (4 * k + i) * (8 * D_ * 2)); };
  auto wd_read = [&](int k) AI {
#pragma unroll
    for (int nt = 0; nt < 4; ++nt) { const u32 a = opq(wda[nt] + wd_blk(k)); Wd[k][nt] = cat(trd(lds, a), trd(lds, a + 1024)); }
  };
  const int j0 = (14 * w) >> 3, jn = ((14 * w + 13) >> 3) - j0;
  const u32 f3o = WS_F3 + (u32)(ri * 56 + j0 + ((l & 3) < jn ? (l & 3) : jn)) * 128;

  __builtin_amdgcn_s_setprio(1);
  f32x16 accA, accB;
  u32x4 tm1, tm2, tm3;
  const u32 s2o = WS_S2 + (u32)(2 * gi * 512 + tk * 16);
  auto h_dma = [&](int g, int k) AI {
    if (k < 3 || l < 32) dma<SC1>(dw, lb + hbase(g) + w * 3584 + k * 1024, lo16, WS_H + (u32)((32 * (g >> 1) + 2 * gi + (g & 1)) * TB + w * 3584 + k * 1024));
  };
  const u32 cso = l < 32 ? WS_CS + (u32)(((gj * 8 + w) * 32 + l) * 8) : OOB;
  u32x4 cv[4];
  auto h_rd = [&](int g) AI {
    const u32 a = opq(hbase(g) + w * 3584 + lo16), a3 = opq(hbase(g) + w * 3584 + 3072 + tk * 16);
    cv[0] = *(const u32x4*)(lds + a); cv[1] = *(const u32x4*)(lds + a + 1024); cv[2] = *(const u32x4*)(lds + a + 2048); cv[3] = *(const u32x4*)(lds + a3);
  };
  auto h_fix = [&](int g) AI {
    u32 cm = g == 1 ? max((u32)csv, (u32)(csv >> 32)) : g == 2 ? max4(tm1) : max(max4(tm2), max4(tm3));
    cm = max(max(cm, max(max4(cv[0]), max4(cv[1]))), max(max4(cv[2]), max4(cv[3])));
#pragma unroll 1
    for (int it = 0; it < 4000 && __any(cm == EMPTY); ++it) {
      h_dma(g, 0); h_dma(g, 1); h_dma(g, 2); h_dma(g, 3);
      if (g == 1) { csv = flag_ld(dw, cso); VMWP(0, csv); }
      if (g == 2) { tm1 = ldg(dw, s2o, 512); vmw<0>(tm1); }
      if (g == 3) { tm2 = ldg(dw, s2o, 32 * 512); tm3 = ldg(dw, s2o, 33 * 512); vmw<0>(tm2); PIN(tm3); }
      h_rd(g);
      cm = g == 1 ? max((u32)csv, (u32)(csv >> 32)) : g == 2 ? max4(tm1) : max(max4(tm2), max4(tm3));
      cm = max(max(cm, max(max4(cv[0]), max4(cv[1]))), max(max4(cv[2]), max4(cv[3])));
    }
  };
  SB();
  {
    const u32 b0 = opq(hbase(0) + sta);
#pragma unroll
    for (int k = 0; k < PF; ++k) B[k] = cat(trd(lds, ba + k * 1024), trd(lds, ba + k * 1024 + 256));
    st[0] = trd(lds, b0); st[1] = trd(lds, b0 + 512);
    SB();
    chain(0, accA, [&](int k) AI {
      stats_step(b0, k);
      if (k <= 6 && !(k & 1)) h_dma(1, k >> 1);
      if (k == 7) csv = flag_ld(dw, cso);
      if (k == 10 || k == 13) __builtin_amdgcn_raw_buffer_store_b128(u32x4{EMPTY, EMPTY, EMPTY, EMPTY}, rw, gso, gofs(k == 10 ? 2 : 3), SC1);
      if (k == 14) { VMW(7); RBAR(); const u32 ra3 = opq(w1.ra + L_WE); w1_read<12>(lds, ra3, W); w1_read<13>(lds, ra3 + 16384, W); }
      if (k == 19) { VMWP(2, csv); h_rd(1); }
      if (k == 22) { h_fix(1); RBAR(); }
      if (k >= 23 && (k & 1)) h_dma(2, (k - 23) >> 1);
    });
    stats_step(b0, 28);
  }
  STAMP(16);
  chain(1, accB, [&](int k) AI {
    if (k == 1) { bar(); if (tid == 0 && b == 0) setflag(ws, WS_E, tag); h_dma(2, 3); }
    geglu_step(0, accA, k);
    if (k == 3) tm1 = ldg(dw, s2o, 512);
    if (k >= 5 && k <= 14 && k % 3 == 2) wd_dma(0, (k - 5) / 3);
    if (k >= 16 && k <= 25 && k % 3 == 1) h_dma(3, (k - 16) / 3);
    if (k == 20) { vmw<6>(tm1); h_rd(2); }
    if (k == 23) { h_fix(2); RBAR(); }
  });
  center(accB, __uint_as_float(tm1.x));
  geglu_step(0, accA, 28);
  tm = __builtin_bit_cast(f32x4, tm1);
  SB();
  STAMP(18);
  chain(2, accA, [&](int k) AI {
    if (k == 1) RBAR();
    geglu_step(1, accB, k);
    if (k <= 6 && !(k & 1)) wd_dma(1, k >> 1);
    if (k == 3) { VMW(7); wd_read(0); }
    if (k == 8) { tm2 = ldg(dw, s2o, 32 * 512); tm3 = ldg(dw, s2o, 33 * 512); }
    if (k >= 10 && k <= 24 && !(k & 1)) wd_dma(2 + ((k - 10) >> 3), ((k - 10) >> 1) & 3);
    if (k == 19) { VMW(11); h_rd(3); }
    if (k == 23) { vmw<7>(tm2); PIN(tm3); h_fix(3); RBAR(); if (tid == 0) flag3(0); }
  });
  center(accA, __uint_as_float(tm2.x));
  geglu_step(1, accB, 28);
  tm = __builtin_bit_cast(f32x4, tm2);
  SB();
  STAMP(20);
  chain(3, accB, [&](int k) AI {
    geglu_step(2, accA, k);
    if (k == 12) fl = flag_ld(dw, f3o);
    if (k == 15) { VMW(1); RBAR(); if (tid == 0) flag3(1); }
    if (k >= 15 && k < 18) wd_read(k - 14);
    if (k == 19) LG0();
    if (k >= 19 && k < 27 && (k & 1)) wd_dma(4, (k - 19) >> 1);
  });
  center(accB, __uint_as_float(tm3.x));
  geglu_step(2, accA, 28);
  tm = __builtin_bit_cast(f32x4, tm3);
  STAMP(21);
#pragma unroll
  for (int e = 0; e < 8; ++e) {
    const float a = accB[e], u = accB[e + 8];
    gu[e] = (a * u) * __builtin_amdgcn_rcpf(__builtin_amdgcn_exp2f(a * (tm.w * (a * a) + tm.z)) * tm.y + tm.y);
  }
#pragma unroll
  for (int e = 0; e < 4; ++e) od[e] = pk(gu[2 * e], gu[2 * e + 1]);
  geglu_step(3, accB, 28);
  __builtin_amdgcn_s_setprio(0);
  SB();
  STAMP(22);
  const u32 gb = WS_G + (u32)((2 * ri * 2 + rq2) * 56 + 7 * w) * 1024;
  u32x4 Hf[3][7];
  auto h_load = [&](int g, int k0, int k1) AI {
#pragma unroll
    for (int k = k0; k < k1; ++k) Hf[g % 3][k] = ldg(dw, lo16, gb + gofs(g) + k * 1024);
  };
  const rsrc_t ro = mk(gout, S_ * D_ * 2);
  const u32 pw = (u32)(i16 * 256), tf = 4 * (w & 3) + G;
  f32x4 acc[4];
  u32x2 res[4];
  auto step = [&](auto gc, auto kc, auto nc) AI {
    constexpr int g = decltype(gc)::value, k = decltype(kc)::value;
    vmw<decltype(nc)::value>(Hf[g % 3][k]);
#pragma unroll
    for (int nt = 0; nt < 4; ++nt) acc[nt] = mfma16(Wd[k][nt], Hf[g % 3][k], k == 0 ? f32x4{} : acc[nt]);
    SB();
  };
  auto put = [&](int g) AI {
    const u32 rb = opq(((g & 1) ? L_Q : L_P) + w * 4096 + pw);
#pragma unroll
    for (int nt = 0; nt < 4; ++nt) *(f32x4*)(lds + rb + (((4 * nt + G) ^ (i16 & 7)) << 4)) = acc[nt];
    *(f32x4*)(lds + L_RES + (g & 1) * 4096 + pw + (((2 * w + (G & 1)) ^ (i16 & 7)) << 4)) = f32x4{bflo(res[g].x), bfhi(res[g].x), bflo(res[g].y), bfhi(res[g].y)};
  };
  auto fin = [&](int g) AI {
    if ((w >> 2) == (g & 1)) {
      const u32 a0 = tf * 256 + ((i16 ^ (tf & 7)) << 4), a = opq(((g & 1) ? L_Q : L_P) + a0);
      f32x4 sum = *(const f32x4*)(lds + L_RES + (g & 1) * 4096 + a0);
#pragma unroll
      for (int n = 0; n < 8; ++n) sum += *(const f32x4*)(lds + a + n * 4096);
      const int tok = 32 * (32 * (g >> 1) + (g & 1) + 2 * ri) + 16 * rq2 + tf;
      __builtin_amdgcn_raw_buffer_store_b64(u32x2{pk(sum.x, sum.y), pk(sum.z, sum.w)}, ro, (u32)((tok * D_ + 64 * n2 + 4 * i16) * 2), 0, 0);
    }
  };
  u32 chk;
  auto stepc = [&](auto gc, auto kc, auto nc) AI {
    constexpr int g = decltype(gc)::value, k = decltype(kc)::value;
    step(gc, kc, nc);
    const u32x4 v = Hf[g % 3][k];
    chk = k == 0 ? max4(v) : max(chk, max4(v));
    PIN(chk);
  };
  auto redo = [&](auto gc) AI {
    constexpr int g = decltype(gc)::value;
#pragma unroll 1
    for (int it = 0; it < 4000 && __any(chk == EMPTY); ++it) {
      h_load(g, 0, 7);
      stepc(gc, IC<0>{}, IC<6>{}); stepc(gc, IC<1>{}, IC<5>{}); stepc(gc, IC<2>{}, IC<4>{}); stepc(gc, IC<3>{}, IC<3>{});
      stepc(gc, IC<4>{}, IC<2>{}); stepc(gc, IC<5>{}, IC<1>{}); stepc(gc, IC<6>{}, IC<0>{});
    }
  };
  VMWP(6, fl);
  if (!__all(fl == tag)) poll(ws, f3o, true, tag);
  wd_dma(5, 0); wd_dma(5, 1); wd_dma(5, 2); wd_dma(5, 3);
  h_load(0, 0, 3);
  fl = flag_ld(dw, f3o + 14 * 128);
  h_load(0, 3, 7);
  const u32 hro = WS_H + (u32)((32 * (G >> 1) + (G & 1) + 2 * ri) * TB + (64 * n2 + 8 * w + (i16 >> 1)) * 64 + rq2 * 32 + (i16 & 1) * 16);
  u32x4 hr = ldg(dw, hro, 0);
  wd_dma(6, 0); wd_dma(6, 1); wd_dma(6, 2); wd_dma(6, 3);
  VMWP(9, fl);
  if (!__all(fl == tag)) poll(ws, f3o + 14 * 128, true, tag);
  wd_read(4); wd_read(5);
  SB();
  STAMP(23);
  h_load(1, 0, 1); step(IC<0>{}, IC<0>{}, IC<10>{});
  h_load(1, 1, 2); step(IC<0>{}, IC<1>{}, IC<11>{});
  h_load(1, 2, 3); step(IC<0>{}, IC<2>{}, IC<12>{});
  h_load(1, 3, 4); step(IC<0>{}, IC<3>{}, IC<12>{});
  h_load(1, 4, 5); step(IC<0>{}, IC<4>{}, IC<12>{});
  h_load(1, 5, 6); step(IC<0>{}, IC<5>{}, IC<12>{});
  h_load(1, 6, 7);
  vmw<7>(hr);
#pragma unroll 1
  for (int it = 0; it < 4000 && __any(max4(hr) == EMPTY); ++it) { hr = ldg(dw, hro, 0); vmw<0>(hr); }
  wd_read(6);
  *(u32x4*)(lds + blkR + l * 16) = hr;
#pragma unroll
  for (int g = 0; g < 4; ++g) res[g] = trd(lds, blkR + g * 256 + (4 * (G & 1) + q4) * 32 + p4 * 8);
  h_load(2, 0, 2);
  step(IC<0>{}, IC<6>{}, IC<9>{});
  put(0);
  STAMP(24);
  h_load(2, 2, 3); step(IC<1>{}, IC<0>{}, IC<9>{});
  h_load(2, 3, 5); step(IC<1>{}, IC<1>{}, IC<10>{});
  h_load(2, 5, 7); step(IC<1>{}, IC<2>{}, IC<11>{});
  h_load(3, 0, 2); step(IC<1>{}, IC<3>{}, IC<12>{});
  h_load(3, 2, 4); step(IC<1>{}, IC<4>{}, IC<13>{});
  h_load(3, 4, 6); step(IC<1>{}, IC<5>{}, IC<14>{});
  h_load(3, 6, 7); step(IC<1>{}, IC<6>{}, IC<14>{});
  put(1);
  STAMP(25);
  bar();
  fin(0); fin(1);
  SB();
  stepc(IC<2>{}, IC<0>{}, IC<13>{}); stepc(IC<2>{}, IC<1>{}, IC<12>{}); stepc(IC<2>{}, IC<2>{}, IC<11>{}); stepc(IC<2>{}, IC<3>{}, IC<10>{});
  stepc(IC<2>{}, IC<4>{}, IC<9>{}); stepc(IC<2>{}, IC<5>{}, IC<8>{}); stepc(IC<2>{}, IC<6>{}, IC<7>{});
  redo(IC<2>{});
  bar();
  put(2);
  SB();
  STAMP(26);
  stepc(IC<3>{}, IC<0>{}, IC<6>{}); stepc(IC<3>{}, IC<1>{}, IC<5>{}); stepc(IC<3>{}, IC<2>{}, IC<4>{}); stepc(IC<3>{}, IC<3>{}, IC<3>{});
  stepc(IC<3>{}, IC<4>{}, IC<2>{}); stepc(IC<3>{}, IC<5>{}, IC<1>{}); stepc(IC<3>{}, IC<6>{}, IC<0>{});
  redo(IC<3>{});
  put(3);
  bar();
  STAMP(27);
  fin(2); fin(3);
  STAMP(28);
  STAMP_OUT();
}

}

extern "C" void kernel_launch(void* const* d_in, const int* in_sizes, int n_in, void* d_out, int out_size, void* d_ws, size_t ws_size,
                              hipStream_t stream) {
  h3_ffn<<<256, 512, 0, stream>>>((const char*)d_in[0], (const char*)d_in[1], (const char*)d_in[2], (const char*)d_in[3], (const char*)d_in[4],
                                  (const char*)d_in[5], (const char*)d_in[7], (const char*)d_in[8], (const char*)d_in[9], (char*)d_out, (char*)d_ws);
}
```

```cpp
#include <hip/hip_runtime.h>
#include <stdint.h>

namespace {

#define LAS __attribute__((address_space(3)))
#define GAS __attribute__((address_space(1)))
#define DI __device__ __forceinline__
#define AI __attribute__((always_inline))
typedef uint32_t u32;
typedef unsigned long long u64;
typedef u32 u32x2 __attribute__((ext_vector_type(2)));
typedef u32 u32x4 __attribute__((ext_vector_type(4)));
typedef short s16x4 __attribute__((ext_vector_type(4)));
typedef short s16x8 __attribute__((ext_vector_type(8)));
typedef float f32x4 __attribute__((ext_vector_type(4)));
typedef float f32x16 __attribute__((ext_vector_type(16)));
typedef __bf16 bf2 __attribute__((ext_vector_type(2)));
typedef __amdgpu_buffer_rsrc_t rsrc_t;
#define RLX_AGENT __ATOMIC_RELAXED, __HIP_MEMORY_SCOPE_AGENT

constexpr int S_ = 2048, D_ = 448, F_ = 1792;
constexpr u32 TB = 448 * 64;
constexpr u32 WS_E  = 0;
constexpr u32 WS_F1 = 128;
constexpr u32 WS_F3 = WS_F1 + 256 * 128;
constexpr u32 WS_F2 = WS_F3 + 896 * 128;
constexpr u32 WS_S2 = 256 * 1024 + 16384;
constexpr u32 WS_CS = 224 * 1024;
constexpr u32 WS_ST = 256 * 1024;
constexpr u32 WS_U  = WS_S2 + 32768;
constexpr u32 WS_X  = WS_U + 64 * TB;
constexpr u32 WS_H  = WS_X + 64 * TB;
constexpr u32 WS_G  = WS_H + 64 * TB;
constexpr u32 WS_END = WS_G + 64 * 112 * 1024;
constexpr u32 WS_DBG = 16u << 20;
constexpr int SC1 = 16;
constexpr u32 OOB = 0xFFFFFF00u;
constexpr u32 ONES = 0x3f803f80u;
constexpr u32 EMPTY = 0xffffffffu;

constexpr u32 L_K = 0, L_V = 9216, L_X = 17408, L_SK = 25600;
constexpr u32 L_RED = 0, L_FC = 32768, FCS = 4160, FCH = 4 * FCS;
constexpr u32 L_U = L_FC + 2 * FCH, UCH = 4608, L_RING = L_U + 2 * UCH;
constexpr u32 L_H0 = 32768, L_H1 = L_H0 + TB, L_WE = L_H1 + TB, L_H2 = 0, L_H3 = L_WE + 32768, L_SP = 155648;
constexpr u32 L_P = TB, L_Q = L_P + 32768, L_R = L_Q + 32768, L_R2 = L_H3 + TB;
constexpr u32 L_RES = 0;
constexpr u32 LDS_BYTES = 163840;

DI float bflo(u32 x) { return __uint_as_float(x << 16); }
DI float bfhi(u32 x) { return __uint_as_float(x & 0xffff0000u); }
DI u32 pk(float lo, float hi) { bf2 p; p.x = (__bf16)lo; p.y = (__bf16)hi; return __builtin_bit_cast(u32, p); }
DI rsrc_t mk(const void* p, u32 bytes) { return __builtin_amdgcn_make_buffer_rsrc((void*)p, 0, bytes, 0x00020000); }
DI u32x2 trd(const char* lds, u32 a) { return __builtin_bit_cast(u32x2, __builtin_amdgcn_ds_read_tr16_b64_v4i16((LAS s16x4*)(lds + a))); }
DI u32x4 cat(u32x2 a, u32x2 b) { return u32x4{a.x, a.y, b.x, b.y}; }
DI u32x4 srd(const void* p, u32 bytes) { const u64 a = (u64)p; return u32x4{(u32)a, (u32)(a >> 32) & 0xffffu, bytes, 0x00020000u}; }
template <int AUX = 0> DI void dma(u32x4 r, u32 ldsoff, u32 voff, u32 soff) {
  if (AUX) asm volatile("s_mov_b32 m0, %0\n\ts_nop 0\n\tbuffer_load_dwordx4 %1, %2, %3 offen sc1 lds" :: "s"(ldsoff), "v"(voff), "s"(r), "s"(soff) : "memory");
  else asm volatile("s_mov_b32 m0, %0\n\ts_nop 0\n\tbuffer_load_dwordx4 %1, %2, %3 offen lds" :: "s"(ldsoff), "v"(voff), "s"(r), "s"(soff) : "memory");
}
DI float fnma(float a, float b, float c) { float r; asm("v_fma_f32 %0, -%1, %2, %3" : "=v"(r) : "v"(a), "v"(b), "v"(c)); return r; }
DI float op(float x) { asm("" : "+v"(x)); return x; }
DI u32 opq(u32 x) { asm volatile("" : "+v"(x)); return x; }
#define PIN(x) asm volatile("" : "+v"(x))
DI f32x4 mfma16(u32x4 a, u32x4 b, f32x4 c) { return __builtin_amdgcn_mfma_f32_16x16x32_bf16(__builtin_bit_cast(s16x8, a), __builtin_bit_cast(s16x8, b), c, 0, 0, 0); }
DI f32x16 mfma32(u32x4 a, u32x4 b, f32x16 c) { return __builtin_amdgcn_mfma_f32_32x32x16_bf16(__builtin_bit_cast(s16x8, a), __builtin_bit_cast(s16x8, b), c, 0, 0, 0); }
DI float dot2(u32 a, u32 b, float c) { return __builtin_amdgcn_fdot2_f32_bf16(__builtin_bit_cast(bf2, a), __builtin_bit_cast(bf2, b), c, false); }
#define VMW(n) asm volatile("s_waitcnt vmcnt(" #n ")" ::: "memory")
#define LG0() asm volatile("s_waitcnt lgkmcnt(0)" ::: "memory")
#define SB() __builtin_amdgcn_sched_barrier(0)
DI void bar() { asm volatile("s_waitcnt lgkmcnt(0)\n\ts_barrier" ::: "memory"); }
#define RBAR() asm volatile("s_barrier" ::: "memory")
DI float wsum(float v) {
  v += __builtin_amdgcn_update_dpp(0.f, v, 0x111, 0xf, 0xf, true);
  v += __builtin_amdgcn_update_dpp(0.f, v, 0x112, 0xf, 0xf, true);
  v += __builtin_amdgcn_update_dpp(0.f, v, 0x114, 0xf, 0xf, true);
  v += __builtin_amdgcn_update_dpp(0.f, v, 0x118, 0xf, 0xf, true);
  v += __builtin_amdgcn_update_dpp(0.f, v, 0x142, 0xa, 0xf, true);
  v += __builtin_amdgcn_update_dpp(0.f, v, 0x143, 0xc, 0xf, true);
  return __uint_as_float(__builtin_amdgcn_readlane(__float_as_uint(v), 63));
}
DI float hsum(float v) {
  const auto r = __builtin_amdgcn_permlane32_swap(__float_as_uint(v), __float_as_uint(v), false, false);
  return __uint_as_float(r[0]) + __uint_as_float(r[1]);
}
template <int SLEEP = 1> DI void poll(char* ws, u32 off, bool act, u64 tag) {
  bool ok = !act;
#pragma unroll 1
  for (int it = 0; it < 20000; ++it) {
    u64 x = tag;
    if (!ok) x = __hip_atomic_load((GAS u64*)(ws + off), RLX_AGENT);
    ok = (x == tag);
    if (__all(ok)) break;
    __builtin_amdgcn_s_sleep(SLEEP);
  }
  asm volatile("" ::: "memory");
}
DI u32 max4(u32x4 v) { return max(max(v.x, v.y), max(v.z, v.w)); }
DI u64 flag_ld(u32x4 r, u32 voff) { u64 v; asm volatile("buffer_load_dwordx2 %0, %1, %2, 0 offen sc1" : "=v"(v) : "v"(voff), "s"(r) : "memory"); return v; }
DI u32x4 ldg(u32x4 r, u32 voff, u32 soff) { u32x4 v; asm volatile("buffer_load_dwordx4 %0, %1, %2, %3 offen sc1" : "=v"(v) : "v"(voff), "s"(r), "s"(soff) : "memory"); return v; }
template <int N> DI void vmw(u32x4& x) { asm volatile("s_waitcnt vmcnt(%1)" : "+v"(x) : "n"(N) : "memory"); }
#define VMWP(n, x) asm volatile("s_waitcnt vmcnt(" #n ")" : "+v"(x) :: "memory")
DI void setflag(char* ws, u32 off, u64 tag) { __hip_atomic_store((GAS u64*)(ws + off), tag, RLX_AGENT); }

#define STAMP(i) do { if ((i) == 0) t_0 = __builtin_amdgcn_s_memrealtime(); } while (0)
#define STAMPC(i)
#define STAMP_OUT() do { if (b == 9 && tid == 0) __hip_atomic_store((GAS u64*)(ws + WS_DBG + 128), (u64)__builtin_amdgcn_s_memrealtime(), RLX_AGENT); } while (0)

DI float xsum(float a, float c) {
  const auto r = __builtin_amdgcn_permlane32_swap(__float_as_uint(a), __float_as_uint(c), false, false);
  return __uint_as_float(r[0]) + __uint_as_float(r[1]);
}
DI f32x4 ln2_terms(float t) {
  const auto r = __builtin_amdgcn_permlane32_swap(__float_as_uint(t), __float_as_uint(t), false, false);
  const float mean = __uint_as_float(r[0]) * (1.f / D_);
  const float V = fmaxf(__uint_as_float(r[1]) * (1.f / D_) - mean * mean, 0.f) + 1e-5f;
  const float rs = __builtin_amdgcn_rsqf(V);
  const float K1 = -2.f * 0.7978845608028654f * 1.4426950408889634f * rs;
  const float lo = -mean - bflo(pk(-mean, 0.f));
  return f32x4{__uint_as_float(pk(-mean, lo)), V, K1, (K1 * rs) * (0.044715f * rs)};
}
template <int N> struct IC { static constexpr int value = N; };
template <int N, int I = 0, class F> DI void sfor(F&& f) { if constexpr (I < N) { f(IC<I>{}); sfor<N, I + 1>(f); } }
struct W1 { u32x4 rg, ru; u32 vg, vu, ra, lb; };
template <int T> DI void w1_g(const W1& c, u32 base) { dma(c.rg, c.lb + base, c.vg, T * (32 * F_ * 2)); }
template <int T> DI void w1_u(const W1& c, u32 base) { dma(c.ru, c.lb + base + 8192, c.vu, T * (32 * F_ * 2)); }
template <int T> DI void w1_issue(const W1& c, u32 base) { w1_g<T>(c, base); w1_u<T>(c, base); }
template <int T> DI void w1_read(const char* lds, u32 a, u32x4 (&W)[28]) {
#pragma unroll
  for (int kk = 0; kk < 2; ++kk) W[2 * T + kk] = cat(trd(lds, a + kk * 4096), trd(lds, a + kk * 4096 + 1024));
}
DI float x8sum(float v) {
  v += __builtin_amdgcn_update_dpp(0.f, v, 0xb1, 0xf, 0xf, true);
  v += __builtin_amdgcn_update_dpp(0.f, v, 0x4e, 0xf, 0xf, true);
  v += __builtin_amdgcn_update_dpp(0.f, v, 0x141, 0xf, 0xf, true);
  return v;
}

__global__ __launch_bounds__(512, 2) void h3_ffn(const char* __restrict__ gq, const char* __restrict__ gk, const char* __restrict__ gv,
                                                 const char* __restrict__ gsk, const char* __restrict__ gf, const char* __restrict__ gD,
                                                 const char* __restrict__ gwg, const char* __restrict__ gwu, const char* __restrict__ gwd,
                                                 char* __restrict__ gout, char* ws) {
  __shared__ __attribute__((aligned(16))) char lds[LDS_BYTES];
  const int tid = threadIdx.x, l = tid & 63, w = __builtin_amdgcn_readfirstlane(tid >> 6);
  const int b = blockIdx.x, xc = b & 7, sl = b >> 3;
  const int tb64 = 4 * xc + (sl >> 3), role = sl & 7;
  const int sc = (sl >> 3) * 7 + role;
  u64 t_0 = 0;
  asm volatile("" :: "s"(gq), "s"(gk), "s"(gv), "s"(gsk), "s"(gf), "s"(gD), "s"(gwg), "s"(gwu), "s"(gwd), "s"(gout), "s"(ws));
  STAMP(0);
  const rsrc_t rw = mk(ws, WS_END);
  const int G = l >> 4, i16 = l & 15, q4 = i16 >> 2, p4 = l & 3, r8 = l >> 3;
  auto mktag = [&](u64 e0) AI { return ((u64)__builtin_amdgcn_readfirstlane((u32)e0) | ((u64)__builtin_amdgcn_readfirstlane((u32)(e0 >> 32)) << 32)) * 6364136223846793005ull + 1442695040888963407ull; };
  if (role == 7) {
    const rsrc_t rq = mk(gq, S_ * D_ * 2);
    const int row = 64 * tb64 + 8 * w + r8;
    u32x4 xs[7];
#pragma unroll
    for (int i = 0; i < 7; ++i) xs[i] = __builtin_amdgcn_raw_buffer_load_b128(rq, (u32)(row * 896 + (l & 7) * 16 + i * 128), 0, 0);
    const u64 e0 = __hip_atomic_load((GAS u64*)(ws + WS_E), RLX_AGENT);
    SB();
    SB();
    const u64 tag = mktag(e0);
    float s[4] = {};
#pragma unroll
    for (int i = 0; i < 7; ++i)
#pragma unroll
      for (int e = 0; e < 4; ++e) s[e] += bflo(xs[i][e]) + bfhi(xs[i][e]);
    const float mean = x8sum((s[0] + s[1]) + (s[2] + s[3])) * (1.f / D_);
    float v[4] = {};
#pragma unroll
    for (int i = 0; i < 7; ++i)
#pragma unroll
      for (int e = 0; e < 4; ++e) { const float d0 = bflo(xs[i][e]) - mean, d1 = bfhi(xs[i][e]) - mean; v[e] += d0 * d0; v[e] += d1 * d1; }
    const float rstd = __builtin_amdgcn_rsqf(x8sum((v[0] + v[1]) + (v[2] + v[3])) * (1.f / D_) + 1e-5f);
    const u32 so = WS_ST + (row >> 8) * 2048 + ((row >> 1) & 3) * 512 + ((row >> 5) & 7) * 64 + ((row >> 3) & 3) * 16 + (row & 1) * 8;
    __builtin_amdgcn_raw_buffer_store_b64(u32x2{__float_as_uint(mean), __float_as_uint(rstd)}, rw, (l & 7) == 0 ? so : OOB, 0, SC1);
    VMW(0); bar();
    if (tid == 0) setflag(ws, WS_F1 + (tb64 * 8 + 7) * 128, tag);
#pragma unroll
    for (int i = 0; i < 7; ++i) __builtin_amdgcn_raw_buffer_store_b128(u32x4{EMPTY, EMPTY, EMPTY, EMPTY}, rw, (u32)(tid * 16), WS_H + (u32)(tb64 * 2 * TB + i * 8192), SC1);
    __builtin_amdgcn_raw_buffer_store_b128(u32x4{EMPTY, EMPTY, EMPTY, EMPTY}, rw, tid < 64 ? WS_S2 + (u32)(tb64 * 1024 + tid * 16) : tid < 120 ? WS_CS + (u32)(tb64 * 896 + (tid - 64) * 16) : OOB, 0, SC1);
    VMW(0); bar();
    if (tid == 0) setflag(ws, WS_F2 + tb64 * 128, tag);
    STAMP(1);
    {
      const int mat = tb64 >= 14, j = tb64 < 28 ? tb64 - 14 * mat : 0;
      const rsrc_t rm = mk(mat ? gwu : gwg, D_ * F_ * 2);
      u32x4 wv[14];
#pragma unroll
      for (int i = 0; i < 14; ++i) wv[i] = __builtin_amdgcn_raw_buffer_load_b128(rm, tb64 < 28 ? (u32)(((4 * w + G + 32 * i) * F_ + 128 * j) * 2 + i16 * 16) : OOB, 0, 0);
      float c[8] = {};
#pragma unroll
      for (int i = 0; i < 14; ++i)
#pragma unroll
        for (int e = 0; e < 4; ++e) { c[2 * e] += bflo(wv[i][e]); c[2 * e + 1] += bfhi(wv[i][e]); }
      *(f32x4*)(lds + (4 * w + G) * 512 + i16 * 32) = f32x4{c[0], c[1], c[2], c[3]};
      *(f32x4*)(lds + (4 * w + G) * 512 + i16 * 32 + 16) = f32x4{c[4], c[5], c[6], c[7]};
      poll(ws, WS_F2 + (u32)(l & 31) * 128, true, tag);
      bar();
      if (tid < 128 && tb64 < 28) {
        float t = 0.f;
#pragma unroll
        for (int n = 0; n < 32; ++n) t += *(const float*)(lds + n * 512 + tid * 4);
        const u32 hi = pk(t, t), lo = pk(t - bflo(hi), 0.f);
        __builtin_amdgcn_raw_buffer_store_b64(u32x2{hi, lo}, rw, WS_CS + (u32)(((j * 8 + (tid >> 4)) * 32 + mat * 16 + (tid & 15)) * 8), 0, SC1);
      }
    }
    STAMP(2);
    const u32x4 dw = srd(ws, WS_END);
    const u32 lb = (u32)(size_t)(LAS char*)lds;
#pragma unroll 1
    for (int half = (tb64 & 1) ^ 1; half < 2; ++half) {
      const int tb = 32 * half + tb64;
#pragma unroll 1
      for (int it = 0; it < 4000; ++it) {
#pragma unroll
        for (int k = 0; k < 4; ++k) if (k < 3 || l < 32) dma<SC1>(dw, lb + 32768 + w * 3584 + k * 1024, l * 16, WS_H + (u32)(tb * TB + w * 3584 + k * 1024));
        VMW(0);
        u32 m = 0;
#pragma unroll
        for (int k = 0; k < 4; ++k) m = max(m, max4(*(const u32x4*)(lds + 32768 + w * 3584 + (k < 3 ? k * 1024 + l * 16 : 3072 + (l & 31) * 16))));
        if (!__any(m == EMPTY)) break;
        __builtin_amdgcn_s_sleep(6);
      }
      float s1 = 0.f, s2 = 0.f;
#pragma unroll
      for (int i = 0; i < 7; ++i) {
        const u32x2 t = trd(lds, 32768 + (u32)((56 * w + 4 * (G >> 1) + q4) * 64 + (G & 1) * 32 + p4 * 8) + i * 512);
        s1 = dot2(t.x, ONES, s1); s2 = dot2(t.x, t.x, s2); s1 = dot2(t.y, ONES, s1); s2 = dot2(t.y, t.y, s2);
      }
      *(float*)(lds + 20480 + half * 2048 + l * 32 + w * 4) = xsum(s1, s2);
      bar();
      if (w == 0) {
        const f32x4 a = *(const f32x4*)(lds + 20480 + half * 2048 + l * 32), c = *(const f32x4*)(lds + 20480 + half * 2048 + l * 32 + 16);
        __builtin_amdgcn_raw_buffer_store_b128(__builtin_bit_cast(u32x4, ln2_terms(((a.x + a.y) + (a.z + a.w)) + ((c.x + c.y) + (c.z + c.w)))), rw, l < 32 ? WS_S2 + (u32)(tb * 512 + l * 16) : OOB, 0, SC1);
      }
    }
    STAMP(3);
    STAMP_OUT();
    return;
  }
  const int cp = xc * 28 + sc, cch = w & 1, vp = w >> 1;
  u32x4 fhi; u32x2 flo; u32 Dh; u64 e0;
  const u32 lb = (u32)(size_t)(LAS char*)lds;
  const u32x4 dw = srd(ws, WS_END);
  {
    const u32x4 rk = srd(gk, S_ * D_ * 2), rv = srd(gv, S_ * D_ * 2);
    const int c8 = l & 7, s0 = 64 * tb64;
    const int swv = ((r8 >> 1) & 1) | ((w & 1) << 1), swk = swv ^ 2;
    const u32 g0 = (u32)((s0 + 8 * w + r8) * 896 + role * 128);
    if (w == 0) dma(rk, lb + L_K, g0 - 8 * 896 + ((c8 ^ (swv << 1)) << 4), 0);
    if (w == 1) dma(srd(gsk, D_ * 8), lb + L_SK, (u32)(512 * role + 16 * l), 0);
    dma(rk, lb + L_K + 1024 + w * 1024, g0 + ((c8 ^ (swk << 1)) << 4), 0);
    dma(rv, lb + L_V + w * 1024, g0 + ((c8 ^ (swv << 1)) << 4), 0);
    dma(srd(gq, S_ * D_ * 2), lb + L_X + w * 1024, g0 + ((c8 ^ (swv << 1)) << 4), 0);
    const rsrc_t rf = mk(gf, S_ * D_ * 2);
    const int fq = vp * 64 + l;
    const u32 fo = (u32)((2 * cp + cch) * 4096 + 16 * fq);
    fhi = __builtin_amdgcn_raw_buffer_load_b128(rf, fo, 0, 0);
    flo = __builtin_amdgcn_raw_buffer_load_b64(rf, fq > 0 ? fo - 8 : OOB, 0, 0);
    Dh = __builtin_amdgcn_raw_buffer_load_b16(mk(gD, D_ * 2), (u32)((2 * cp + ((l >> 2) & 1)) * 2), 0, 0);
    e0 = __hip_atomic_load((GAS u64*)(ws + WS_E), RLX_AGENT);
    SB();
  }
  const int gi = 4 * (xc >> 1) + sc / 7, gj = 7 * (xc & 1) + sc % 7;
  W1 w1;
  {
    const u32 v0 = (u32)(((4 * w + G) * F_ + 128 * gj) * 2);
    w1.rg = srd(gwg, D_ * F_ * 2); w1.ru = srd(gwu, D_ * F_ * 2); w1.lb = lb + w * 1024;
    w1.vg = v0 + ((i16 ^ (4 * G)) << 4); w1.vu = v0 + ((i16 ^ (4 * G + 2)) << 4);
    w1.ra = (u32)((G & 1) * 8192 + (8 * (G >> 1) + q4) * 256 + ((w ^ (2 * q4 + (G & 1))) * 32) + p4 * 8);
  }
  VMW(5); bar();
  STAMP(1);
  {
    const int tbl = w >> 2, chg = w & 3;
    const u32 rb = (u32)((32 * tbl + 8 * G + q4) * 128 + p4 * 8);
    const u32 sv = (u32)((chg ^ ((q4 >> 1) | ((G & 1) << 1))) * 32), sk2 = sv ^ 64;
    const u32x2 km = trd(lds, L_K + rb + 512 + sv), k0 = trd(lds, L_K + rb + 1024 + sk2), k1 = trd(lds, L_K + rb + 1536 + sk2);
    const u32x2 v0 = trd(lds, L_V + rb + sv), v1 = trd(lds, L_V + rb + 512 + sv);
    const u32x2 skv = *(const u32x2*)(lds + L_SK + (16 * chg + i16) * 8);
    const float kk[11] = {bfhi(km.x), bflo(km.y), bfhi(km.y), bflo(k0.x), bfhi(k0.x), bflo(k0.y), bfhi(k0.y), bflo(k1.x), bfhi(k1.x), bflo(k1.y), bfhi(k1.y)};
    const float vv[8] = {bflo(v0.x), bfhi(v0.x), bflo(v0.y), bfhi(v0.y), bflo(v1.x), bfhi(v1.x), bflo(v1.y), bfhi(v1.y)};
    const float s0 = bflo(skv.x), s1 = bfhi(skv.x), s2 = bflo(skv.y), s3 = bfhi(skv.y);
    float u[8];
#pragma unroll
    for (int e = 0; e < 8; ++e) u[e] = (kk[e + 3] * s0 + kk[e + 2] * s1 + kk[e + 1] * s2 + kk[e] * s3) * vv[e];
    const u32 o = (u32)((2 * tb64 + tbl) * TB + (64 * role + 16 * chg + i16) * 64 + G * 16);
    __builtin_amdgcn_raw_buffer_store_b128(u32x4{pk(u[0], u[1]), pk(u[2], u[3]), pk(u[4], u[5]), pk(u[6], u[7])}, rw, WS_U + o, 0, SC1);
    SB();
    STAMP(2);
    VMW(5); bar();
    const u32x2 x0 = trd(lds, L_X + rb + sv), x1 = trd(lds, L_X + rb + 512 + sv);
    __builtin_amdgcn_raw_buffer_store_b128(cat(x0, x1), rw, WS_X + o, 0, SC1);
    PIN(fhi); PIN(flo); PIN(Dh); PIN(e0);
    SB();
  }
  STAMP(3);
  {
    const u32 a0 = __builtin_amdgcn_alignbit(flo.y, flo.x, 16), a1 = __builtin_amdgcn_alignbit(fhi.x, flo.y, 16), a2 = __builtin_amdgcn_alignbit(fhi.y, fhi.x, 16),
              a3 = __builtin_amdgcn_alignbit(fhi.z, fhi.y, 16), a4 = __builtin_amdgcn_alignbit(fhi.w, fhi.z, 16);
    char* fc = lds + L_FC + cch * FCH + (vp * 64 + l) * 16 + 32;
    *(u32x4*)(fc) = fhi;
    *(u32x4*)(fc + FCS) = u32x4{a1, a2, a3, a4};
    *(u32x4*)(fc + 2 * FCS) = u32x4{flo.y, fhi.x, fhi.y, fhi.z};
    *(u32x4*)(fc + 3 * FCS) = u32x4{a0, a1, a2, a3};
    if (tid >= 128 && tid < 144) *(u32x4*)(lds + L_FC + (tid & 7) * FCS + ((tid >> 3) & 1) * 16) = u32x4{0, 0, 0, 0};
    if (tid >= 192 && tid < 252) { const int z = tid - 192; *(u32x4*)(lds + L_U + (z / 30) * UCH + (z % 30) * 16) = u32x4{0, 0, 0, 0}; }
  }
  const u64 tag = mktag(e0);
  const float Dv = bflo(Dh);
  SB();
  w1_issue<0>(w1, L_RING); w1_issue<1>(w1, L_RING + 16384);
  STAMP(4);
  VMW(4); bar();
  if (tid == 0) setflag(ws, WS_F1 + (tb64 * 8 + role) * 128, tag);
  STAMP(5);
  const int hh = l >> 5, tk = l & 31;
  const u32 gso = WS_G + (u32)(((2 * gi * 2 + (tk >> 4)) * 56 + 4 * gj + (w >> 1)) * 1024 + (2 * (w & 1) + hh) * 256 + i16 * 16);
  auto gofs = [](int g) AI { return (u32)((32 * (g >> 1) + (g & 1)) * 2 * 56 * 1024); };
  u32x4 W[28];
  w1_issue<2>(w1, L_RING + 32768); w1_issue<3>(w1, L_RING + 49152); w1_issue<4>(w1, L_RING + 65536); w1_issue<5>(w1, L_RED); w1_issue<6>(w1, L_RED + 16384);
  SB();

  const int ech = (l >> 2) & 1;
  const u32 eo = (u32)((8 * w + r8) * TB + (2 * cp + ech) * 64 + p4 * 16);
  STAMP(6);
  poll(ws, WS_F1 + (u32)(32 * w + 8 * (l & 3) + ((l & 4) ? 7 : (cp >> 5))) * 128, l < 8, tag);
  STAMP(7);
  u32x4 uu = ldg(dw, eo, WS_U), ex = ldg(dw, eo, WS_X);
  u32x4 est[4];
#pragma unroll
  for (int i = 0; i < 4; ++i) est[i] = ldg(dw, (u32)(r8 * 64 + p4 * 16), WS_ST + (u32)(w * 2048 + i * 512));
  u64 fl = flag_ld(dw, WS_F2 + (u32)(4 * w + (l & 3)) * 128);
  bar();
  const u32 ra0 = opq(w1.ra), ra1 = opq(w1.ra + L_RING), ra2 = opq(w1.ra + L_RING + 49152);
  w1_read<5>(lds, ra0 + L_RED, W); w1_read<6>(lds, ra0 + L_RED + 16384, W);
  w1_read<0>(lds, ra1, W); w1_read<1>(lds, ra1 + 16384, W); w1_read<2>(lds, ra1 + 32768, W); w1_read<3>(lds, ra2, W); w1_read<4>(lds, ra2 + 16384, W);
  const u32 aA = opq(L_FC + cch * FCH + q4 * FCS + 64 - 16 * G + 8 * p4 - 8 + 128 * vp);
  const u32 aB = opq(L_U + cch * UCH + 32 * i16 + 16 * G + 448 - 128 * vp - 64);
  const u32 rwr = opq(L_RED + w * 4096 + G * 256 + ((i16 ^ ((G >> 1) << 3)) << 4));
  const u32 rr = opq(L_RED + ech * 4096 + (w & 3) * 1024 + (p4 & 1) * 512 + (((2 * r8 + (p4 >> 1)) ^ ((p4 & 1) << 3)) << 4));
  SB();
  vmw<6>(uu);
  *(u32x4*)(lds + L_U + ech * UCH + 480 + (8 * w + r8) * 64 + p4 * 16) = uu;
  bar();
  STAMP(8);
  auto rdA = [&](int v, int m) AI { const u32 o = 64 * v + 512 * m; return cat(trd(lds, aA + o + 8), trd(lds, aA + o)); };
  auto rdB = [&](int v, int m) AI { return *(const u32x4*)(lds + aB + 64 * (1 - v) + 512 * m); };
  float ex8[8], eq8[8], eu8[8];
  auto terms = [&]() AI {
#pragma unroll
    for (int i = 0; i < 4; ++i) {
      ex8[2 * i] = bflo(ex[i]); ex8[2 * i + 1] = bfhi(ex[i]);
      eu8[2 * i] = bflo(uu[i]) * Dv; eu8[2 * i + 1] = op(bfhi(uu[i]) * Dv);
      eq8[2 * i] = (ex8[2 * i] - __uint_as_float(est[i].x)) * __uint_as_float(est[i].y);
      eq8[2 * i + 1] = op(op(ex8[2 * i + 1] - __uint_as_float(est[i].z)) * __uint_as_float(est[i].w));
    }
#pragma unroll
    for (int i = 0; i < 8; ++i) { PIN(ex8[i]); PIN(eq8[i]); PIN(eu8[i]); }
  };
  auto epilogue = [&]() AI {
    f32x4 p[8];
#pragma unroll
    for (int n = 0; n < 4; ++n) { p[2 * n] = *(const f32x4*)(lds + rr + n * 8192); p[2 * n + 1] = *(const f32x4*)(lds + rr + n * 8192 + 256); }
    float h[8];
#pragma unroll
    for (int i = 0; i < 8; ++i) {
      const float y = op(op(eu8[i] + p[i >> 2][i & 3]) + p[2 + (i >> 2)][i & 3]) + op(p[4 + (i >> 2)][i & 3] + p[6 + (i >> 2)][i & 3]);
      h[i] = op(ex8[i] + eq8[i] * op(y));
    }
    __builtin_amdgcn_raw_buffer_store_b128(u32x4{pk(h[0], h[1]), pk(h[2], h[3]), pk(h[4], h[5]), pk(h[6], h[7])}, rw, WS_H + eo, 0, SC1);
  };
  {
    f32x4 acc[4];
    u32x4 A[2][4], B[2][4];
#pragma unroll
    for (int v = 0; v < 2; ++v) {
      A[v][0] = rdA(v, 0); SB();
#pragma unroll
      for (int m = 0; m < 4; ++m) { B[v][m] = rdB(v, m); SB(); }
#pragma unroll
      for (int m = 1; m < 4; ++m) { A[v][m] = rdA(v, m); SB(); }
      if (v == 0) {
        vmw<0>(ex); PIN(est[0]); PIN(est[1]); PIN(est[2]); PIN(est[3]); PIN(fl);
        if (!__all(fl == tag)) poll<2>(ws, WS_F2 + (u32)(4 * w + (l & 3)) * 128, true, tag);
        if (w < 4) terms();
        SB();
      }
    }
#pragma unroll
    for (int v = 0; v < 2; ++v)
#pragma unroll
      for (int m = 0; m < 4; ++m) {
#pragma unroll
        for (int c = m; c < 4; ++c) acc[c] = mfma16(A[v][m], B[v][c - m], (v == 0 && m == 0) ? f32x4{} : acc[c]);
        SB();
        if (v == 0 && m == 0) w1_g<7>(w1, L_RING);
        if (v == 0 && m == 2) w1_u<7>(w1, L_RING);
        if (v == 1 && m == 0) w1_g<8>(w1, L_RING + 16384);
        SB();
      }
#pragma unroll
    for (int c = 0; c < 4; ++c) *(f32x4*)(lds + rwr + c * 1024) = acc[c];
  }
  bar();
  STAMP(9);
  if (w < 4) epilogue();
  SB();
  STAMP(10);
  {
    f32x4 acc[4];
    u32x4 A[2][8], B[2][8];
    auto rd = [&](int v, int m) AI {
      if (m == 0) {
        A[v][0] = rdA(v, 0);
#pragma unroll
        for (int d = 4; d < 8; ++d) B[v][d] = rdB(v, d);
      } else { A[v][m] = rdA(v, m); if (m <= 4) B[v][4 - m] = rdB(v, 4 - m); }
      SB();
    };
    rd(0, 0); rd(0, 1); rd(0, 2); rd(0, 3);
#pragma unroll
    for (int v = 0; v < 2; ++v) {
#pragma unroll
      for (int m = 0; m < 8; ++m) {
#pragma unroll
        for (int c = 4; c < 8; ++c) if (m <= c) acc[c - 4] = mfma16(A[v][m], B[v][c - m], (v == 0 && m == 0) ? f32x4{} : acc[c - 4]);
        SB();
        if (m < 4) rd(v, m + 4); else if (v == 0) rd(1, m - 4);
        if (v == 0) {
          if (m == 0) w1_u<8>(w1, L_RING + 16384);
          if (m == 1) w1_g<9>(w1, L_RING + 32768);
          if (m == 2) w1_u<9>(w1, L_RING + 32768);
          if (m == 3) w1_g<10>(w1, L_RING + 49152);
          if (m == 4) w1_u<10>(w1, L_RING + 49152);
          if (m == 6) w1_g<11>(w1, L_RING + 65536);
          if (m == 7) w1_u<11>(w1, L_RING + 65536);
        }
        SB();
      }
    }
    if (w >= 4) terms();
    STAMP(11);
    VMW(0); bar();
#pragma unroll
    for (int c = 0; c < 4; ++c) *(f32x4*)(lds + rwr + c * 1024) = acc[c];
  }
  const u32 lo16 = l * 16;
  if (w < 4) {
#pragma unroll
    for (int k = 0; k < 7; ++k) dma<SC1>(dw, lb + L_H0 + (7 * w + k) * 1024, lo16, WS_H + (u32)(2 * gi * TB + (7 * w + k) * 1024));
  }
  w1_read<7>(lds, ra1, W); w1_read<8>(lds, ra1 + 16384, W); w1_read<9>(lds, ra1 + 32768, W); w1_read<10>(lds, ra2, W); w1_read<11>(lds, ra2 + 16384, W);
  bar();
  STAMP(12);
  if (w >= 4) {
    epilogue();
    SB();
#pragma unroll
    for (int i = 0; i < 2; ++i) {
      W1 c = w1; c.lb -= i * 4096; c.vg -= i * (16 * F_ * 2); c.vu -= i * (16 * F_ * 2);
      w1_issue<12>(c, L_WE); w1_issue<13>(c, L_WE + 16384);
    }
  } else {
    const u32 ha = opq(L_H0 + 7 * w * 1024 + lo16);
#pragma unroll 1
    for (int it = 0; it < 4000; ++it) {
      VMW(0);
      u32 m = 0;
#pragma unroll
      for (int k = 0; k < 7; ++k) { const u32x4 v = *(const u32x4*)(lds + ha + k * 1024); m = max(m, max4(v)); }
      if (!__any(m == EMPTY)) break;
#pragma unroll
      for (int k = 0; k < 7; ++k) dma<SC1>(dw, lb + L_H0 + (7 * w + k) * 1024, lo16, WS_H + (u32)(2 * gi * TB + (7 * w + k) * 1024));
    }
    VMW(0);
    STAMP(13);
  }
  bar();
  STAMP(14);

  u64 csv;
  const u32 bta = (u32)((8 * (G >> 1) + q4) * 64 + (G & 1) * 32 + p4 * 8);
  const u32 sta = (u32)((56 * w + 4 * (G >> 1) + q4) * 64 + (G & 1) * 32 + p4 * 8);
  auto hbase = [](int g) AI { return g == 0 ? L_H0 : g == 1 ? L_H1 : g == 2 ? L_H2 : L_H3; };
  const u32 spa = opq(L_SP + l * 32);
  float s1a, s1b, s2a, s2b, xs; u32x2 st[2];
  auto stats_step = [&](u32 b0, int k) AI {
    if (k > 0) {
      if (k & 1) { s1a = k == 1 ? xs : s1a + xs; s2a = k == 1 ? xs * xs : s2a + xs * xs; PIN(s1a); PIN(s2a); }
      else       { s1b = k == 2 ? xs : s1b + xs; s2b = k == 2 ? xs * xs : s2b + xs * xs; PIN(s1b); PIN(s2b); }
    }
    if (k < 28) {
      const int u = k >> 2;
      const u32 d = (k & 2) ? st[u & 1].y : st[u & 1].x;
      xs = (k & 1) ? bfhi(d) : bflo(d); PIN(xs);
      if ((k & 3) == 3 && u + 2 < 7) st[u & 1] = trd(lds, b0 + (u + 2) * 512);
    } else *(float*)(lds + spa + w * 4) = xsum(s1a + s1b, s2a + s2b);
  };
  f32x4 sp0, sp1, tm; float gu[8], gt[8]; u32 od[4];
  auto center = [&](f32x16& acc, float m) AI {
    const u32 d = __float_as_uint(m);
    acc = mfma32(u32x4{(u32)csv, (u32)(csv >> 32), 0, 0}, u32x4{d, d & 0xffffu, 0, 0}, acc);
  };
  auto geglu_step = [&](int g, f32x16& acc, int i) AI {
    if (g == 0) {
      if (i == 1) { sp0 = *(const f32x4*)(lds + spa); sp1 = *(const f32x4*)(lds + spa + 16); }
      if (i == 3) { sp0.x = op(sp0.x + sp0.y); sp0.z = op(sp0.z + sp0.w); sp1.x = op(sp1.x + sp1.y); sp1.z = op(sp1.z + sp1.w); PIN(sp0); PIN(sp1); }
      if (i == 4) { tm = ln2_terms((sp0.x + sp0.z) + (sp1.x + sp1.z)); PIN(tm); }
      if (i == 5) center(acc, tm.x);
    }
#pragma unroll
    for (int e = 0; e < 8; ++e) {
      const int s = i - (g == 0 ? 7 + (13 * e) / 7 : 2 + (18 * e) / 7);
      if (s == 0) { gt[e] = acc[e] * acc[e]; gu[e] = op(acc[e] * acc[e + 8]); PIN(gt[e]); PIN(gu[e]); }
      if (s == 1) { gt[e] = tm.w * gt[e] + tm.z; PIN(gt[e]); }
      if (s == 2) { gt[e] = acc[e] * gt[e]; PIN(gt[e]); }
      if (s == 3) { gt[e] = __builtin_amdgcn_exp2f(gt[e]); PIN(gt[e]); }
      if (s == 4) { gt[e] = gt[e] * tm.y + tm.y; PIN(gt[e]); }
      if (s == 5) { gt[e] = __builtin_amdgcn_rcpf(gt[e]); PIN(gt[e]); }
      if (s == 6) { gu[e] = gu[e] * gt[e]; PIN(gu[e]); }
      if (s == 7 && (e & 1)) { od[e >> 1] = pk(gu[e - 1], gu[e]); PIN(od[e >> 1]); }
    }
    if (i == 28) {
      __builtin_amdgcn_raw_buffer_store_b128(u32x4{od[0], od[1], od[2], od[3]}, rw, gso, gofs(g), SC1);
    }
  };
  constexpr int PF = 4;
  u32x4 B[PF];
  u32 ba = opq(hbase(0) + bta);
  auto chain = [&](int g, f32x16& acc, auto side) AI {
    const u32 bn = opq(hbase(g < 3 ? g + 1 : g) + bta);
    sfor<28>([&](auto kc) AI {
      constexpr int k = decltype(kc)::value;
      acc = mfma32(W[k], B[k % PF], k == 0 ? f32x16{} : acc);
      SB();
      side(k);
      if (k + PF < 28) B[k % PF] = cat(trd(lds, ba + (k + PF) * 1024), trd(lds, ba + (k + PF) * 1024 + 256));
      else if (g < 3) B[k % PF] = cat(trd(lds, bn + (k + PF - 28) * 1024), trd(lds, bn + (k + PF - 28) * 1024 + 256));
      SB();
    });
    ba = bn;
  };
  auto flag3 = [&](int g) AI { setflag(ws, WS_F3 + (u32)(((gi * 4 + g) * 14) + gj) * 128, tag); };
  const int ri = 2 * xc + sc / 14, rq2 = (sc / 7) & 1, n2 = sc % 7;
  const u32x4 rd = srd(gwd, D_ * F_ * 2);
  const u32 wdv = (u32)(((224 * w + r8) * D_ + 64 * n2) * 2 + (((l & 7) ^ (((r8 >> 1) & 3) << 1)) << 4));
  const u32 blkR = w < 7 ? L_R + w * 4096 : L_R2;
  u32 wda[4];
#pragma unroll
  for (int nt = 0; nt < 4; ++nt) wda[nt] = (16 * (G >> 1) + 4 * (G & 1) + q4) * 128 + ((nt ^ (2 * (G & 1) + (q4 >> 1))) * 32) + p4 * 8;
  u32x4 Wd[7][4];
  auto wd_blk = [&](int k) AI { return k % 3 == 1 ? blkR : (k % 3 == 2 ? L_Q : L_P) + w * 4096; };
  auto wd_dma = [&](int k, int i) AI { dma(rd, lb + wd_blk(k) + i * 1024, wdv, (4 * k + i) * (8 * D_ * 2)); };
  auto wd_read = [&](int k) AI {
#pragma unroll
    for (int nt = 0; nt < 4; ++nt) { const u32 a = opq(wda[nt] + wd_blk(k)); Wd[k][nt] = cat(trd(lds, a), trd(lds, a + 1024)); }
  };
  const int j0 = (14 * w) >> 3, jn = ((14 * w + 13) >> 3) - j0;
  const u32 f3o = WS_F3 + (u32)(ri * 56 + j0 + ((l & 3) < jn ? (l & 3) : jn)) * 128;

  __builtin_amdgcn_s_setprio(1);
  f32x16 accA, accB;
  u32x4 tm1, tm2, tm3;
  const u32 s2o = WS_S2 + (u32)(2 * gi * 512 + tk * 16);
  auto h_dma = [&](int g, int k) AI {
    if (k < 3 || l < 32) dma<SC1>(dw, lb + hbase(g) + w * 3584 + k * 1024, lo16, WS_H + (u32)((32 * (g >> 1) + 2 * gi + (g & 1)) * TB + w * 3584 + k * 1024));
  };
  const u32 cso = l < 32 ? WS_CS + (u32)(((gj * 8 + w) * 32 + l) * 8) : OOB;
  u32x4 cv[4];
  auto h_rd = [&](int g) AI {
    const u32 a = opq(hbase(g) + w * 3584 + lo16), a3 = opq(hbase(g) + w * 3584 + 3072 + tk * 16);
    cv[0] = *(const u32x4*)(lds + a); cv[1] = *(const u32x4*)(lds + a + 1024); cv[2] = *(const u32x4*)(lds + a + 2048); cv[3] = *(const u32x4*)(lds + a3);
  };
  auto h_fix = [&](int g) AI {
    u32 cm = g == 1 ? max((u32)csv, (u32)(csv >> 32)) : g == 2 ? max4(tm1) : max(max4(tm2), max4(tm3));
    cm = max(max(cm, max(max4(cv[0]), max4(cv[1]))), max(max4(cv[2]), max4(cv[3])));
#pragma unroll 1
    for (int it = 0; it < 4000 && __any(cm == EMPTY); ++it) {
      h_dma(g, 0); h_dma(g, 1); h_dma(g, 2); h_dma(g, 3);
      if (g == 1) { csv = flag_ld(dw, cso); VMWP(0, csv); }
      if (g == 2) { tm1 = ldg(dw, s2o, 512); vmw<0>(tm1); }
      if (g == 3) { tm2 = ldg(dw, s2o, 32 * 512); tm3 = ldg(dw, s2o, 33 * 512); vmw<0>(tm2); PIN(tm3); }
      h_rd(g);
      cm = g == 1 ? max((u32)csv, (u32)(csv >> 32)) : g == 2 ? max4(tm1) : max(max4(tm2), max4(tm3));
      cm = max(max(cm, max(max4(cv[0]), max4(cv[1]))), max(max4(cv[2]), max4(cv[3])));
    }
  };
  SB();
  {
    const u32 b0 = opq(hbase(0) + sta);
#pragma unroll
    for (int k = 0; k < PF; ++k) B[k] = cat(trd(lds, ba + k * 1024), trd(lds, ba + k * 1024 + 256));
    st[0] = trd(lds, b0); st[1] = trd(lds, b0 + 512);
    SB();
    chain(0, accA, [&](int k) AI {
      stats_step(b0, k);
      if (k <= 6 && !(k & 1)) h_dma(1, k >> 1);
      if (k == 7) csv = flag_ld(dw, cso);
      if (k == 10 || k == 13) __builtin_amdgcn_raw_buffer_store_b128(u32x4{EMPTY, EMPTY, EMPTY, EMPTY}, rw, gso, gofs(k == 10 ? 2 : 3), SC1);
      if (k == 14) { VMW(7); RBAR(); const u32 ra3 = opq(w1.ra + L_WE); w1_read<12>(lds, ra3, W); w1_read<13>(lds, ra3 + 16384, W); }
      if (k == 19) { VMWP(2, csv); h_rd(1); }
      if (k == 22) { h_fix(1); RBAR(); }
      if (k >= 23 && (k & 1)) h_dma(2, (k - 23) >> 1);
    });
    stats_step(b0, 28);
  }
  STAMP(16);
  chain(1, accB, [&](int k) AI {
    if (k == 1) { bar(); if (tid == 0 && b == 0) setflag(ws, WS_E, tag); h_dma(2, 3); }
    geglu_step(0, accA, k);
    if (k == 3) tm1 = ldg(dw, s2o, 512);
    if (k >= 5 && k <= 14 && k % 3 == 2) wd_dma(0, (k - 5) / 3);
    if (k >= 16 && k <= 25 && k % 3 == 1) h_dma(3, (k - 16) / 3);
    if (k == 20) { vmw<6>(tm1); h_rd(2); }
    if (k == 23) { h_fix(2); RBAR(); }
  });
  center(accB, __uint_as_float(tm1.x));
  geglu_step(0, accA, 28);
  tm = __builtin_bit_cast(f32x4, tm1);
  SB();
  STAMP(18);
  chain(2, accA, [&](int k) AI {
    if (k == 1) RBAR();
    geglu_step(1, accB, k);
    if (k <= 6 && !(k & 1)) wd_dma(1, k >> 1);
    if (k == 3) { VMW(7); wd_read(0); }
    if (k == 8) { tm2 = ldg(dw, s2o, 32 * 512); tm3 = ldg(dw, s2o, 33 * 512); }
    if (k >= 10 && k <= 24 && !(k & 1)) wd_dma(2 + ((k - 10) >> 3), ((k - 10) >> 1) & 3);
    if (k == 19) { VMW(11); h_rd(3); }
    if (k == 23) { vmw<7>(tm2); PIN(tm3); h_fix(3); RBAR(); if (tid == 0) flag3(0); }
  });
  center(accA, __uint_as_float(tm2.x));
  geglu_step(1, accB, 28);
  tm = __builtin_bit_cast(f32x4, tm2);
  SB();
  STAMP(20);
  chain(3, accB, [&](int k) AI {
    geglu_step(2, accA, k);
    if (k == 12) fl = flag_ld(dw, f3o);
    if (k == 15) { VMW(1); RBAR(); if (tid == 0) flag3(1); }
    if (k >= 15 && k < 18) wd_read(k - 14);
    if (k == 19) LG0();
    if (k >= 19 && k < 27 && (k & 1)) wd_dma(4, (k - 19) >> 1);
  });
  center(accB, __uint_as_float(tm3.x));
  geglu_step(2, accA, 28);
  tm = __builtin_bit_cast(f32x4, tm3);
  STAMP(21);
#pragma unroll
  for (int e = 0; e < 8; ++e) {
    const float a = accB[e], u = accB[e + 8];
    gu[e] = (a * u) * __builtin_amdgcn_rcpf(__builtin_amdgcn_exp2f(a * (tm.w * (a * a) + tm.z)) * tm.y + tm.y);
  }
#pragma unroll
  for (int e = 0; e < 4; ++e) od[e] = pk(gu[2 * e], gu[2 * e + 1]);
  geglu_step(3, accB, 28);
  __builtin_amdgcn_s_setprio(0);
  SB();
  STAMP(22);
  const u32 gb = WS_G + (u32)((2 * ri * 2 + rq2) * 56 + 7 * w) * 1024;
  u32x4 Hf[3][7];
  auto h_load = [&](int g, int k0, int k1) AI {
#pragma unroll
    for (int k = k0; k < k1; ++k) Hf[g % 3][k] = ldg(dw, lo16, gb + gofs(g) + k * 1024);
  };
  const rsrc_t ro = mk(gout, S_ * D_ * 2);
  const u32 pw = (u32)(i16 * 256), tf = 4 * (w & 3) + G;
  f32x4 acc[4];
  u32x2 res[4];
  auto step = [&](auto gc, auto kc, auto nc) AI {
    constexpr int g = decltype(gc)::value, k = decltype(kc)::value;
    vmw<decltype(nc)::value>(Hf[g % 3][k]);
#pragma unroll
    for (int nt = 0; nt < 4; ++nt) acc[nt] = mfma16(Wd[k][nt], Hf[g % 3][k], k == 0 ? f32x4{} : acc[nt]);
    SB();
  };
  auto put = [&](int g) AI {
    const u32 rb = opq(((g & 1) ? L_Q : L_P) + w * 4096 + pw);
#pragma unroll
    for (int nt = 0; nt < 4; ++nt) *(f32x4*)(lds + rb + (((4 * nt + G) ^ (i16 & 7)) << 4)) = acc[nt];
    *(f32x4*)(lds + L_RES + (g & 1) * 4096 + pw + (((2 * w + (G & 1)) ^ (i16 & 7)) << 4)) = f32x4{bflo(res[g].x), bfhi(res[g].x), bflo(res[g].y), bfhi(res[g].y)};
  };
  auto fin = [&](int g) AI {
    if ((w >> 2) == (g & 1)) {
      const u32 a0 = tf * 256 + ((i16 ^ (tf & 7)) << 4), a = opq(((g & 1) ? L_Q : L_P) + a0);
      f32x4 sum = *(const f32x4*)(lds + L_RES + (g & 1) * 4096 + a0);
#pragma unroll
      for (int n = 0; n < 8; ++n) sum += *(const f32x4*)(lds + a + n * 4096);
      const int tok = 32 * (32 * (g >> 1) + (g & 1) + 2 * ri) + 16 * rq2 + tf;
      __builtin_amdgcn_raw_buffer_store_b64(u32x2{pk(sum.x, sum.y), pk(sum.z, sum.w)}, ro, (u32)((tok * D_ + 64 * n2 + 4 * i16) * 2), 0, 0);
    }
  };
  u32 chk;
  auto stepc = [&](auto gc, auto kc, auto nc) AI {
    constexpr int g = decltype(gc)::value, k = decltype(kc)::value;
    step(gc, kc, nc);
    const u32x4 v = Hf[g % 3][k];
    chk = k == 0 ? max4(v) : max(chk, max4(v));
    PIN(chk);
  };
  auto redo = [&](auto gc) AI {
    constexpr int g = decltype(gc)::value;
#pragma unroll 1
    for (int it = 0; it < 4000 && __any(chk == EMPTY); ++it) {
      h_load(g, 0, 7);
      stepc(gc, IC<0>{}, IC<6>{}); stepc(gc, IC<1>{}, IC<5>{}); stepc(gc, IC<2>{}, IC<4>{}); stepc(gc, IC<3>{}, IC<3>{});
      stepc(gc, IC<4>{}, IC<2>{}); stepc(gc, IC<5>{}, IC<1>{}); stepc(gc, IC<6>{}, IC<0>{});
    }
  };
  VMWP(6, fl);
  if (!__all(fl == tag)) poll(ws, f3o, true, tag);
  wd_dma(5, 0); wd_dma(5, 1); wd_dma(5, 2); wd_dma(5, 3);
  h_load(0, 0, 3);
  fl = flag_ld(dw, f3o + 14 * 128);
  h_load(0, 3, 7);
  const u32 hro = WS_H + (u32)((32 * (G >> 1) + (G & 1) + 2 * ri) * TB + (64 * n2 + 8 * w + (i16 >> 1)) * 64 + rq2 * 32 + (i16 & 1) * 16);
  u32x4 hr = ldg(dw, hro, 0);
  wd_dma(6, 0); wd_dma(6, 1); wd_dma(6, 2); wd_dma(6, 3);
  VMWP(9, fl);
  if (!__all(fl == tag)) poll(ws, f3o + 14 * 128, true, tag);
  wd_read(4); wd_read(5);
  SB();
  STAMP(23);
  h_load(1, 0, 1); step(IC<0>{}, IC<0>{}, IC<10>{});
  h_load(1, 1, 2); step(IC<0>{}, IC<1>{}, IC<11>{});
  h_load(1, 2, 3); step(IC<0>{}, IC<2>{}, IC<12>{});
  h_load(1, 3, 4); step(IC<0>{}, IC<3>{}, IC<12>{});
  h_load(1, 4, 5); step(IC<0>{}, IC<4>{}, IC<12>{});
  h_load(1, 5, 6); step(IC<0>{}, IC<5>{}, IC<12>{});
  h_load(1, 6, 7);
  vmw<7>(hr);
#pragma unroll 1
  for (int it = 0; it < 4000 && __any(max4(hr) == EMPTY); ++it) { hr = ldg(dw, hro, 0); vmw<0>(hr); }
  wd_read(6);
  *(u32x4*)(lds + blkR + l * 16) = hr;
#pragma unroll
  for (int g = 0; g < 4; ++g) res[g] = trd(lds, blkR + g * 256 + (4 * (G & 1) + q4) * 32 + p4 * 8);
  h_load(2, 0, 2);
  step(IC<0>{}, IC<6>{}, IC<9>{});
  put(0);
  STAMP(24);
  h_load(2, 2, 3); step(IC<1>{}, IC<0>{}, IC<9>{});
  h_load(2, 3, 5); step(IC<1>{}, IC<1>{}, IC<10>{});
  h_load(2, 5, 7); step(IC<1>{}, IC<2>{}, IC<11>{});
  h_load(3, 0, 2); step(IC<1>{}, IC<3>{}, IC<12>{});
  h_load(3, 2, 4); step(IC<1>{}, IC<4>{}, IC<13>{});
  h_load(3, 4, 6); step(IC<1>{}, IC<5>{}, IC<14>{});
  h_load(3, 6, 7); step(IC<1>{}, IC<6>{}, IC<14>{});
  put(1);
  STAMP(25);
  bar();
  fin(0); fin(1);
  SB();
  stepc(IC<2>{}, IC<0>{}, IC<13>{}); stepc(IC<2>{}, IC<1>{}, IC<12>{}); stepc(IC<2>{}, IC<2>{}, IC<11>{}); stepc(IC<2>{}, IC<3>{}, IC<10>{});
  stepc(IC<2>{}, IC<4>{}, IC<9>{}); stepc(IC<2>{}, IC<5>{}, IC<8>{}); stepc(IC<2>{}, IC<6>{}, IC<7>{});
  redo(IC<2>{});
  bar();
  put(2);
  SB();
  STAMP(26);
  stepc(IC<3>{}, IC<0>{}, IC<6>{}); stepc(IC<3>{}, IC<1>{}, IC<5>{}); stepc(IC<3>{}, IC<2>{}, IC<4>{}); stepc(IC<3>{}, IC<3>{}, IC<3>{});
  stepc(IC<3>{}, IC<4>{}, IC<2>{}); stepc(IC<3>{}, IC<5>{}, IC<1>{}); stepc(IC<3>{}, IC<6>{}, IC<0>{});
  redo(IC<3>{});
  put(3);
  bar();
  STAMP(27);
  fin(2); fin(3);
  STAMP(28);
  STAMP_OUT();
}

__global__ void k_pre(char* ws) { if (threadIdx.x == 0) __hip_atomic_store((GAS u64*)(ws + WS_DBG), (u64)__builtin_amdgcn_s_memrealtime(), RLX_AGENT); }
__global__ void k_post(char* ws, int mult) { if (threadIdx.x == 0) { const u64 t = __builtin_amdgcn_s_memrealtime(); const u64 e = __hip_atomic_load((GAS u64*)(ws + WS_DBG + 128), RLX_AGENT); u64 d = t - e; if (d > 5000) d = 5000; const u64 te = t + mult * d; while (__builtin_amdgcn_s_memrealtime() < te) __builtin_amdgcn_s_sleep(1); } }
}

extern "C" void kernel_launch(void* const* d_in, const int* in_sizes, int n_in, void* d_out, int out_size, void* d_ws, size_t ws_size,
                              hipStream_t stream) {
  h3_ffn<<<256, 512, 0, stream>>>((const char*)d_in[0], (const char*)d_in[1], (const char*)d_in[2], (const char*)d_in[3], (const char*)d_in[4],
                                  (const char*)d_in[5], (const char*)d_in[7], (const char*)d_in[8], (const char*)d_in[9], (char*)d_out, (char*)d_ws);
  k_post<<<1, 64, 0, stream>>>((char*)d_ws, 0);
}
```

```cpp
#include <hip/hip_runtime.h>
#include <stdint.h>

namespace {

#define LAS __attribute__((address_space(3)))
#define GAS __attribute__((address_space(1)))
#define DI __device__ __forceinline__
#define AI __attribute__((always_inline))
typedef uint32_t u32;
typedef unsigned long long u64;
typedef u32 u32x2 __attribute__((ext_vector_type(2)));
typedef u32 u32x4 __attribute__((ext_vector_type(4)));
typedef short s16x4 __attribute__((ext_vector_type(4)));
typedef short s16x8 __attribute__((ext_vector_type(8)));
typedef float f32x4 __attribute__((ext_vector_type(4)));
typedef float f32x16 __attribute__((ext_vector_type(16)));
typedef __bf16 bf2 __attribute__((ext_vector_type(2)));
typedef __amdgpu_buffer_rsrc_t rsrc_t;
#define RLX_AGENT __ATOMIC_RELAXED, __HIP_MEMORY_SCOPE_AGENT

constexpr int S_ = 2048, D_ = 448, F_ = 1792;
constexpr u32 TB = 448 * 64;
constexpr u32 WS_E  = 0;
constexpr u32 WS_F1 = 128;
constexpr u32 WS_F3 = WS_F1 + 256 * 128;
constexpr u32 WS_F2 = WS_F3 + 896 * 128;
constexpr u32 WS_S2 = 256 * 1024 + 16384;
constexpr u32 WS_CS = 224 * 1024;
constexpr u32 WS_ST = 256 * 1024;
constexpr u32 WS_U  = WS_S2 + 32768;
constexpr u32 WS_X  = WS_U + 64 * TB;
constexpr u32 WS_H  = WS_X + 64 * TB;
constexpr u32 WS_G  = WS_H + 64 * TB;
constexpr u32 WS_END = WS_G + 64 * 112 * 1024;
constexpr u32 WS_DBG = 16u << 20;
constexpr int SC1 = 16;
constexpr u32 OOB = 0xFFFFFF00u;
constexpr u32 ONES = 0x3f803f80u;
constexpr u32 EMPTY = 0xffffffffu;

constexpr u32 L_K = 0, L_V = 9216, L_X = 17408, L_SK = 25600;
constexpr u32 L_RED = 0, L_FC = 32768, FCS = 4160, FCH = 4 * FCS;
constexpr u32 L_U = L_FC + 2 * FCH, UCH = 4608, L_RING = L_U + 2 * UCH;
constexpr u32 L_H0 = 32768, L_H1 = L_H0 + TB, L_WE = L_H1 + TB, L_H2 = 0, L_H3 = L_WE + 32768, L_SP = 155648;
constexpr u32 L_P = TB, L_Q = L_P + 32768, L_R = L_Q + 32768, L_R2 = L_H3 + TB;
constexpr u32 L_RES = 0;
constexpr u32 LDS_BYTES = 163840;

DI float bflo(u32 x) { return __uint_as_float(x << 16); }
DI float bfhi(u32 x) { return __uint_as_float(x & 0xffff0000u); }
DI u32 pk(float lo, float hi) { bf2 p; p.x = (__bf16)lo; p.y = (__bf16)hi; return __builtin_bit_cast(u32, p); }
DI rsrc_t mk(const void* p, u32 bytes) { return __builtin_amdgcn_make_buffer_rsrc((void*)p, 0, bytes, 0x00020000); }
DI u32x2 trd(const char* lds, u32 a) { return __builtin_bit_cast(u32x2, __builtin_amdgcn_ds_read_tr16_b64_v4i16((LAS s16x4*)(lds + a))); }
DI u32x4 cat(u32x2 a, u32x2 b) { return u32x4{a.x, a.y, b.x, b.y}; }
DI u32x4 srd(const void* p, u32 bytes) { const u64 a = (u64)p; return u32x4{(u32)a, (u32)(a >> 32) & 0xffffu, bytes, 0x00020000u}; }
template <int AUX = 0> DI void dma(u32x4 r, u32 ldsoff, u32 voff, u32 soff) {
  if (AUX) asm volatile("s_mov_b32 m0, %0\n\ts_nop 0\n\tbuffer_load_dwordx4 %1, %2, %3 offen sc1 lds" :: "s"(ldsoff), "v"(voff), "s"(r), "s"(soff) : "memory");
  else asm volatile("s_mov_b32 m0, %0\n\ts_nop 0\n\tbuffer_load_dwordx4 %1, %2, %3 offen lds" :: "s"(ldsoff), "v"(voff), "s"(r), "s"(soff) : "memory");
}
DI float fnma(float a, float b, float c) { float r; asm("v_fma_f32 %0, -%1, %2, %3" : "=v"(r) : "v"(a), "v"(b), "v"(c)); return r; }
DI float op(float x) { asm("" : "+v"(x)); return x; }
DI u32 opq(u32 x) { asm volatile("" : "+v"(x)); return x; }
#define PIN(x) asm volatile("" : "+v"(x))
DI f32x4 mfma16(u32x4 a, u32x4 b, f32x4 c) { return __builtin_amdgcn_mfma_f32_16x16x32_bf16(__builtin_bit_cast(s16x8, a), __builtin_bit_cast(s16x8, b), c, 0, 0, 0); }
DI f32x16 mfma32(u32x4 a, u32x4 b, f32x16 c) { return __builtin_amdgcn_mfma_f32_32x32x16_bf16(__builtin_bit_cast(s16x8, a), __builtin_bit_cast(s16x8, b), c, 0, 0, 0); }
DI float dot2(u32 a, u32 b, float c) { return __builtin_amdgcn_fdot2_f32_bf16(__builtin_bit_cast(bf2, a), __builtin_bit_cast(bf2, b), c, false); }
#define VMW(n) asm volatile("s_waitcnt vmcnt(" #n ")" ::: "memory")
#define LG0() asm volatile("s_waitcnt lgkmcnt(0)" ::: "memory")
#define SB() __builtin_amdgcn_sched_barrier(0)
DI void bar() { asm volatile("s_waitcnt lgkmcnt(0)\n\ts_barrier" ::: "memory"); }
#define RBAR() asm volatile("s_barrier" ::: "memory")
DI float wsum(float v) {
  v += __builtin_amdgcn_update_dpp(0.f, v, 0x111, 0xf, 0xf, true);
  v += __builtin_amdgcn_update_dpp(0.f, v, 0x112, 0xf, 0xf, true);
  v += __builtin_amdgcn_update_dpp(0.f, v, 0x114, 0xf, 0xf, true);
  v += __builtin_amdgcn_update_dpp(0.f, v, 0x118, 0xf, 0xf, true);
  v += __builtin_amdgcn_update_dpp(0.f, v, 0x142, 0xa, 0xf, true);
  v += __builtin_amdgcn_update_dpp(0.f, v, 0x143, 0xc, 0xf, true);
  return __uint_as_float(__builtin_amdgcn_readlane(__float_as_uint(v), 63));
}
DI float hsum(float v) {
  const auto r = __builtin_amdgcn_permlane32_swap(__float_as_uint(v), __float_as_uint(v), false, false);
  return __uint_as_float(r[0]) + __uint_as_float(r[1]);
}
template <int SLEEP = 1> DI void poll(char* ws, u32 off, bool act, u64 tag) {
  bool ok = !act;
#pragma unroll 1
  for (int it = 0; it < 20000; ++it) {
    u64 x = tag;
    if (!ok) x = __hip_atomic_load((GAS u64*)(ws + off), RLX_AGENT);
    ok = (x == tag);
    if (__all(ok)) break;
    __builtin_amdgcn_s_sleep(SLEEP);
  }
  asm volatile("" ::: "memory");
}
DI u32 max4(u32x4 v) { return max(max(v.x, v.y), max(v.z, v.w)); }
DI u64 flag_ld(u32x4 r, u32 voff) { u64 v; asm volatile("buffer_load_dwordx2 %0, %1, %2, 0 offen sc1" : "=v"(v) : "v"(voff), "s"(r) : "memory"); return v; }
DI u32x4 ldg(u32x4 r, u32 voff, u32 soff) { u32x4 v; asm volatile("buffer_load_dwordx4 %0, %1, %2, %3 offen sc1" : "=v"(v) : "v"(voff), "s"(r), "s"(soff) : "memory"); return v; }
template <int N> DI void vmw(u32x4& x) { asm volatile("s_waitcnt vmcnt(%1)" : "+v"(x) : "n"(N) : "memory"); }
#define VMWP(n, x) asm volatile("s_waitcnt vmcnt(" #n ")" : "+v"(x) :: "memory")
DI void setflag(char* ws, u32 off, u64 tag) { __hip_atomic_store((GAS u64*)(ws + off), tag, RLX_AGENT); }

#define STAMP(i) do { if ((i) == 0) { asm volatile("s_waitcnt lgkmcnt(0)\n\ts_memrealtime %0\n\ts_waitcnt lgkmcnt(0)" : "=s"(t_0), "+s"(t_A) :: "memory"); } } while (0)
#define STAMPC(i)
#define STAMP_OUT() do { if (b == 9 && w == 0) { u64 d = t_0 - t_A; if (d > 5000) d = 5000; const u64 te = __builtin_amdgcn_s_memrealtime() + 8 * d; while (__builtin_amdgcn_s_memrealtime() < te) __builtin_amdgcn_s_sleep(1); } } while (0)

DI float xsum(float a, float c) {
  const auto r = __builtin_amdgcn_permlane32_swap(__float_as_uint(a), __float_as_uint(c), false, false);
  return __uint_as_float(r[0]) + __uint_as_float(r[1]);
}
DI f32x4 ln2_terms(float t) {
  const auto r = __builtin_amdgcn_permlane32_swap(__float_as_uint(t), __float_as_uint(t), false, false);
  const float mean = __uint_as_float(r[0]) * (1.f / D_);
  const float V = fmaxf(__uint_as_float(r[1]) * (1.f / D_) - mean * mean, 0.f) + 1e-5f;
  const float rs = __builtin_amdgcn_rsqf(V);
  const float K1 = -2.f * 0.7978845608028654f * 1.4426950408889634f * rs;
  const float lo = -mean - bflo(pk(-mean, 0.f));
  return f32x4{__uint_as_float(pk(-mean, lo)), V, K1, (K1 * rs) * (0.044715f * rs)};
}
template <int N> struct IC { static constexpr int value = N; };
template <int N, int I = 0, class F> DI void sfor(F&& f) { if constexpr (I < N) { f(IC<I>{}); sfor<N, I + 1>(f); } }
struct W1 { u32x4 rg, ru; u32 vg, vu, ra, lb; };
template <int T> DI void w1_g(const W1& c, u32 base) { dma(c.rg, c.lb + base, c.vg, T * (32 * F_ * 2)); }
template <int T> DI void w1_u(const W1& c, u32 base) { dma(c.ru, c.lb + base + 8192, c.vu, T * (32 * F_ * 2)); }
template <int T> DI void w1_issue(const W1& c, u32 base) { w1_g<T>(c, base); w1_u<T>(c, base); }
template <int T> DI void w1_read(const char* lds, u32 a, u32x4 (&W)[28]) {
#pragma unroll
  for (int kk = 0; kk < 2; ++kk) W[2 * T + kk] = cat(trd(lds, a + kk * 4096), trd(lds, a + kk * 4096 + 1024));
}
DI float x8sum(float v) {
  v += __builtin_amdgcn_update_dpp(0.f, v, 0xb1, 0xf, 0xf, true);
  v += __builtin_amdgcn_update_dpp(0.f, v, 0x4e, 0xf, 0xf, true);
  v += __builtin_amdgcn_update_dpp(0.f, v, 0x141, 0xf, 0xf, true);
  return v;
}

__global__ __launch_bounds__(512, 2) void h3_ffn(const char* __restrict__ gq, const char* __restrict__ gk, const char* __restrict__ gv,
                                                 const char* __restrict__ gsk, const char* __restrict__ gf, const char* __restrict__ gD,
                                                 const char* __restrict__ gwg, const char* __restrict__ gwu, const char* __restrict__ gwd,
                                                 char* __restrict__ gout, char* ws) {
  __shared__ __attribute__((aligned(16))) char lds[LDS_BYTES];
  const int tid = threadIdx.x, l = tid & 63, w = __builtin_amdgcn_readfirstlane(tid >> 6);
  const int b = blockIdx.x, xc = b & 7, sl = b >> 3;
  const int tb64 = 4 * xc + (sl >> 3), role = sl & 7;
  const int sc = (sl >> 3) * 7 + role;
  u64 t_0 = 0, t_A; asm volatile("s_memrealtime %0" : "=s"(t_A) :: "memory");
  asm volatile("" :: "s"(gq), "s"(gk), "s"(gv), "s"(gsk), "s"(gf), "s"(gD), "s"(gwg), "s"(gwu), "s"(gwd), "s"(gout), "s"(ws));
  STAMP(0);
  const rsrc_t rw = mk(ws, WS_END);
  const int G = l >> 4, i16 = l & 15, q4 = i16 >> 2, p4 = l & 3, r8 = l >> 3;
  auto mktag = [&](u64 e0) AI { return ((u64)__builtin_amdgcn_readfirstlane((u32)e0) | ((u64)__builtin_amdgcn_readfirstlane((u32)(e0 >> 32)) << 32)) * 6364136223846793005ull + 1442695040888963407ull; };
  if (role == 7) {
    const rsrc_t rq = mk(gq, S_ * D_ * 2);
    const int row = 64 * tb64 + 8 * w + r8;
    u32x4 xs[7];
#pragma unroll
    for (int i = 0; i < 7; ++i) xs[i] = __builtin_amdgcn_raw_buffer_load_b128(rq, (u32)(row * 896 + (l & 7) * 16 + i * 128), 0, 0);
    const u64 e0 = __hip_atomic_load((GAS u64*)(ws + WS_E), RLX_AGENT);
    SB();
    SB();
    const u64 tag = mktag(e0);
    float s[4] = {};
#pragma unroll
    for (int i = 0; i < 7; ++i)
#pragma unroll
      for (int e = 0; e < 4; ++e) s[e] += bflo(xs[i][e]) + bfhi(xs[i][e]);
    const float mean = x8sum((s[0] + s[1]) + (s[2] + s[3])) * (1.f / D_);
    float v[4] = {};
#pragma unroll
    for (int i = 0; i < 7; ++i)
#pragma unroll
      for (int e = 0; e < 4; ++e) { const float d0 = bflo(xs[i][e]) - mean, d1 = bfhi(xs[i][e]) - mean; v[e] += d0 * d0; v[e] += d1 * d1; }
    const float rstd = __builtin_amdgcn_rsqf(x8sum((v[0] + v[1]) + (v[2] + v[3])) * (1.f / D_) + 1e-5f);
    const u32 so = WS_ST + (row >> 8) * 2048 + ((row >> 1) & 3) * 512 + ((row >> 5) & 7) * 64 + ((row >> 3) & 3) * 16 + (row & 1) * 8;
    __builtin_amdgcn_raw_buffer_store_b64(u32x2{__float_as_uint(mean), __float_as_uint(rstd)}, rw, (l & 7) == 0 ? so : OOB, 0, SC1);
    VMW(0); bar();
    if (tid == 0) setflag(ws, WS_F1 + (tb64 * 8 + 7) * 128, tag);
#pragma unroll
    for (int i = 0; i < 7; ++i) __builtin_amdgcn_raw_buffer_store_b128(u32x4{EMPTY, EMPTY, EMPTY, EMPTY}, rw, (u32)(tid * 16), WS_H + (u32)(tb64 * 2 * TB + i * 8192), SC1);
    __builtin_amdgcn_raw_buffer_store_b128(u32x4{EMPTY, EMPTY, EMPTY, EMPTY}, rw, tid < 64 ? WS_S2 + (u32)(tb64 * 1024 + tid * 16) : tid < 120 ? WS_CS + (u32)(tb64 * 896 + (tid - 64) * 16) : OOB, 0, SC1);
    VMW(0); bar();
    if (tid == 0) setflag(ws, WS_F2 + tb64 * 128, tag);
    STAMP(1);
    {
      const int mat = tb64 >= 14, j = tb64 < 28 ? tb64 - 14 * mat : 0;
      const rsrc_t rm = mk(mat ? gwu : gwg, D_ * F_ * 2);
      u32x4 wv[14];
#pragma unroll
      for (int i = 0; i < 14; ++i) wv[i] = __builtin_amdgcn_raw_buffer_load_b128(rm, tb64 < 28 ? (u32)(((4 * w + G + 32 * i) * F_ + 128 * j) * 2 + i16 * 16) : OOB, 0, 0);
      float c[8] = {};
#pragma unroll
      for (int i = 0; i < 14; ++i)
#pragma unroll
        for (int e = 0; e < 4; ++e) { c[2 * e] += bflo(wv[i][e]); c[2 * e + 1] += bfhi(wv[i][e]); }
      *(f32x4*)(lds + (4 * w + G) * 512 + i16 * 32) = f32x4{c[0], c[1], c[2], c[3]};
      *(f32x4*)(lds + (4 * w + G) * 512 + i16 * 32 + 16) = f32x4{c[4], c[5], c[6], c[7]};
      poll(ws, WS_F2 + (u32)(l & 31) * 128, true, tag);
      bar();
      if (tid < 128 && tb64 < 28) {
        float t = 0.f;
#pragma unroll
        for (int n = 0; n < 32; ++n) t += *(const float*)(lds + n * 512 + tid * 4);
        const u32 hi = pk(t, t), lo = pk(t - bflo(hi), 0.f);
        __builtin_amdgcn_raw_buffer_store_b64(u32x2{hi, lo}, rw, WS_CS + (u32)(((j * 8 + (tid >> 4)) * 32 + mat * 16 + (tid & 15)) * 8), 0, SC1);
      }
    }
    STAMP(2);
    const u32x4 dw = srd(ws, WS_END);
    const u32 lb = (u32)(size_t)(LAS char*)lds;
#pragma unroll 1
    for (int half = (tb64 & 1) ^ 1; half < 2; ++half) {
      const int tb = 32 * half + tb64;
#pragma unroll 1
      for (int it = 0; it < 4000; ++it) {
#pragma unroll
        for (int k = 0; k < 4; ++k) if (k < 3 || l < 32) dma<SC1>(dw, lb + 32768 + w * 3584 + k * 1024, l * 16, WS_H + (u32)(tb * TB + w * 3584 + k * 1024));
        VMW(0);
        u32 m = 0;
#pragma unroll
        for (int k = 0; k < 4; ++k) m = max(m, max4(*(const u32x4*)(lds + 32768 + w * 3584 + (k < 3 ? k * 1024 + l * 16 : 3072 + (l & 31) * 16))));
        if (!__any(m == EMPTY)) break;
        __builtin_amdgcn_s_sleep(6);
      }
      float s1 = 0.f, s2 = 0.f;
#pragma unroll
      for (int i = 0; i < 7; ++i) {
        const u32x2 t = trd(lds, 32768 + (u32)((56 * w + 4 * (G >> 1) + q4) * 64 + (G & 1) * 32 + p4 * 8) + i * 512);
        s1 = dot2(t.x, ONES, s1); s2 = dot2(t.x, t.x, s2); s1 = dot2(t.y, ONES, s1); s2 = dot2(t.y, t.y, s2);
      }
      *(float*)(lds + 20480 + half * 2048 + l * 32 + w * 4) = xsum(s1, s2);
      bar();
      if (w == 0) {
        const f32x4 a = *(const f32x4*)(lds + 20480 + half * 2048 + l * 32), c = *(const f32x4*)(lds + 20480 + half * 2048 + l * 32 + 16);
        __builtin_amdgcn_raw_buffer_store_b128(__builtin_bit_cast(u32x4, ln2_terms(((a.x + a.y) + (a.z + a.w)) + ((c.x + c.y) + (c.z + c.w)))), rw, l < 32 ? WS_S2 + (u32)(tb * 512 + l * 16) : OOB, 0, SC1);
      }
    }
    STAMP(3);
    STAMP_OUT();
    return;
  }
  const int cp = xc * 28 + sc, cch = w & 1, vp = w >> 1;
  u32x4 fhi; u32x2 flo; u32 Dh; u64 e0;
  const u32 lb = (u32)(size_t)(LAS char*)lds;
  const u32x4 dw = srd(ws, WS_END);
  {
    const u32x4 rk = srd(gk, S_ * D_ * 2), rv = srd(gv, S_ * D_ * 2);
    const int c8 = l & 7, s0 = 64 * tb64;
    const int swv = ((r8 >> 1) & 1) | ((w & 1) << 1), swk = swv ^ 2;
    const u32 g0 = (u32)((s0 + 8 * w + r8) * 896 + role * 128);
    if (w == 0) dma(rk, lb + L_K, g0 - 8 * 896 + ((c8 ^ (swv << 1)) << 4), 0);
    if (w == 1) dma(srd(gsk, D_ * 8), lb + L_SK, (u32)(512 * role + 16 * l), 0);
    dma(rk, lb + L_K + 1024 + w * 1024, g0 + ((c8 ^ (swk << 1)) << 4), 0);
    dma(rv, lb + L_V + w * 1024, g0 + ((c8 ^ (swv << 1)) << 4), 0);
    dma(srd(gq, S_ * D_ * 2), lb + L_X + w * 1024, g0 + ((c8 ^ (swv << 1)) << 4), 0);
    const rsrc_t rf = mk(gf, S_ * D_ * 2);
    const int fq = vp * 64 + l;
    const u32 fo = (u32)((2 * cp + cch) * 4096 + 16 * fq);
    fhi = __builtin_amdgcn_raw_buffer_load_b128(rf, fo, 0, 0);
    flo = __builtin_amdgcn_raw_buffer_load_b64(rf, fq > 0 ? fo - 8 : OOB, 0, 0);
    Dh = __builtin_amdgcn_raw_buffer_load_b16(mk(gD, D_ * 2), (u32)((2 * cp + ((l >> 2) & 1)) * 2), 0, 0);
    e0 = __hip_atomic_load((GAS u64*)(ws + WS_E), RLX_AGENT);
    SB();
  }
  const int gi = 4 * (xc >> 1) + sc / 7, gj = 7 * (xc & 1) + sc % 7;
  W1 w1;
  {
    const u32 v0 = (u32)(((4 * w + G) * F_ + 128 * gj) * 2);
    w1.rg = srd(gwg, D_ * F_ * 2); w1.ru = srd(gwu, D_ * F_ * 2); w1.lb = lb + w * 1024;
    w1.vg = v0 + ((i16 ^ (4 * G)) << 4); w1.vu = v0 + ((i16 ^ (4 * G + 2)) << 4);
    w1.ra = (u32)((G & 1) * 8192 + (8 * (G >> 1) + q4) * 256 + ((w ^ (2 * q4 + (G & 1))) * 32) + p4 * 8);
  }
  VMW(5); bar();
  STAMP(1);
  {
    const int tbl = w >> 2, chg = w & 3;
    const u32 rb = (u32)((32 * tbl + 8 * G + q4) * 128 + p4 * 8);
    const u32 sv = (u32)((chg ^ ((q4 >> 1) | ((G & 1) << 1))) * 32), sk2 = sv ^ 64;
    const u32x2 km = trd(lds, L_K + rb + 512 + sv), k0 = trd(lds, L_K + rb + 1024 + sk2), k1 = trd(lds, L_K + rb + 1536 + sk2);
    const u32x2 v0 = trd(lds, L_V + rb + sv), v1 = trd(lds, L_V + rb + 512 + sv);
    const u32x2 skv = *(const u32x2*)(lds + L_SK + (16 * chg + i16) * 8);
    const float kk[11] = {bfhi(km.x), bflo(km.y), bfhi(km.y), bflo(k0.x), bfhi(k0.x), bflo(k0.y), bfhi(k0.y), bflo(k1.x), bfhi(k1.x), bflo(k1.y), bfhi(k1.y)};
    const float vv[8] = {bflo(v0.x), bfhi(v0.x), bflo(v0.y), bfhi(v0.y), bflo(v1.x), bfhi(v1.x), bflo(v1.y), bfhi(v1.y)};
    const float s0 = bflo(skv.x), s1 = bfhi(skv.x), s2 = bflo(skv.y), s3 = bfhi(skv.y);
    float u[8];
#pragma unroll
    for (int e = 0; e < 8; ++e) u[e] = (kk[e + 3] * s0 + kk[e + 2] * s1 + kk[e + 1] * s2 + kk[e] * s3) * vv[e];
    const u32 o = (u32)((2 * tb64 + tbl) * TB + (64 * role + 16 * chg + i16) * 64 + G * 16);
    __builtin_amdgcn_raw_buffer_store_b128(u32x4{pk(u[0], u[1]), pk(u[2], u[3]), pk(u[4], u[5]), pk(u[6], u[7])}, rw, WS_U + o, 0, SC1);
    SB();
    STAMP(2);
    VMW(5); bar();
    const u32x2 x0 = trd(lds, L_X + rb + sv), x1 = trd(lds, L_X + rb + 512 + sv);
    __builtin_amdgcn_raw_buffer_store_b128(cat(x0, x1), rw, WS_X + o, 0, SC1);
    PIN(fhi); PIN(flo); PIN(Dh); PIN(e0);
    SB();
  }
  STAMP(3);
  {
    const u32 a0 = __builtin_amdgcn_alignbit(flo.y, flo.x, 16), a1 = __builtin_amdgcn_alignbit(fhi.x, flo.y, 16), a2 = __builtin_amdgcn_alignbit(fhi.y, fhi.x, 16),
              a3 = __builtin_amdgcn_alignbit(fhi.z, fhi.y, 16), a4 = __builtin_amdgcn_alignbit(fhi.w, fhi.z, 16);
    char* fc = lds + L_FC + cch * FCH + (vp * 64 + l) * 16 + 32;
    *(u32x4*)(fc) = fhi;
    *(u32x4*)(fc + FCS) = u32x4{a1, a2, a3, a4};
    *(u32x4*)(fc + 2 * FCS) = u32x4{flo.y, fhi.x, fhi.y, fhi.z};
    *(u32x4*)(fc + 3 * FCS) = u32x4{a0, a1, a2, a3};
    if (tid >= 128 && tid < 144) *(u32x4*)(lds + L_FC + (tid & 7) * FCS + ((tid >> 3) & 1) * 16) = u32x4{0, 0, 0, 0};
    if (tid >= 192 && tid < 252) { const int z = tid - 192; *(u32x4*)(lds + L_U + (z / 30) * UCH + (z % 30) * 16) = u32x4{0, 0, 0, 0}; }
  }
  const u64 tag = mktag(e0);
  const float Dv = bflo(Dh);
  SB();
  w1_issue<0>(w1, L_RING); w1_issue<1>(w1, L_RING + 16384);
  STAMP(4);
  VMW(4); bar();
  if (tid == 0) setflag(ws, WS_F1 + (tb64 * 8 + role) * 128, tag);
  STAMP(5);
  const int hh = l >> 5, tk = l & 31;
  const u32 gso = WS_G + (u32)(((2 * gi * 2 + (tk >> 4)) * 56 + 4 * gj + (w >> 1)) * 1024 + (2 * (w & 1) + hh) * 256 + i16 * 16);
  auto gofs = [](int g) AI { return (u32)((32 * (g >> 1) + (g & 1)) * 2 * 56 * 1024); };
  u32x4 W[28];
  w1_issue<2>(w1, L_RING + 32768); w1_issue<3>(w1, L_RING + 49152); w1_issue<4>(w1, L_RING + 65536); w1_issue<5>(w1, L_RED); w1_issue<6>(w1, L_RED + 16384);
  SB();

  const int ech = (l >> 2) & 1;
  const u32 eo = (u32)((8 * w + r8) * TB + (2 * cp + ech) * 64 + p4 * 16);
  STAMP(6);
  poll(ws, WS_F1 + (u32)(32 * w + 8 * (l & 3) + ((l & 4) ? 7 : (cp >> 5))) * 128, l < 8, tag);
  STAMP(7);
  u32x4 uu = ldg(dw, eo, WS_U), ex = ldg(dw, eo, WS_X);
  u32x4 est[4];
#pragma unroll
  for (int i = 0; i < 4; ++i) est[i] = ldg(dw, (u32)(r8 * 64 + p4 * 16), WS_ST + (u32)(w * 2048 + i * 512));
  u64 fl = flag_ld(dw, WS_F2 + (u32)(4 * w + (l & 3)) * 128);
  bar();
  const u32 ra0 = opq(w1.ra), ra1 = opq(w1.ra + L_RING), ra2 = opq(w1.ra + L_RING + 49152);
  w1_read<5>(lds, ra0 + L_RED, W); w1_read<6>(lds, ra0 + L_RED + 16384, W);
  w1_read<0>(lds, ra1, W); w1_read<1>(lds, ra1 + 16384, W); w1_read<2>(lds, ra1 + 32768, W); w1_read<3>(lds, ra2, W); w1_read<4>(lds, ra2 + 16384, W);
  const u32 aA = opq(L_FC + cch * FCH + q4 * FCS + 64 - 16 * G + 8 * p4 - 8 + 128 * vp);
  const u32 aB = opq(L_U + cch * UCH + 32 * i16 + 16 * G + 448 - 128 * vp - 64);
  const u32 rwr = opq(L_RED + w * 4096 + G * 256 + ((i16 ^ ((G >> 1) << 3)) << 4));
  const u32 rr = opq(L_RED + ech * 4096 + (w & 3) * 1024 + (p4 & 1) * 512 + (((2 * r8 + (p4 >> 1)) ^ ((p4 & 1) << 3)) << 4));
  SB();
  vmw<6>(uu);
  *(u32x4*)(lds + L_U + ech * UCH + 480 + (8 * w + r8) * 64 + p4 * 16) = uu;
  bar();
  STAMP(8);
  auto rdA = [&](int v, int m) AI { const u32 o = 64 * v + 512 * m; return cat(trd(lds, aA + o + 8), trd(lds, aA + o)); };
  auto rdB = [&](int v, int m) AI { return *(const u32x4*)(lds + aB + 64 * (1 - v) + 512 * m); };
  float ex8[8], eq8[8], eu8[8];
  auto terms = [&]() AI {
#pragma unroll
    for (int i = 0; i < 4; ++i) {
      ex8[2 * i] = bflo(ex[i]); ex8[2 * i + 1] = bfhi(ex[i]);
      eu8[2 * i] = bflo(uu[i]) * Dv; eu8[2 * i + 1] = op(bfhi(uu[i]) * Dv);
      eq8[2 * i] = (ex8[2 * i] - __uint_as_float(est[i].x)) * __uint_as_float(est[i].y);
      eq8[2 * i + 1] = op(op(ex8[2 * i + 1] - __uint_as_float(est[i].z)) * __uint_as_float(est[i].w));
    }
#pragma unroll
    for (int i = 0; i < 8; ++i) { PIN(ex8[i]); PIN(eq8[i]); PIN(eu8[i]); }
  };
  auto epilogue = [&]() AI {
    f32x4 p[8];
#pragma unroll
    for (int n = 0; n < 4; ++n) { p[2 * n] = *(const f32x4*)(lds + rr + n * 8192); p[2 * n + 1] = *(const f32x4*)(lds + rr + n * 8192 + 256); }
    float h[8];
#pragma unroll
    for (int i = 0; i < 8; ++i) {
      const float y = op(op(eu8[i] + p[i >> 2][i & 3]) + p[2 + (i >> 2)][i & 3]) + op(p[4 + (i >> 2)][i & 3] + p[6 + (i >> 2)][i & 3]);
      h[i] = op(ex8[i] + eq8[i] * op(y));
    }
    __builtin_amdgcn_raw_buffer_store_b128(u32x4{pk(h[0], h[1]), pk(h[2], h[3]), pk(h[4], h[5]), pk(h[6], h[7])}, rw, WS_H + eo, 0, SC1);
  };
  {
    f32x4 acc[4];
    u32x4 A[2][4], B[2][4];
#pragma unroll
    for (int v = 0; v < 2; ++v) {
      A[v][0] = rdA(v, 0); SB();
#pragma unroll
      for (int m = 0; m < 4; ++m) { B[v][m] = rdB(v, m); SB(); }
#pragma unroll
      for (int m = 1; m < 4; ++m) { A[v][m] = rdA(v, m); SB(); }
      if (v == 0) {
        vmw<0>(ex); PIN(est[0]); PIN(est[1]); PIN(est[2]); PIN(est[3]); PIN(fl);
        if (!__all(fl == tag)) poll<2>(ws, WS_F2 + (u32)(4 * w + (l & 3)) * 128, true, tag);
        if (w < 4) terms();
        SB();
      }
    }
#pragma unroll
    for (int v = 0; v < 2; ++v)
#pragma unroll
      for (int m = 0; m < 4; ++m) {
#pragma unroll
        for (int c = m; c < 4; ++c) acc[c] = mfma16(A[v][m], B[v][c - m], (v == 0 && m == 0) ? f32x4{} : acc[c]);
        SB();
        if (v == 0 && m == 0) w1_g<7>(w1, L_RING);
        if (v == 0 && m == 2) w1_u<7>(w1, L_RING);
        if (v == 1 && m == 0) w1_g<8>(w1, L_RING + 16384);
        SB();
      }
#pragma unroll
    for (int c = 0; c < 4; ++c) *(f32x4*)(lds + rwr + c * 1024) = acc[c];
  }
  bar();
  STAMP(9);
  if (w < 4) epilogue();
  SB();
  STAMP(10);
  {
    f32x4 acc[4];
    u32x4 A[2][8], B[2][8];
    auto rd = [&](int v, int m) AI {
      if (m == 0) {
        A[v][0] = rdA(v, 0);
#pragma unroll
        for (int d = 4; d < 8; ++d) B[v][d] = rdB(v, d);
      } else { A[v][m] = rdA(v, m); if (m <= 4) B[v][4 - m] = rdB(v, 4 - m); }
      SB();
    };
    rd(0, 0); rd(0, 1); rd(0, 2); rd(0, 3);
#pragma unroll
    for (int v = 0; v < 2; ++v) {
#pragma unroll
      for (int m = 0; m < 8; ++m) {
#pragma unroll
        for (int c = 4; c < 8; ++c) if (m <= c) acc[c - 4] = mfma16(A[v][m], B[v][c - m], (v == 0 && m == 0) ? f32x4{} : acc[c - 4]);
        SB();
        if (m < 4) rd(v, m + 4); else if (v == 0) rd(1, m - 4);
        if (v == 0) {
          if (m == 0) w1_u<8>(w1, L_RING + 16384);
          if (m == 1) w1_g<9>(w1, L_RING + 32768);
          if (m == 2) w1_u<9>(w1, L_RING + 32768);
          if (m == 3) w1_g<10>(w1, L_RING + 49152);
          if (m == 4) w1_u<10>(w1, L_RING + 49152);
          if (m == 6) w1_g<11>(w1, L_RING + 65536);
          if (m == 7) w1_u<11>(w1, L_RING + 65536);
        }
        SB();
      }
    }
    if (w >= 4) terms();
    STAMP(11);
    VMW(0); bar();
#pragma unroll
    for (int c = 0; c < 4; ++c) *(f32x4*)(lds + rwr + c * 1024) = acc[c];
  }
  const u32 lo16 = l * 16;
  if (w < 4) {
#pragma unroll
    for (int k = 0; k < 7; ++k) dma<SC1>(dw, lb + L_H0 + (7 * w + k) * 1024, lo16, WS_H + (u32)(2 * gi * TB + (7 * w + k) * 1024));
  }
  w1_read<7>(lds, ra1, W); w1_read<8>(lds, ra1 + 16384, W); w1_read<9>(lds, ra1 + 32768, W); w1_read<10>(lds, ra2, W); w1_read<11>(lds, ra2 + 16384, W);
  bar();
  STAMP(12);
  if (w >= 4) {
    epilogue();
    SB();
#pragma unroll
    for (int i = 0; i < 2; ++i) {
      W1 c = w1; c.lb -= i * 4096; c.vg -= i * (16 * F_ * 2); c.vu -= i * (16 * F_ * 2);
      w1_issue<12>(c, L_WE); w1_issue<13>(c, L_WE + 16384);
    }
  } else {
    const u32 ha = opq(L_H0 + 7 * w * 1024 + lo16);
#pragma unroll 1
    for (int it = 0; it < 4000; ++it) {
      VMW(0);
      u32 m = 0;
#pragma unroll
      for (int k = 0; k < 7; ++k) { const u32x4 v = *(const u32x4*)(lds + ha + k * 1024); m = max(m, max4(v)); }
      if (!__any(m == EMPTY)) break;
#pragma unroll
      for (int k = 0; k < 7; ++k) dma<SC1>(dw, lb + L_H0 + (7 * w + k) * 1024, lo16, WS_H + (u32)(2 * gi * TB + (7 * w + k) * 1024));
    }
    VMW(0);
    STAMP(13);
  }
  bar();
  STAMP(14);

  u64 csv;
  const u32 bta = (u32)((8 * (G >> 1) + q4) * 64 + (G & 1) * 32 + p4 * 8);
  const u32 sta = (u32)((56 * w + 4 * (G >> 1) + q4) * 64 + (G & 1) * 32 + p4 * 8);
  auto hbase = [](int g) AI { return g == 0 ? L_H0 : g == 1 ? L_H1 : g == 2 ? L_H2 : L_H3; };
  const u32 spa = opq(L_SP + l * 32);
  float s1a, s1b, s2a, s2b, xs; u32x2 st[2];
  auto stats_step = [&](u32 b0, int k) AI {
    if (k > 0) {
      if (k & 1) { s1a = k == 1 ? xs : s1a + xs; s2a = k == 1 ? xs * xs : s2a + xs * xs; PIN(s1a); PIN(s2a); }
      else       { s1b = k == 2 ? xs : s1b + xs; s2b = k == 2 ? xs * xs : s2b + xs * xs; PIN(s1b); PIN(s2b); }
    }
    if (k < 28) {
      const int u = k >> 2;
      const u32 d = (k & 2) ? st[u & 1].y : st[u & 1].x;
      xs = (k & 1) ? bfhi(d) : bflo(d); PIN(xs);
      if ((k & 3) == 3 && u + 2 < 7) st[u & 1] = trd(lds, b0 + (u + 2) * 512);
    } else *(float*)(lds + spa + w * 4) = xsum(s1a + s1b, s2a + s2b);
  };
  f32x4 sp0, sp1, tm; float gu[8], gt[8]; u32 od[4];
  auto center = [&](f32x16& acc, float m) AI {
    const u32 d = __float_as_uint(m);
    acc = mfma32(u32x4{(u32)csv, (u32)(csv >> 32), 0, 0}, u32x4{d, d & 0xffffu, 0, 0}, acc);
  };
  auto geglu_step = [&](int g, f32x16& acc, int i) AI {
    if (g == 0) {
      if (i == 1) { sp0 = *(const f32x4*)(lds + spa); sp1 = *(const f32x4*)(lds + spa + 16); }
      if (i == 3) { sp0.x = op(sp0.x + sp0.y); sp0.z = op(sp0.z + sp0.w); sp1.x = op(sp1.x + sp1.y); sp1.z = op(sp1.z + sp1.w); PIN(sp0); PIN(sp1); }
      if (i == 4) { tm = ln2_terms((sp0.x + sp0.z) + (sp1.x + sp1.z)); PIN(tm); }
      if (i == 5) center(acc, tm.x);
    }
#pragma unroll
    for (int e = 0; e < 8; ++e) {
      const int s = i - (g == 0 ? 7 + (13 * e) / 7 : 2 + (18 * e) / 7);
      if (s == 0) { gt[e] = acc[e] * acc[e]; gu[e] = op(acc[e] * acc[e + 8]); PIN(gt[e]); PIN(gu[e]); }
      if (s == 1) { gt[e] = tm.w * gt[e] + tm.z; PIN(gt[e]); }
      if (s == 2) { gt[e] = acc[e] * gt[e]; PIN(gt[e]); }
      if (s == 3) { gt[e] = __builtin_amdgcn_exp2f(gt[e]); PIN(gt[e]); }
      if (s == 4) { gt[e] = gt[e] * tm.y + tm.y; PIN(gt[e]); }
      if (s == 5) { gt[e] = __builtin_amdgcn_rcpf(gt[e]); PIN(gt[e]); }
      if (s == 6) { gu[e] = gu[e] * gt[e]; PIN(gu[e]); }
      if (s == 7 && (e & 1)) { od[e >> 1] = pk(gu[e - 1], gu[e]); PIN(od[e >> 1]); }
    }
    if (i == 28) {
      __builtin_amdgcn_raw_buffer_store_b128(u32x4{od[0], od[1], od[2], od[3]}, rw, gso, gofs(g), SC1);
    }
  };
  constexpr int PF = 4;
  u32x4 B[PF];
  u32 ba = opq(hbase(0) + bta);
  auto chain = [&](int g, f32x16& acc, auto side) AI {
    const u32 bn = opq(hbase(g < 3 ? g + 1 : g) + bta);
    sfor<28>([&](auto kc) AI {
      constexpr int k = decltype(kc)::value;
      acc = mfma32(W[k], B[k % PF], k == 0 ? f32x16{} : acc);
      SB();
      side(k);
      if (k + PF < 28) B[k % PF] = cat(trd(lds, ba + (k + PF) * 1024), trd(lds, ba + (k + PF) * 1024 + 256));
      else if (g < 3) B[k % PF] = cat(trd(lds, bn + (k + PF - 28) * 1024), trd(lds, bn + (k + PF - 28) * 1024 + 256));
      SB();
    });
    ba = bn;
  };
  auto flag3 = [&](int g) AI { setflag(ws, WS_F3 + (u32)(((gi * 4 + g) * 14) + gj) * 128, tag); };
  const int ri = 2 * xc + sc / 14, rq2 = (sc / 7) & 1, n2 = sc % 7;
  const u32x4 rd = srd(gwd, D_ * F_ * 2);
  const u32 wdv = (u32)(((224 * w + r8) * D_ + 64 * n2) * 2 + (((l & 7) ^ (((r8 >> 1) & 3) << 1)) << 4));
  const u32 blkR = w < 7 ? L_R + w * 4096 : L_R2;
  u32 wda[4];
#pragma unroll
  for (int nt = 0; nt < 4; ++nt) wda[nt] = (16 * (G >> 1) + 4 * (G & 1) + q4) * 128 + ((nt ^ (2 * (G & 1) + (q4 >> 1))) * 32) + p4 * 8;
  u32x4 Wd[7][4];
  auto wd_blk = [&](int k) AI { return k % 3 == 1 ? blkR : (k % 3 == 2 ? L_Q : L_P) + w * 4096; };
  auto wd_dma = [&](int k, int i) AI { dma(rd, lb + wd_blk(k) + i * 1024, wdv, (4 * k + i) * (8 * D_ * 2)); };
  auto wd_read = [&](int k) AI {
#pragma unroll
    for (int nt = 0; nt < 4; ++nt) { const u32 a = opq(wda[nt] + wd_blk(k)); Wd[k][nt] = cat(trd(lds, a), trd(lds, a + 1024)); }
  };
  const int j0 = (14 * w) >> 3, jn = ((14 * w + 13) >> 3) - j0;
  const u32 f3o = WS_F3 + (u32)(ri * 56 + j0 + ((l & 3) < jn ? (l & 3) : jn)) * 128;

  __builtin_amdgcn_s_setprio(1);
  f32x16 accA, accB;
  u32x4 tm1, tm2, tm3;
  const u32 s2o = WS_S2 + (u32)(2 * gi * 512 + tk * 16);
  auto h_dma = [&](int g, int k) AI {
    if (k < 3 || l < 32) dma<SC1>(dw, lb + hbase(g) + w * 3584 + k * 1024, lo16, WS_H + (u32)((32 * (g >> 1) + 2 * gi + (g & 1)) * TB + w * 3584 + k * 1024));
  };
  const u32 cso = l < 32 ? WS_CS + (u32)(((gj * 8 + w) * 32 + l) * 8) : OOB;
  u32x4 cv[4];
  auto h_rd = [&](int g) AI {
    const u32 a = opq(hbase(g) + w * 3584 + lo16), a3 = opq(hbase(g) + w * 3584 + 3072 + tk * 16);
    cv[0] = *(const u32x4*)(lds + a); cv[1] = *(const u32x4*)(lds + a + 1024); cv[2] = *(const u32x4*)(lds + a + 2048); cv[3] = *(const u32x4*)(lds + a3);
  };
  auto h_fix = [&](int g) AI {
    u32 cm = g == 1 ? max((u32)csv, (u32)(csv >> 32)) : g == 2 ? max4(tm1) : max(max4(tm2), max4(tm3));
    cm = max(max(cm, max(max4(cv[0]), max4(cv[1]))), max(max4(cv[2]), max4(cv[3])));
#pragma unroll 1
    for (int it = 0; it < 4000 && __any(cm == EMPTY); ++it) {
      h_dma(g, 0); h_dma(g, 1); h_dma(g, 2); h_dma(g, 3);
      if (g == 1) { csv = flag_ld(dw, cso); VMWP(0, csv); }
      if (g == 2) { tm1 = ldg(dw, s2o, 512); vmw<0>(tm1); }
      if (g == 3) { tm2 = ldg(dw, s2o, 32 * 512); tm3 = ldg(dw, s2o, 33 * 512); vmw<0>(tm2); PIN(tm3); }
      h_rd(g);
      cm = g == 1 ? max((u32)csv, (u32)(csv >> 32)) : g == 2 ? max4(tm1) : max(max4(tm2), max4(tm3));
      cm = max(max(cm, max(max4(cv[0]), max4(cv[1]))), max(max4(cv[2]), max4(cv[3])));
    }
  };
  SB();
  {
    const u32 b0 = opq(hbase(0) + sta);
#pragma unroll
    for (int k = 0; k < PF; ++k) B[k] = cat(trd(lds, ba + k * 1024), trd(lds, ba + k * 1024 + 256));
    st[0] = trd(lds, b0); st[1] = trd(lds, b0 + 512);
    SB();
    chain(0, accA, [&](int k) AI {
      stats_step(b0, k);
      if (k <= 6 && !(k & 1)) h_dma(1, k >> 1);
      if (k == 7) csv = flag_ld(dw, cso);
      if (k == 10 || k == 13) __builtin_amdgcn_raw_buffer_store_b128(u32x4{EMPTY, EMPTY, EMPTY, EMPTY}, rw, gso, gofs(k == 10 ? 2 : 3), SC1);
      if (k == 14) { VMW(7); RBAR(); const u32 ra3 = opq(w1.ra + L_WE); w1_read<12>(lds, ra3, W); w1_read<13>(lds, ra3 + 16384, W); }
      if (k == 19) { VMWP(2, csv); h_rd(1); }
      if (k == 22) { h_fix(1); RBAR(); }
      if (k >= 23 && (k & 1)) h_dma(2, (k - 23) >> 1);
    });
    stats_step(b0, 28);
  }
  STAMP(16);
  chain(1, accB, [&](int k) AI {
    if (k == 1) { bar(); if (tid == 0 && b == 0) setflag(ws, WS_E, tag); h_dma(2, 3); }
    geglu_step(0, accA, k);
    if (k == 3) tm1 = ldg(dw, s2o, 512);
    if (k >= 5 && k <= 14 && k % 3 == 2) wd_dma(0, (k - 5) / 3);
    if (k >= 16 && k <= 25 && k % 3 == 1) h_dma(3, (k - 16) / 3);
    if (k == 20) { vmw<6>(tm1); h_rd(2); }
    if (k == 23) { h_fix(2); RBAR(); }
  });
  center(accB, __uint_as_float(tm1.x));
  geglu_step(0, accA, 28);
  tm = __builtin_bit_cast(f32x4, tm1);
  SB();
  STAMP(18);
  chain(2, accA, [&](int k) AI {
    if (k == 1) RBAR();
    geglu_step(1, accB, k);
    if (k <= 6 && !(k & 1)) wd_dma(1, k >> 1);
    if (k == 3) { VMW(7); wd_read(0); }
    if (k == 8) { tm2 = ldg(dw, s2o, 32 * 512); tm3 = ldg(dw, s2o, 33 * 512); }
    if (k >= 10 && k <= 24 && !(k & 1)) wd_dma(2 + ((k - 10) >> 3), ((k - 10) >> 1) & 3);
    if (k == 19) { VMW(11); h_rd(3); }
    if (k == 23) { vmw<7>(tm2); PIN(tm3); h_fix(3); RBAR(); if (tid == 0) flag3(0); }
  });
  center(accA, __uint_as_float(tm2.x));
  geglu_step(1, accB, 28);
  tm = __builtin_bit_cast(f32x4, tm2);
  SB();
  STAMP(20);
  chain(3, accB, [&](int k) AI {
    geglu_step(2, accA, k);
    if (k == 12) fl = flag_ld(dw, f3o);
    if (k == 15) { VMW(1); RBAR(); if (tid == 0) flag3(1); }
    if (k >= 15 && k < 18) wd_read(k - 14);
    if (k == 19) LG0();
    if (k >= 19 && k < 27 && (k & 1)) wd_dma(4, (k - 19) >> 1);
  });
  center(accB, __uint_as_float(tm3.x));
  geglu_step(2, accA, 28);
  tm = __builtin_bit_cast(f32x4, tm3);
  STAMP(21);
#pragma unroll
  for (int e = 0; e < 8; ++e) {
    const float a = accB[e], u = accB[e + 8];
    gu[e] = (a * u) * __builtin_amdgcn_rcpf(__builtin_amdgcn_exp2f(a * (tm.w * (a * a) + tm.z)) * tm.y + tm.y);
  }
#pragma unroll
  for (int e = 0; e < 4; ++e) od[e] = pk(gu[2 * e], gu[2 * e + 1]);
  geglu_step(3, accB, 28);
  __builtin_amdgcn_s_setprio(0);
  SB();
  STAMP(22);
  const u32 gb = WS_G + (u32)((2 * ri * 2 + rq2) * 56 + 7 * w) * 1024;
  u32x4 Hf[3][7];
  auto h_load = [&](int g, int k0, int k1) AI {
#pragma unroll
    for (int k = k0; k < k1; ++k) Hf[g % 3][k] = ldg(dw, lo16, gb + gofs(g) + k * 1024);
  };
  const rsrc_t ro = mk(gout, S_ * D_ * 2);
  const u32 pw = (u32)(i16 * 256), tf = 4 * (w & 3) + G;
  f32x4 acc[4];
  u32x2 res[4];
  auto step = [&](auto gc, auto kc, auto nc) AI {
    constexpr int g = decltype(gc)::value, k = decltype(kc)::value;
    vmw<decltype(nc)::value>(Hf[g % 3][k]);
#pragma unroll
    for (int nt = 0; nt < 4; ++nt) acc[nt] = mfma16(Wd[k][nt], Hf[g % 3][k], k == 0 ? f32x4{} : acc[nt]);
    SB();
  };
  auto put = [&](int g) AI {
    const u32 rb = opq(((g & 1) ? L_Q : L_P) + w * 4096 + pw);
#pragma unroll
    for (int nt = 0; nt < 4; ++nt) *(f32x4*)(lds + rb + (((4 * nt + G) ^ (i16 & 7)) << 4)) = acc[nt];
    *(f32x4*)(lds + L_RES + (g & 1) * 4096 + pw + (((2 * w + (G & 1)) ^ (i16 & 7)) << 4)) = f32x4{bflo(res[g].x), bfhi(res[g].x), bflo(res[g].y), bfhi(res[g].y)};
  };
  auto fin = [&](int g) AI {
    if ((w >> 2) == (g & 1)) {
      const u32 a0 = tf * 256 + ((i16 ^ (tf & 7)) << 4), a = opq(((g & 1) ? L_Q : L_P) + a0);
      f32x4 sum = *(const f32x4*)(lds + L_RES + (g & 1) * 4096 + a0);
#pragma unroll
      for (int n = 0; n < 8; ++n) sum += *(const f32x4*)(lds + a + n * 4096);
      const int tok = 32 * (32 * (g >> 1) + (g & 1) + 2 * ri) + 16 * rq2 + tf;
      __builtin_amdgcn_raw_buffer_store_b64(u32x2{pk(sum.x, sum.y), pk(sum.z, sum.w)}, ro, (u32)((tok * D_ + 64 * n2 + 4 * i16) * 2), 0, 0);
    }
  };
  u32 chk;
  auto stepc = [&](auto gc, auto kc, auto nc) AI {
    constexpr int g = decltype(gc)::value, k = decltype(kc)::value;
    step(gc, kc, nc);
    const u32x4 v = Hf[g % 3][k];
    chk = k == 0 ? max4(v) : max(chk, max4(v));
    PIN(chk);
  };
  auto redo = [&](auto gc) AI {
    constexpr int g = decltype(gc)::value;
#pragma unroll 1
    for (int it = 0; it < 4000 && __any(chk == EMPTY); ++it) {
      h_load(g, 0, 7);
      stepc(gc, IC<0>{}, IC<6>{}); stepc(gc, IC<1>{}, IC<5>{}); stepc(gc, IC<2>{}, IC<4>{}); stepc(gc, IC<3>{}, IC<3>{});
      stepc(gc, IC<4>{}, IC<2>{}); stepc(gc, IC<5>{}, IC<1>{}); stepc(gc, IC<6>{}, IC<0>{});
    }
  };
  VMWP(6, fl);
  if (!__all(fl == tag)) poll(ws, f3o, true, tag);
  wd_dma(5, 0); wd_dma(5, 1); wd_dma(5, 2); wd_dma(5, 3);
  h_load(0, 0, 3);
  fl = flag_ld(dw, f3o + 14 * 128);
  h_load(0, 3, 7);
  const u32 hro = WS_H + (u32)((32 * (G >> 1) + (G & 1) + 2 * ri) * TB + (64 * n2 + 8 * w + (i16 >> 1)) * 64 + rq2 * 32 + (i16 & 1) * 16);
  u32x4 hr = ldg(dw, hro, 0);
  wd_dma(6, 0); wd_dma(6, 1); wd_dma(6, 2); wd_dma(6, 3);
  VMWP(9, fl);
  if (!__all(fl == tag)) poll(ws, f3o + 14 * 128, true, tag);
  wd_read(4); wd_read(5);
  SB();
  STAMP(23);
  h_load(1, 0, 1); step(IC<0>{}, IC<0>{}, IC<10>{});
  h_load(1, 1, 2); step(IC<0>{}, IC<1>{}, IC<11>{});
  h_load(1, 2, 3); step(IC<0>{}, IC<2>{}, IC<12>{});
  h_load(1, 3, 4); step(IC<0>{}, IC<3>{}, IC<12>{});
  h_load(1, 4, 5); step(IC<0>{}, IC<4>{}, IC<12>{});
  h_load(1, 5, 6); step(IC<0>{}, IC<5>{}, IC<12>{});
  h_load(1, 6, 7);
  vmw<7>(hr);
#pragma unroll 1
  for (int it = 0; it < 4000 && __any(max4(hr) == EMPTY); ++it) { hr = ldg(dw, hro, 0); vmw<0>(hr); }
  wd_read(6);
  *(u32x4*)(lds + blkR + l * 16) = hr;
#pragma unroll
  for (int g = 0; g < 4; ++g) res[g] = trd(lds, blkR + g * 256 + (4 * (G & 1) + q4) * 32 + p4 * 8);
  h_load(2, 0, 2);
  step(IC<0>{}, IC<6>{}, IC<9>{});
  put(0);
  STAMP(24);
  h_load(2, 2, 3); step(IC<1>{}, IC<0>{}, IC<9>{});
  h_load(2, 3, 5); step(IC<1>{}, IC<1>{}, IC<10>{});
  h_load(2, 5, 7); step(IC<1>{}, IC<2>{}, IC<11>{});
  h_load(3, 0, 2); step(IC<1>{}, IC<3>{}, IC<12>{});
  h_load(3, 2, 4); step(IC<1>{}, IC<4>{}, IC<13>{});
  h_load(3, 4, 6); step(IC<1>{}, IC<5>{}, IC<14>{});
  h_load(3, 6, 7); step(IC<1>{}, IC<6>{}, IC<14>{});
  put(1);
  STAMP(25);
  bar();
  fin(0); fin(1);
  SB();
  stepc(IC<2>{}, IC<0>{}, IC<13>{}); stepc(IC<2>{}, IC<1>{}, IC<12>{}); stepc(IC<2>{}, IC<2>{}, IC<11>{}); stepc(IC<2>{}, IC<3>{}, IC<10>{});
  stepc(IC<2>{}, IC<4>{}, IC<9>{}); stepc(IC<2>{}, IC<5>{}, IC<8>{}); stepc(IC<2>{}, IC<6>{}, IC<7>{});
  redo(IC<2>{});
  bar();
  put(2);
  SB();
  STAMP(26);
  stepc(IC<3>{}, IC<0>{}, IC<6>{}); stepc(IC<3>{}, IC<1>{}, IC<5>{}); stepc(IC<3>{}, IC<2>{}, IC<4>{}); stepc(IC<3>{}, IC<3>{}, IC<3>{});
  stepc(IC<3>{}, IC<4>{}, IC<2>{}); stepc(IC<3>{}, IC<5>{}, IC<1>{}); stepc(IC<3>{}, IC<6>{}, IC<0>{});
  redo(IC<3>{});
  put(3);
  bar();
  STAMP(27);
  fin(2); fin(3);
  STAMP(28);
  STAMP_OUT();
}

__global__ void k_pre(char* ws) { if (threadIdx.x == 0) __hip_atomic_store((GAS u64*)(ws + WS_DBG), (u64)__builtin_amdgcn_s_memrealtime(), RLX_AGENT); }
__global__ void k_post(char* ws, int mult) { if (threadIdx.x == 0) { const u64 t = __builtin_amdgcn_s_memrealtime(); const u64 e = __hip_atomic_load((GAS u64*)(ws + WS_DBG + 128), RLX_AGENT); u64 d = t - e; if (d > 5000) d = 5000; const u64 te = t + mult * d; while (__builtin_amdgcn_s_memrealtime() < te) __builtin_amdgcn_s_sleep(1); } }
}

extern "C" void kernel_launch(void* const* d_in, const int* in_sizes, int n_in, void* d_out, int out_size, void* d_ws, size_t ws_size,
                              hipStream_t stream) {
  h3_ffn<<<256, 512, 0, stream>>>((const char*)d_in[0], (const char*)d_in[1], (const char*)d_in[2], (const char*)d_in[3], (const char*)d_in[4],
                                  (const char*)d_in[5], (const char*)d_in[7], (const char*)d_in[8], (const char*)d_in[9], (char*)d_out, (char*)d_ws);
}
```

```cpp
#include <hip/hip_runtime.h>
#include <stdint.h>

namespace {

#define LAS __attribute__((address_space(3)))
#define GAS __attribute__((address_space(1)))
#define DI __device__ __forceinline__
#define AI __attribute__((always_inline))
typedef uint32_t u32;
typedef unsigned long long u64;
typedef u32 u32x2 __attribute__((ext_vector_type(2)));
typedef u32 u32x4 __attribute__((ext_vector_type(4)));
typedef short s16x4 __attribute__((ext_vector_type(4)));
typedef short s16x8 __attribute__((ext_vector_type(8)));
typedef float f32x4 __attribute__((ext_vector_type(4)));
typedef float f32x16 __attribute__((ext_vector_type(16)));
typedef __bf16 bf2 __attribute__((ext_vector_type(2)));
typedef __amdgpu_buffer_rsrc_t rsrc_t;
#define RLX_AGENT __ATOMIC_RELAXED, __HIP_MEMORY_SCOPE_AGENT

constexpr int S_ = 2048, D_ = 448, F_ = 1792;
constexpr u32 TB = 448 * 64;
constexpr u32 WS_E  = 0;
constexpr u32 WS_F1 = 128;
constexpr u32 WS_F3 = WS_F1 + 256 * 128;
constexpr u32 WS_F2 = WS_F3 + 896 * 128;
constexpr u32 WS_S2 = 256 * 1024 + 16384;
constexpr u32 WS_CS = 224 * 1024;
constexpr u32 WS_ST = 256 * 1024;
constexpr u32 WS_U  = WS_S2 + 32768;
constexpr u32 WS_X  = WS_U + 64 * TB;
constexpr u32 WS_H  = WS_X + 64 * TB;
constexpr u32 WS_G  = WS_H + 64 * TB;
constexpr u32 WS_END = WS_G + 64 * 112 * 1024;
constexpr u32 WS_DBG = 16u << 20;
constexpr int SC1 = 16;
constexpr u32 OOB = 0xFFFFFF00u;
constexpr u32 ONES = 0x3f803f80u;
constexpr u32 EMPTY = 0xffffffffu;

constexpr u32 L_K = 0, L_V = 9216, L_X = 17408, L_SK = 25600;
constexpr u32 L_RED = 0, L_FC = 32768, FCS = 4160, FCH = 4 * FCS;
constexpr u32 L_U = L_FC + 2 * FCH, UCH = 4608, L_RING = L_U + 2 * UCH;
constexpr u32 L_H0 = 32768, L_H1 = L_H0 + TB, L_WE = L_H1 + TB, L_H2 = 0, L_H3 = L_WE + 32768, L_SP = 155648;
constexpr u32 L_P = TB, L_Q = L_P + 32768, L_R = L_Q + 32768, L_R2 = L_H3 + TB;
constexpr u32 L_RES = 0;
constexpr u32 LDS_BYTES = 163840;

DI float bflo(u32 x) { return __uint_as_float(x << 16); }
DI float bfhi(u32 x) { return __uint_as_float(x & 0xffff0000u); }
DI u32 pk(float lo, float hi) { bf2 p; p.x = (__bf16)lo; p.y = (__bf16)hi; return __builtin_bit_cast(u32, p); }
DI rsrc_t mk(const void* p, u32 bytes) { return __builtin_amdgcn_make_buffer_rsrc((void*)p, 0, bytes, 0x00020000); }
DI u32x2 trd(const char* lds, u32 a) { return __builtin_bit_cast(u32x2, __builtin_amdgcn_ds_read_tr16_b64_v4i16((LAS s16x4*)(lds + a))); }
DI u32x4 cat(u32x2 a, u32x2 b) { return u32x4{a.x, a.y, b.x, b.y}; }
DI u32x4 srd(const void* p, u32 bytes) { const u64 a = (u64)p; return u32x4{(u32)a, (u32)(a >> 32) & 0xffffu, bytes, 0x00020000u}; }
template <int AUX = 0> DI void dma(u32x4 r, u32 ldsoff, u32 voff, u32 soff) {
  if (AUX) asm volatile("s_mov_b32 m0, %0\n\ts_nop 0\n\tbuffer_load_dwordx4 %1, %2, %3 offen sc1 lds" :: "s"(ldsoff), "v"(voff), "s"(r), "s"(soff) : "memory");
  else asm volatile("s_mov_b32 m0, %0\n\ts_nop 0\n\tbuffer_load_dwordx4 %1, %2, %3 offen lds" :: "s"(ldsoff), "v"(voff), "s"(r), "s"(soff) : "memory");
}
DI float fnma(float a, float b, float c) { float r; asm("v_fma_f32 %0, -%1, %2, %3" : "=v"(r) : "v"(a), "v"(b), "v"(c)); return r; }
DI float op(float x) { asm("" : "+v"(x)); return x; }
DI u32 opq(u32 x) { asm volatile("" : "+v"(x)); return x; }
#define PIN(x) asm volatile("" : "+v"(x))
DI f32x4 mfma16(u32x4 a, u32x4 b, f32x4 c) { return __builtin_amdgcn_mfma_f32_16x16x32_bf16(__builtin_bit_cast(s16x8, a), __builtin_bit_cast(s16x8, b), c, 0, 0, 0); }
DI f32x16 mfma32(u32x4 a, u32x4 b, f32x16 c) { return __builtin_amdgcn_mfma_f32_32x32x16_bf16(__builtin_bit_cast(s16x8, a), __builtin_bit_cast(s16x8, b), c, 0, 0, 0); }
DI float dot2(u32 a, u32 b, float c) { return __builtin_amdgcn_fdot2_f32_bf16(__builtin_bit_cast(bf2, a), __builtin_bit_cast(bf2, b), c, false); }
#define VMW(n) asm volatile("s_waitcnt vmcnt(" #n ")" ::: "memory")
#define LG0() asm volatile("s_waitcnt lgkmcnt(0)" ::: "memory")
#define SB() __builtin_amdgcn_sched_barrier(0)
DI void bar() { asm volatile("s_waitcnt lgkmcnt(0)\n\ts_barrier" ::: "memory"); }
#define RBAR() asm volatile("s_barrier" ::: "memory")
DI float wsum(float v) {
  v += __builtin_amdgcn_update_dpp(0.f, v, 0x111, 0xf, 0xf, true);
  v += __builtin_amdgcn_update_dpp(0.f, v, 0x112, 0xf, 0xf, true);
  v += __builtin_amdgcn_update_dpp(0.f, v, 0x114, 0xf, 0xf, true);
  v += __builtin_amdgcn_update_dpp(0.f, v, 0x118, 0xf, 0xf, true);
  v += __builtin_amdgcn_update_dpp(0.f, v, 0x142, 0xa, 0xf, true);
  v += __builtin_amdgcn_update_dpp(0.f, v, 0x143, 0xc, 0xf, true);
  return __uint_as_float(__builtin_amdgcn_readlane(__float_as_uint(v), 63));
}
DI float hsum(float v) {
  const auto r = __builtin_amdgcn_permlane32_swap(__float_as_uint(v), __float_as_uint(v), false, false);
  return __uint_as_float(r[0]) + __uint_as_float(r[1]);
}
template <int SLEEP = 1> DI void poll(char* ws, u32 off, bool act, u64 tag) {
  bool ok = !act;
#pragma unroll 1
  for (int it = 0; it < 20000; ++it) {
    u64 x = tag;
    if (!ok) x = __hip_atomic_load((GAS u64*)(ws + off), RLX_AGENT);
    ok = (x == tag);
    if (__all(ok)) break;
    __builtin_amdgcn_s_sleep(SLEEP);
  }
  asm volatile("" ::: "memory");
}
DI u32 max4(u32x4 v) { return max(max(v.x, v.y), max(v.z, v.w)); }
DI u64 flag_ld(u32x4 r, u32 voff) { u64 v; asm volatile("buffer_load_dwordx2 %0, %1, %2, 0 offen sc1" : "=v"(v) : "v"(voff), "s"(r) : "memory"); return v; }
DI u32x4 ldg(u32x4 r, u32 voff, u32 soff) { u32x4 v; asm volatile("buffer_load_dwordx4 %0, %1, %2, %3 offen sc1" : "=v"(v) : "v"(voff), "s"(r), "s"(soff) : "memory"); return v; }
template <int N> DI void vmw(u32x4& x) { asm volatile("s_waitcnt vmcnt(%1)" : "+v"(x) : "n"(N) : "memory"); }
#define VMWP(n, x) asm volatile("s_waitcnt vmcnt(" #n ")" : "+v"(x) :: "memory")
DI void setflag(char* ws, u32 off, u64 tag) { __hip_atomic_store((GAS u64*)(ws + off), tag, RLX_AGENT); }

#define STAMP(i) do { if ((i) == 0) t_0 = __builtin_amdgcn_s_memrealtime(); } while (0)
#define STAMPC(i)
#define STAMP_OUT() do { if (b == 9 && w == 0) { u64 d = __hip_atomic_load((GAS u64*)(ws + WS_DBG + 256), RLX_AGENT) - __hip_atomic_load((GAS u64*)(ws + WS_DBG), RLX_AGENT); if (d > 5000) d = 5000; const u64 te = __builtin_amdgcn_s_memrealtime() + 8 * d; while (__builtin_amdgcn_s_memrealtime() < te) __builtin_amdgcn_s_sleep(1); } } while (0)

DI float xsum(float a, float c) {
  const auto r = __builtin_amdgcn_permlane32_swap(__float_as_uint(a), __float_as_uint(c), false, false);
  return __uint_as_float(r[0]) + __uint_as_float(r[1]);
}
DI f32x4 ln2_terms(float t) {
  const auto r = __builtin_amdgcn_permlane32_swap(__float_as_uint(t), __float_as_uint(t), false, false);
  const float mean = __uint_as_float(r[0]) * (1.f / D_);
  const float V = fmaxf(__uint_as_float(r[1]) * (1.f / D_) - mean * mean, 0.f) + 1e-5f;
  const float rs = __builtin_amdgcn_rsqf(V);
  const float K1 = -2.f * 0.7978845608028654f * 1.4426950408889634f * rs;
  const float lo = -mean - bflo(pk(-mean, 0.f));
  return f32x4{__uint_as_float(pk(-mean, lo)), V, K1, (K1 * rs) * (0.044715f * rs)};
}
template <int N> struct IC { static constexpr int value = N; };
template <int N, int I = 0, class F> DI void sfor(F&& f) { if constexpr (I < N) { f(IC<I>{}); sfor<N, I + 1>(f); } }
struct W1 { u32x4 rg, ru; u32 vg, vu, ra, lb; };
template <int T> DI void w1_g(const W1& c, u32 base) { dma(c.rg, c.lb + base, c.vg, T * (32 * F_ * 2)); }
template <int T> DI void w1_u(const W1& c, u32 base) { dma(c.ru, c.lb + base + 8192, c.vu, T * (32 * F_ * 2)); }
template <int T> DI void w1_issue(const W1& c, u32 base) { w1_g<T>(c, base); w1_u<T>(c, base); }
template <int T> DI void w1_read(const char* lds, u32 a, u32x4 (&W)[28]) {
#pragma unroll
  for (int kk = 0; kk < 2; ++kk) W[2 * T + kk] = cat(trd(lds, a + kk * 4096), trd(lds, a + kk * 4096 + 1024));
}
DI float x8sum(float v) {
  v += __builtin_amdgcn_update_dpp(0.f, v, 0xb1, 0xf, 0xf, true);
  v += __builtin_amdgcn_update_dpp(0.f, v, 0x4e, 0xf, 0xf, true);
  v += __builtin_amdgcn_update_dpp(0.f, v, 0x141, 0xf, 0xf, true);
  return v;
}

__global__ __launch_bounds__(512, 2) void h3_ffn(const char* __restrict__ gq, const char* __restrict__ gk, const char* __restrict__ gv,
                                                 const char* __restrict__ gsk, const char* __restrict__ gf, const char* __restrict__ gD,
                                                 const char* __restrict__ gwg, const char* __restrict__ gwu, const char* __restrict__ gwd,
                                                 char* __restrict__ gout, char* ws) {
  __shared__ __attribute__((aligned(16))) char lds[LDS_BYTES];
  const int tid = threadIdx.x, l = tid & 63, w = __builtin_amdgcn_readfirstlane(tid >> 6);
  const int b = blockIdx.x, xc = b & 7, sl = b >> 3;
  const int tb64 = 4 * xc + (sl >> 3), role = sl & 7;
  const int sc = (sl >> 3) * 7 + role;
  u64 t_0 = 0;
  asm volatile("" :: "s"(gq), "s"(gk), "s"(gv), "s"(gsk), "s"(gf), "s"(gD), "s"(gwg), "s"(gwu), "s"(gwd), "s"(gout), "s"(ws));
  STAMP(0);
  const rsrc_t rw = mk(ws, WS_END);
  const int G = l >> 4, i16 = l & 15, q4 = i16 >> 2, p4 = l & 3, r8 = l >> 3;
  auto mktag = [&](u64 e0) AI { return ((u64)__builtin_amdgcn_readfirstlane((u32)e0) | ((u64)__builtin_amdgcn_readfirstlane((u32)(e0 >> 32)) << 32)) * 6364136223846793005ull + 1442695040888963407ull; };
  if (role == 7) {
    const rsrc_t rq = mk(gq, S_ * D_ * 2);
    const int row = 64 * tb64 + 8 * w + r8;
    u32x4 xs[7];
#pragma unroll
    for (int i = 0; i < 7; ++i) xs[i] = __builtin_amdgcn_raw_buffer_load_b128(rq, (u32)(row * 896 + (l & 7) * 16 + i * 128), 0, 0);
    const u64 e0 = __hip_atomic_load((GAS u64*)(ws + WS_E), RLX_AGENT);
    SB();
    SB();
    const u64 tag = mktag(e0);
    float s[4] = {};
#pragma unroll
    for (int i = 0; i < 7; ++i)
#pragma unroll
      for (int e = 0; e < 4; ++e) s[e] += bflo(xs[i][e]) + bfhi(xs[i][e]);
    const float mean = x8sum((s[0] + s[1]) + (s[2] + s[3])) * (1.f / D_);
    float v[4] = {};
#pragma unroll
    for (int i = 0; i < 7; ++i)
#pragma unroll
      for (int e = 0; e < 4; ++e) { const float d0 = bflo(xs[i][e]) - mean, d1 = bfhi(xs[i][e]) - mean; v[e] += d0 * d0; v[e] += d1 * d1; }
    const float rstd = __builtin_amdgcn_rsqf(x8sum((v[0] + v[1]) + (v[2] + v[3])) * (1.f / D_) + 1e-5f);
    const u32 so = WS_ST + (row >> 8) * 2048 + ((row >> 1) & 3) * 512 + ((row >> 5) & 7) * 64 + ((row >> 3) & 3) * 16 + (row & 1) * 8;
    __builtin_amdgcn_raw_buffer_store_b64(u32x2{__float_as_uint(mean), __float_as_uint(rstd)}, rw, (l & 7) == 0 ? so : OOB, 0, SC1);
    VMW(0); bar();
    if (tid == 0) setflag(ws, WS_F1 + (tb64 * 8 + 7) * 128, tag);
#pragma unroll
    for (int i = 0; i < 7; ++i) __builtin_amdgcn_raw_buffer_store_b128(u32x4{EMPTY, EMPTY, EMPTY, EMPTY}, rw, (u32)(tid * 16), WS_H + (u32)(tb64 * 2 * TB + i * 8192), SC1);
    __builtin_amdgcn_raw_buffer_store_b128(u32x4{EMPTY, EMPTY, EMPTY, EMPTY}, rw, tid < 64 ? WS_S2 + (u32)(tb64 * 1024 + tid * 16) : tid < 120 ? WS_CS + (u32)(tb64 * 896 + (tid - 64) * 16) : OOB, 0, SC1);
    VMW(0); bar();
    if (tid == 0) setflag(ws, WS_F2 + tb64 * 128, tag);
    STAMP(1);
    {
      const int mat = tb64 >= 14, j = tb64 < 28 ? tb64 - 14 * mat : 0;
      const rsrc_t rm = mk(mat ? gwu : gwg, D_ * F_ * 2);
      u32x4 wv[14];
#pragma unroll
      for (int i = 0; i < 14; ++i) wv[i] = __builtin_amdgcn_raw_buffer_load_b128(rm, tb64 < 28 ? (u32)(((4 * w + G + 32 * i) * F_ + 128 * j) * 2 + i16 * 16) : OOB, 0, 0);
      float c[8] = {};
#pragma unroll
      for (int i = 0; i < 14; ++i)
#pragma unroll
        for (int e = 0; e < 4; ++e) { c[2 * e] += bflo(wv[i][e]); c[2 * e + 1] += bfhi(wv[i][e]); }
      *(f32x4*)(lds + (4 * w + G) * 512 + i16 * 32) = f32x4{c[0], c[1], c[2], c[3]};
      *(f32x4*)(lds + (4 * w + G) * 512 + i16 * 32 + 16) = f32x4{c[4], c[5], c[6], c[7]};
      poll(ws, WS_F2 + (u32)(l & 31) * 128, true, tag);
      bar();
      if (tid < 128 && tb64 < 28) {
        float t = 0.f;
#pragma unroll
        for (int n = 0; n < 32; ++n) t += *(const float*)(lds + n * 512 + tid * 4);
        const u32 hi = pk(t, t), lo = pk(t - bflo(hi), 0.f);
        __builtin_amdgcn_raw_buffer_store_b64(u32x2{hi, lo}, rw, WS_CS + (u32)(((j * 8 + (tid >> 4)) * 32 + mat * 16 + (tid & 15)) * 8), 0, SC1);
      }
    }
    STAMP(2);
    const u32x4 dw = srd(ws, WS_END);
    const u32 lb = (u32)(size_t)(LAS char*)lds;
#pragma unroll 1
    for (int half = (tb64 & 1) ^ 1; half < 2; ++half) {
      const int tb = 32 * half + tb64;
#pragma unroll 1
      for (int it = 0; it < 4000; ++it) {
#pragma unroll
        for (int k = 0; k < 4; ++k) if (k < 3 || l < 32) dma<SC1>(dw, lb + 32768 + w * 3584 + k * 1024, l * 16, WS_H + (u32)(tb * TB + w * 3584 + k * 1024));
        VMW(0);
        u32 m = 0;
#pragma unroll
        for (int k = 0; k < 4; ++k) m = max(m, max4(*(const u32x4*)(lds + 32768 + w * 3584 + (k < 3 ? k * 1024 + l * 16 : 3072 + (l & 31) * 16))));
        if (!__any(m == EMPTY)) break;
        __builtin_amdgcn_s_sleep(6);
      }
      float s1 = 0.f, s2 = 0.f;
#pragma unroll
      for (int i = 0; i < 7; ++i) {
        const u32x2 t = trd(lds, 32768 + (u32)((56 * w + 4 * (G >> 1) + q4) * 64 + (G & 1) * 32 + p4 * 8) + i * 512);
        s1 = dot2(t.x, ONES, s1); s2 = dot2(t.x, t.x, s2); s1 = dot2(t.y, ONES, s1); s2 = dot2(t.y, t.y, s2);
      }
      *(float*)(lds + 20480 + half * 2048 + l * 32 + w * 4) = xsum(s1, s2);
      bar();
      if (w == 0) {
        const f32x4 a = *(const f32x4*)(lds + 20480 + half * 2048 + l * 32), c = *(const f32x4*)(lds + 20480 + half * 2048 + l * 32 + 16);
        __builtin_amdgcn_raw_buffer_store_b128(__builtin_bit_cast(u32x4, ln2_terms(((a.x + a.y) + (a.z + a.w)) + ((c.x + c.y) + (c.z + c.w)))), rw, l < 32 ? WS_S2 + (u32)(tb * 512 + l * 16) : OOB, 0, SC1);
      }
    }
    STAMP(3);
    STAMP_OUT();
    return;
  }
  const int cp = xc * 28 + sc, cch = w & 1, vp = w >> 1;
  u32x4 fhi; u32x2 flo; u32 Dh; u64 e0;
  const u32 lb = (u32)(size_t)(LAS char*)lds;
  const u32x4 dw = srd(ws, WS_END);
  {
    const u32x4 rk = srd(gk, S_ * D_ * 2), rv = srd(gv, S_ * D_ * 2);
    const int c8 = l & 7, s0 = 64 * tb64;
    const int swv = ((r8 >> 1) & 1) | ((w & 1) << 1), swk = swv ^ 2;
    const u32 g0 = (u32)((s0 + 8 * w + r8) * 896 + role * 128);
    if (w == 0) dma(rk, lb + L_K, g0 - 8 * 896 + ((c8 ^ (swv << 1)) << 4), 0);
    if (w == 1) dma(srd(gsk, D_ * 8), lb + L_SK, (u32)(512 * role + 16 * l), 0);
    dma(rk, lb + L_K + 1024 + w * 1024, g0 + ((c8 ^ (swk << 1)) << 4), 0);
    dma(rv, lb + L_V + w * 1024, g0 + ((c8 ^ (swv << 1)) << 4), 0);
    dma(srd(gq, S_ * D_ * 2), lb + L_X + w * 1024, g0 + ((c8 ^ (swv << 1)) << 4), 0);
    const rsrc_t rf = mk(gf, S_ * D_ * 2);
    const int fq = vp * 64 + l;
    const u32 fo = (u32)((2 * cp + cch) * 4096 + 16 * fq);
    fhi = __builtin_amdgcn_raw_buffer_load_b128(rf, fo, 0, 0);
    flo = __builtin_amdgcn_raw_buffer_load_b64(rf, fq > 0 ? fo - 8 : OOB, 0, 0);
    Dh = __builtin_amdgcn_raw_buffer_load_b16(mk(gD, D_ * 2), (u32)((2 * cp + ((l >> 2) & 1)) * 2), 0, 0);
    e0 = __hip_atomic_load((GAS u64*)(ws + WS_E), RLX_AGENT);
    SB();
  }
  const int gi = 4 * (xc >> 1) + sc / 7, gj = 7 * (xc & 1) + sc % 7;
  W1 w1;
  {
    const u32 v0 = (u32)(((4 * w + G) * F_ + 128 * gj) * 2);
    w1.rg = srd(gwg, D_ * F_ * 2); w1.ru = srd(gwu, D_ * F_ * 2); w1.lb = lb + w * 1024;
    w1.vg = v0 + ((i16 ^ (4 * G)) << 4); w1.vu = v0 + ((i16 ^ (4 * G + 2)) << 4);
    w1.ra = (u32)((G & 1) * 8192 + (8 * (G >> 1) + q4) * 256 + ((w ^ (2 * q4 + (G & 1))) * 32) + p4 * 8);
  }
  VMW(5); bar();
  STAMP(1);
  {
    const int tbl = w >> 2, chg = w & 3;
    const u32 rb = (u32)((32 * tbl + 8 * G + q4) * 128 + p4 * 8);
    const u32 sv = (u32)((chg ^ ((q4 >> 1) | ((G & 1) << 1))) * 32), sk2 = sv ^ 64;
    const u32x2 km = trd(lds, L_K + rb + 512 + sv), k0 = trd(lds, L_K + rb + 1024 + sk2), k1 = trd(lds, L_K + rb + 1536 + sk2);
    const u32x2 v0 = trd(lds, L_V + rb + sv), v1 = trd(lds, L_V + rb + 512 + sv);
    const u32x2 skv = *(const u32x2*)(lds + L_SK + (16 * chg + i16) * 8);
    const float kk[11] = {bfhi(km.x), bflo(km.y), bfhi(km.y), bflo(k0.x), bfhi(k0.x), bflo(k0.y), bfhi(k0.y), bflo(k1.x), bfhi(k1.x), bflo(k1.y), bfhi(k1.y)};
    const float vv[8] = {bflo(v0.x), bfhi(v0.x), bflo(v0.y), bfhi(v0.y), bflo(v1.x), bfhi(v1.x), bflo(v1.y), bfhi(v1.y)};
    const float s0 = bflo(skv.x), s1 = bfhi(skv.x), s2 = bflo(skv.y), s3 = bfhi(skv.y);
    float u[8];
#pragma unroll
    for (int e = 0; e < 8; ++e) u[e] = (kk[e + 3] * s0 + kk[e + 2] * s1 + kk[e + 1] * s2 + kk[e] * s3) * vv[e];
    const u32 o = (u32)((2 * tb64 + tbl) * TB + (64 * role + 16 * chg + i16) * 64 + G * 16);
    __builtin_amdgcn_raw_buffer_store_b128(u32x4{pk(u[0], u[1]), pk(u[2], u[3]), pk(u[4], u[5]), pk(u[6], u[7])}, rw, WS_U + o, 0, SC1);
    SB();
    STAMP(2);
    VMW(5); bar();
    const u32x2 x0 = trd(lds, L_X + rb + sv), x1 = trd(lds, L_X + rb + 512 + sv);
    __builtin_amdgcn_raw_buffer_store_b128(cat(x0, x1), rw, WS_X + o, 0, SC1);
    PIN(fhi); PIN(flo); PIN(Dh); PIN(e0);
    SB();
  }
  STAMP(3);
  {
    const u32 a0 = __builtin_amdgcn_alignbit(flo.y, flo.x, 16), a1 = __builtin_amdgcn_alignbit(fhi.x, flo.y, 16), a2 = __builtin_amdgcn_alignbit(fhi.y, fhi.x, 16),
              a3 = __builtin_amdgcn_alignbit(fhi.z, fhi.y, 16), a4 = __builtin_amdgcn_alignbit(fhi.w, fhi.z, 16);
    char* fc = lds + L_FC + cch * FCH + (vp * 64 + l) * 16 + 32;
    *(u32x4*)(fc) = fhi;
    *(u32x4*)(fc + FCS) = u32x4{a1, a2, a3, a4};
    *(u32x4*)(fc + 2 * FCS) = u32x4{flo.y, fhi.x, fhi.y, fhi.z};
    *(u32x4*)(fc + 3 * FCS) = u32x4{a0, a1, a2, a3};
    if (tid >= 128 && tid < 144) *(u32x4*)(lds + L_FC + (tid & 7) * FCS + ((tid >> 3) & 1) * 16) = u32x4{0, 0, 0, 0};
    if (tid >= 192 && tid < 252) { const int z = tid - 192; *(u32x4*)(lds + L_U + (z / 30) * UCH + (z % 30) * 16) = u32x4{0, 0, 0, 0}; }
  }
  const u64 tag = mktag(e0);
  const float Dv = bflo(Dh);
  SB();
  w1_issue<0>(w1, L_RING); w1_issue<1>(w1, L_RING + 16384);
  STAMP(4);
  VMW(4); bar();
  if (tid == 0) setflag(ws, WS_F1 + (tb64 * 8 + role) * 128, tag);
  STAMP(5);
  const int hh = l >> 5, tk = l & 31;
  const u32 gso = WS_G + (u32)(((2 * gi * 2 + (tk >> 4)) * 56 + 4 * gj + (w >> 1)) * 1024 + (2 * (w & 1) + hh) * 256 + i16 * 16);
  auto gofs = [](int g) AI { return (u32)((32 * (g >> 1) + (g & 1)) * 2 * 56 * 1024); };
  u32x4 W[28];
  w1_issue<2>(w1, L_RING + 32768); w1_issue<3>(w1, L_RING + 49152); w1_issue<4>(w1, L_RING + 65536); w1_issue<5>(w1, L_RED); w1_issue<6>(w1, L_RED + 16384);
  SB();

  const int ech = (l >> 2) & 1;
  const u32 eo = (u32)((8 * w + r8) * TB + (2 * cp + ech) * 64 + p4 * 16);
  STAMP(6);
  poll(ws, WS_F1 + (u32)(32 * w + 8 * (l & 3) + ((l & 4) ? 7 : (cp >> 5))) * 128, l < 8, tag);
  STAMP(7);
  u32x4 uu = ldg(dw, eo, WS_U), ex = ldg(dw, eo, WS_X);
  u32x4 est[4];
#pragma unroll
  for (int i = 0; i < 4; ++i) est[i] = ldg(dw, (u32)(r8 * 64 + p4 * 16), WS_ST + (u32)(w * 2048 + i * 512));
  u64 fl = flag_ld(dw, WS_F2 + (u32)(4 * w + (l & 3)) * 128);
  bar();
  const u32 ra0 = opq(w1.ra), ra1 = opq(w1.ra + L_RING), ra2 = opq(w1.ra + L_RING + 49152);
  w1_read<5>(lds, ra0 + L_RED, W); w1_read<6>(lds, ra0 + L_RED + 16384, W);
  w1_read<0>(lds, ra1, W); w1_read<1>(lds, ra1 + 16384, W); w1_read<2>(lds, ra1 + 32768, W); w1_read<3>(lds, ra2, W); w1_read<4>(lds, ra2 + 16384, W);
  const u32 aA = opq(L_FC + cch * FCH + q4 * FCS + 64 - 16 * G + 8 * p4 - 8 + 128 * vp);
  const u32 aB = opq(L_U + cch * UCH + 32 * i16 + 16 * G + 448 - 128 * vp - 64);
  const u32 rwr = opq(L_RED + w * 4096 + G * 256 + ((i16 ^ ((G >> 1) << 3)) << 4));
  const u32 rr = opq(L_RED + ech * 4096 + (w & 3) * 1024 + (p4 & 1) * 512 + (((2 * r8 + (p4 >> 1)) ^ ((p4 & 1) << 3)) << 4));
  SB();
  vmw<6>(uu);
  *(u32x4*)(lds + L_U + ech * UCH + 480 + (8 * w + r8) * 64 + p4 * 16) = uu;
  bar();
  STAMP(8);
  auto rdA = [&](int v, int m) AI { const u32 o = 64 * v + 512 * m; return cat(trd(lds, aA + o + 8), trd(lds, aA + o)); };
  auto rdB = [&](int v, int m) AI { return *(const u32x4*)(lds + aB + 64 * (1 - v) + 512 * m); };
  float ex8[8], eq8[8], eu8[8];
  auto terms = [&]() AI {
#pragma unroll
    for (int i = 0; i < 4; ++i) {
      ex8[2 * i] = bflo(ex[i]); ex8[2 * i + 1] = bfhi(ex[i]);
      eu8[2 * i] = bflo(uu[i]) * Dv; eu8[2 * i + 1] = op(bfhi(uu[i]) * Dv);
      eq8[2 * i] = (ex8[2 * i] - __uint_as_float(est[i].x)) * __uint_as_float(est[i].y);
      eq8[2 * i + 1] = op(op(ex8[2 * i + 1] - __uint_as_float(est[i].z)) * __uint_as_float(est[i].w));
    }
#pragma unroll
    for (int i = 0; i < 8; ++i) { PIN(ex8[i]); PIN(eq8[i]); PIN(eu8[i]); }
  };
  auto epilogue = [&]() AI {
    f32x4 p[8];
#pragma unroll
    for (int n = 0; n < 4; ++n) { p[2 * n] = *(const f32x4*)(lds + rr + n * 8192); p[2 * n + 1] = *(const f32x4*)(lds + rr + n * 8192 + 256); }
    float h[8];
#pragma unroll
    for (int i = 0; i < 8; ++i) {
      const float y = op(op(eu8[i] + p[i >> 2][i & 3]) + p[2 + (i >> 2)][i & 3]) + op(p[4 + (i >> 2)][i & 3] + p[6 + (i >> 2)][i & 3]);
      h[i] = op(ex8[i] + eq8[i] * op(y));
    }
    __builtin_amdgcn_raw_buffer_store_b128(u32x4{pk(h[0], h[1]), pk(h[2], h[3]), pk(h[4], h[5]), pk(h[6], h[7])}, rw, WS_H + eo, 0, SC1);
  };
  {
    f32x4 acc[4];
    u32x4 A[2][4], B[2][4];
#pragma unroll
    for (int v = 0; v < 2; ++v) {
      A[v][0] = rdA(v, 0); SB();
#pragma unroll
      for (int m = 0; m < 4; ++m) { B[v][m] = rdB(v, m); SB(); }
#pragma unroll
      for (int m = 1; m < 4; ++m) { A[v][m] = rdA(v, m); SB(); }
      if (v == 0) {
        vmw<0>(ex); PIN(est[0]); PIN(est[1]); PIN(est[2]); PIN(est[3]); PIN(fl);
        if (!__all(fl == tag)) poll<2>(ws, WS_F2 + (u32)(4 * w + (l & 3)) * 128, true, tag);
        if (w < 4) terms();
        SB();
      }
    }
#pragma unroll
    for (int v = 0; v < 2; ++v)
#pragma unroll
      for (int m = 0; m < 4; ++m) {
#pragma unroll
        for (int c = m; c < 4; ++c) acc[c] = mfma16(A[v][m], B[v][c - m], (v == 0 && m == 0) ? f32x4{} : acc[c]);
        SB();
        if (v == 0 && m == 0) w1_g<7>(w1, L_RING);
        if (v == 0 && m == 2) w1_u<7>(w1, L_RING);
        if (v == 1 && m == 0) w1_g<8>(w1, L_RING + 16384);
        SB();
      }
#pragma unroll
    for (int c = 0; c < 4; ++c) *(f32x4*)(lds + rwr + c * 1024) = acc[c];
  }
  bar();
  STAMP(9);
  if (w < 4) epilogue();
  SB();
  STAMP(10);
  {
    f32x4 acc[4];
    u32x4 A[2][8], B[2][8];
    auto rd = [&](int v, int m) AI {
      if (m == 0) {
        A[v][0] = rdA(v, 0);
#pragma unroll
        for (int d = 4; d < 8; ++d) B[v][d] = rdB(v, d);
      } else { A[v][m] = rdA(v, m); if (m <= 4) B[v][4 - m] = rdB(v, 4 - m); }
      SB();
    };
    rd(0, 0); rd(0, 1); rd(0, 2); rd(0, 3);
#pragma unroll
    for (int v = 0; v < 2; ++v) {
#pragma unroll
      for (int m = 0; m < 8; ++m) {
#pragma unroll
        for (int c = 4; c < 8; ++c) if (m <= c) acc[c - 4] = mfma16(A[v][m], B[v][c - m], (v == 0 && m == 0) ? f32x4{} : acc[c - 4]);
        SB();
        if (m < 4) rd(v, m + 4); else if (v == 0) rd(1, m - 4);
        if (v == 0) {
          if (m == 0) w1_u<8>(w1, L_RING + 16384);
          if (m == 1) w1_g<9>(w1, L_RING + 32768);
          if (m == 2) w1_u<9>(w1, L_RING + 32768);
          if (m == 3) w1_g<10>(w1, L_RING + 49152);
          if (m == 4) w1_u<10>(w1, L_RING + 49152);
          if (m == 6) w1_g<11>(w1, L_RING + 65536);
          if (m == 7) w1_u<11>(w1, L_RING + 65536);
        }
        SB();
      }
    }
    if (w >= 4) terms();
    STAMP(11);
    VMW(0); bar();
#pragma unroll
    for (int c = 0; c < 4; ++c) *(f32x4*)(lds + rwr + c * 1024) = acc[c];
  }
  const u32 lo16 = l * 16;
  if (w < 4) {
#pragma unroll
    for (int k = 0; k < 7; ++k) dma<SC1>(dw, lb + L_H0 + (7 * w + k) * 1024, lo16, WS_H + (u32)(2 * gi * TB + (7 * w + k) * 1024));
  }
  w1_read<7>(lds, ra1, W); w1_read<8>(lds, ra1 + 16384, W); w1_read<9>(lds, ra1 + 32768, W); w1_read<10>(lds, ra2, W); w1_read<11>(lds, ra2 + 16384, W);
  bar();
  STAMP(12);
  if (w >= 4) {
    epilogue();
    SB();
#pragma unroll
    for (int i = 0; i < 2; ++i) {
      W1 c = w1; c.lb -= i * 4096; c.vg -= i * (16 * F_ * 2); c.vu -= i * (16 * F_ * 2);
      w1_issue<12>(c, L_WE); w1_issue<13>(c, L_WE + 16384);
    }
  } else {
    const u32 ha = opq(L_H0 + 7 * w * 1024 + lo16);
#pragma unroll 1
    for (int it = 0; it < 4000; ++it) {
      VMW(0);
      u32 m = 0;
#pragma unroll
      for (int k = 0; k < 7; ++k) { const u32x4 v = *(const u32x4*)(lds + ha + k * 1024); m = max(m, max4(v)); }
      if (!__any(m == EMPTY)) break;
#pragma unroll
      for (int k = 0; k < 7; ++k) dma<SC1>(dw, lb + L_H0 + (7 * w + k) * 1024, lo16, WS_H + (u32)(2 * gi * TB + (7 * w + k) * 1024));
    }
    VMW(0);
    STAMP(13);
  }
  bar();
  STAMP(14);

  u64 csv;
  const u32 bta = (u32)((8 * (G >> 1) + q4) * 64 + (G & 1) * 32 + p4 * 8);
  const u32 sta = (u32)((56 * w + 4 * (G >> 1) + q4) * 64 + (G & 1) * 32 + p4 * 8);
  auto hbase = [](int g) AI { return g == 0 ? L_H0 : g == 1 ? L_H1 : g == 2 ? L_H2 : L_H3; };
  const u32 spa = opq(L_SP + l * 32);
  float s1a, s1b, s2a, s2b, xs; u32x2 st[2];
  auto stats_step = [&](u32 b0, int k) AI {
    if (k > 0) {
      if (k & 1) { s1a = k == 1 ? xs : s1a + xs; s2a = k == 1 ? xs * xs : s2a + xs * xs; PIN(s1a); PIN(s2a); }
      else       { s1b = k == 2 ? xs : s1b + xs; s2b = k == 2 ? xs * xs : s2b + xs * xs; PIN(s1b); PIN(s2b); }
    }
    if (k < 28) {
      const int u = k >> 2;
      const u32 d = (k & 2) ? st[u & 1].y : st[u & 1].x;
      xs = (k & 1) ? bfhi(d) : bflo(d); PIN(xs);
      if ((k & 3) == 3 && u + 2 < 7) st[u & 1] = trd(lds, b0 + (u + 2) * 512);
    } else *(float*)(lds + spa + w * 4) = xsum(s1a + s1b, s2a + s2b);
  };
  f32x4 sp0, sp1, tm; float gu[8], gt[8]; u32 od[4];
  auto center = [&](f32x16& acc, float m) AI {
    const u32 d = __float_as_uint(m);
    acc = mfma32(u32x4{(u32)csv, (u32)(csv >> 32), 0, 0}, u32x4{d, d & 0xffffu, 0, 0}, acc);
  };
  auto geglu_step = [&](int g, f32x16& acc, int i) AI {
    if (g == 0) {
      if (i == 1) { sp0 = *(const f32x4*)(lds + spa); sp1 = *(const f32x4*)(lds + spa + 16); }
      if (i == 3) { sp0.x = op(sp0.x + sp0.y); sp0.z = op(sp0.z + sp0.w); sp1.x = op(sp1.x + sp1.y); sp1.z = op(sp1.z + sp1.w); PIN(sp0); PIN(sp1); }
      if (i == 4) { tm = ln2_terms((sp0.x + sp0.z) + (sp1.x + sp1.z)); PIN(tm); }
      if (i == 5) center(acc, tm.x);
    }
#pragma unroll
    for (int e = 0; e < 8; ++e) {
      const int s = i - (g == 0 ? 7 + (13 * e) / 7 : 2 + (18 * e) / 7);
      if (s == 0) { gt[e] = acc[e] * acc[e]; gu[e] = op(acc[e] * acc[e + 8]); PIN(gt[e]); PIN(gu[e]); }
      if (s == 1) { gt[e] = tm.w * gt[e] + tm.z; PIN(gt[e]); }
      if (s == 2) { gt[e] = acc[e] * gt[e]; PIN(gt[e]); }
      if (s == 3) { gt[e] = __builtin_amdgcn_exp2f(gt[e]); PIN(gt[e]); }
      if (s == 4) { gt[e] = gt[e] * tm.y + tm.y; PIN(gt[e]); }
      if (s == 5) { gt[e] = __builtin_amdgcn_rcpf(gt[e]); PIN(gt[e]); }
      if (s == 6) { gu[e] = gu[e] * gt[e]; PIN(gu[e]); }
      if (s == 7 && (e & 1)) { od[e >> 1] = pk(gu[e - 1], gu[e]); PIN(od[e >> 1]); }
    }
    if (i == 28) {
      __builtin_amdgcn_raw_buffer_store_b128(u32x4{od[0], od[1], od[2], od[3]}, rw, gso, gofs(g), SC1);
    }
  };
  constexpr int PF = 4;
  u32x4 B[PF];
  u32 ba = opq(hbase(0) + bta);
  auto chain = [&](int g, f32x16& acc, auto side) AI {
    const u32 bn = opq(hbase(g < 3 ? g + 1 : g) + bta);
    sfor<28>([&](auto kc) AI {
      constexpr int k = decltype(kc)::value;
      acc = mfma32(W[k], B[k % PF], k == 0 ? f32x16{} : acc);
      SB();
      side(k);
      if (k + PF < 28) B[k % PF] = cat(trd(lds, ba + (k + PF) * 1024), trd(lds, ba + (k + PF) * 1024 + 256));
      else if (g < 3) B[k % PF] = cat(trd(lds, bn + (k + PF - 28) * 1024), trd(lds, bn + (k + PF - 28) * 1024 + 256));
      SB();
    });
    ba = bn;
  };
  auto flag3 = [&](int g) AI { setflag(ws, WS_F3 + (u32)(((gi * 4 + g) * 14) + gj) * 128, tag); };
  const int ri = 2 * xc + sc / 14, rq2 = (sc / 7) & 1, n2 = sc % 7;
  const u32x4 rd = srd(gwd, D_ * F_ * 2);
  const u32 wdv = (u32)(((224 * w + r8) * D_ + 64 * n2) * 2 + (((l & 7) ^ (((r8 >> 1) & 3) << 1)) << 4));
  const u32 blkR = w < 7 ? L_R + w * 4096 : L_R2;
  u32 wda[4];
#pragma unroll
  for (int nt = 0; nt < 4; ++nt) wda[nt] = (16 * (G >> 1) + 4 * (G & 1) + q4) * 128 + ((nt ^ (2 * (G & 1) + (q4 >> 1))) * 32) + p4 * 8;
  u32x4 Wd[7][4];
  auto wd_blk = [&](int k) AI { return k % 3 == 1 ? blkR : (k % 3 == 2 ? L_Q : L_P) + w * 4096; };
  auto wd_dma = [&](int k, int i) AI { dma(rd, lb + wd_blk(k) + i * 1024, wdv, (4 * k + i) * (8 * D_ * 2)); };
  auto wd_read = [&](int k) AI {
#pragma unroll
    for (int nt = 0; nt < 4; ++nt) { const u32 a = opq(wda[nt] + wd_blk(k)); Wd[k][nt] = cat(trd(lds, a), trd(lds, a + 1024)); }
  };
  const int j0 = (14 * w) >> 3, jn = ((14 * w + 13) >> 3) - j0;
  const u32 f3o = WS_F3 + (u32)(ri * 56 + j0 + ((l & 3) < jn ? (l & 3) : jn)) * 128;

  __builtin_amdgcn_s_setprio(1);
  f32x16 accA, accB;
  u32x4 tm1, tm2, tm3;
  const u32 s2o = WS_S2 + (u32)(2 * gi * 512 + tk * 16);
  auto h_dma = [&](int g, int k) AI {
    if (k < 3 || l < 32) dma<SC1>(dw, lb + hbase(g) + w * 3584 + k * 1024, lo16, WS_H + (u32)((32 * (g >> 1) + 2 * gi + (g & 1)) * TB + w * 3584 + k * 1024));
  };
  const u32 cso = l < 32 ? WS_CS + (u32)(((gj * 8 + w) * 32 + l) * 8) : OOB;
  u32x4 cv[4];
  auto h_rd = [&](int g) AI {
    const u32 a = opq(hbase(g) + w * 3584 + lo16), a3 = opq(hbase(g) + w * 3584 + 3072 + tk * 16);
    cv[0] = *(const u32x4*)(lds + a); cv[1] = *(const u32x4*)(lds + a + 1024); cv[2] = *(const u32x4*)(lds + a + 2048); cv[3] = *(const u32x4*)(lds + a3);
  };
  auto h_fix = [&](int g) AI {
    u32 cm = g == 1 ? max((u32)csv, (u32)(csv >> 32)) : g == 2 ? max4(tm1) : max(max4(tm2), max4(tm3));
    cm = max(max(cm, max(max4(cv[0]), max4(cv[1]))), max(max4(cv[2]), max4(cv[3])));
#pragma unroll 1
    for (int it = 0; it < 4000 && __any(cm == EMPTY); ++it) {
      h_dma(g, 0); h_dma(g, 1); h_dma(g, 2); h_dma(g, 3);
      if (g == 1) { csv = flag_ld(dw, cso); VMWP(0, csv); }
      if (g == 2) { tm1 = ldg(dw, s2o, 512); vmw<0>(tm1); }
      if (g == 3) { tm2 = ldg(dw, s2o, 32 * 512); tm3 = ldg(dw, s2o, 33 * 512); vmw<0>(tm2); PIN(tm3); }
      h_rd(g);
      cm = g == 1 ? max((u32)csv, (u32)(csv >> 32)) : g == 2 ? max4(tm1) : max(max4(tm2), max4(tm3));
      cm = max(max(cm, max(max4(cv[0]), max4(cv[1]))), max(max4(cv[2]), max4(cv[3])));
    }
  };
  SB();
  {
    const u32 b0 = opq(hbase(0) + sta);
#pragma unroll
    for (int k = 0; k < PF; ++k) B[k] = cat(trd(lds, ba + k * 1024), trd(lds, ba + k * 1024 + 256));
    st[0] = trd(lds, b0); st[1] = trd(lds, b0 + 512);
    SB();
    chain(0, accA, [&](int k) AI {
      stats_step(b0, k);
      if (k <= 6 && !(k & 1)) h_dma(1, k >> 1);
      if (k == 7) csv = flag_ld(dw, cso);
      if (k == 10 || k == 13) __builtin_amdgcn_raw_buffer_store_b128(u32x4{EMPTY, EMPTY, EMPTY, EMPTY}, rw, gso, gofs(k == 10 ? 2 : 3), SC1);
      if (k == 14) { VMW(7); RBAR(); const u32 ra3 = opq(w1.ra + L_WE); w1_read<12>(lds, ra3, W); w1_read<13>(lds, ra3 + 16384, W); }
      if (k == 19) { VMWP(2, csv); h_rd(1); }
      if (k == 22) { h_fix(1); RBAR(); }
      if (k >= 23 && (k & 1)) h_dma(2, (k - 23) >> 1);
    });
    stats_step(b0, 28);
  }
  STAMP(16);
  chain(1, accB, [&](int k) AI {
    if (k == 1) { bar(); if (tid == 0 && b == 0) setflag(ws, WS_E, tag); h_dma(2, 3); }
    geglu_step(0, accA, k);
    if (k == 3) tm1 = ldg(dw, s2o, 512);
    if (k >= 5 && k <= 14 && k % 3 == 2) wd_dma(0, (k - 5) / 3);
    if (k >= 16 && k <= 25 && k % 3 == 1) h_dma(3, (k - 16) / 3);
    if (k == 20) { vmw<6>(tm1); h_rd(2); }
    if (k == 23) { h_fix(2); RBAR(); }
  });
  center(accB, __uint_as_float(tm1.x));
  geglu_step(0, accA, 28);
  tm = __builtin_bit_cast(f32x4, tm1);
  SB();
  STAMP(18);
  chain(2, accA, [&](int k) AI {
    if (k == 1) RBAR();
    geglu_step(1, accB, k);
    if (k <= 6 && !(k & 1)) wd_dma(1, k >> 1);
    if (k == 3) { VMW(7); wd_read(0); }
    if (k == 8) { tm2 = ldg(dw, s2o, 32 * 512); tm3 = ldg(dw, s2o, 33 * 512); }
    if (k >= 10 && k <= 24 && !(k & 1)) wd_dma(2 + ((k - 10) >> 3), ((k - 10) >> 1) & 3);
    if (k == 19) { VMW(11); h_rd(3); }
    if (k == 23) { vmw<7>(tm2); PIN(tm3); h_fix(3); RBAR(); if (tid == 0) flag3(0); }
  });
  center(accA, __uint_as_float(tm2.x));
  geglu_step(1, accB, 28);
  tm = __builtin_bit_cast(f32x4, tm2);
  SB();
  STAMP(20);
  chain(3, accB, [&](int k) AI {
    geglu_step(2, accA, k);
    if (k == 12) fl = flag_ld(dw, f3o);
    if (k == 15) { VMW(1); RBAR(); if (tid == 0) flag3(1); }
    if (k >= 15 && k < 18) wd_read(k - 14);
    if (k == 19) LG0();
    if (k >= 19 && k < 27 && (k & 1)) wd_dma(4, (k - 19) >> 1);
  });
  center(accB, __uint_as_float(tm3.x));
  geglu_step(2, accA, 28);
  tm = __builtin_bit_cast(f32x4, tm3);
  STAMP(21);
#pragma unroll
  for (int e = 0; e < 8; ++e) {
    const float a = accB[e], u = accB[e + 8];
    gu[e] = (a * u) * __builtin_amdgcn_rcpf(__builtin_amdgcn_exp2f(a * (tm.w * (a * a) + tm.z)) * tm.y + tm.y);
  }
#pragma unroll
  for (int e = 0; e < 4; ++e) od[e] = pk(gu[2 * e], gu[2 * e + 1]);
  geglu_step(3, accB, 28);
  __builtin_amdgcn_s_setprio(0);
  SB();
  STAMP(22);
  const u32 gb = WS_G + (u32)((2 * ri * 2 + rq2) * 56 + 7 * w) * 1024;
  u32x4 Hf[3][7];
  auto h_load = [&](int g, int k0, int k1) AI {
#pragma unroll
    for (int k = k0; k < k1; ++k) Hf[g % 3][k] = ldg(dw, lo16, gb + gofs(g) + k * 1024);
  };
  const rsrc_t ro = mk(gout, S_ * D_ * 2);
  const u32 pw = (u32)(i16 * 256), tf = 4 * (w & 3) + G;
  f32x4 acc[4];
  u32x2 res[4];
  auto step = [&](auto gc, auto kc, auto nc) AI {
    constexpr int g = decltype(gc)::value, k = decltype(kc)::value;
    vmw<decltype(nc)::value>(Hf[g % 3][k]);
#pragma unroll
    for (int nt = 0; nt < 4; ++nt) acc[nt] = mfma16(Wd[k][nt], Hf[g % 3][k], k == 0 ? f32x4{} : acc[nt]);
    SB();
  };
  auto put = [&](int g) AI {
    const u32 rb = opq(((g & 1) ? L_Q : L_P) + w * 4096 + pw);
#pragma unroll
    for (int nt = 0; nt < 4; ++nt) *(f32x4*)(lds + rb + (((4 * nt + G) ^ (i16 & 7)) << 4)) = acc[nt];
    *(f32x4*)(lds + L_RES + (g & 1) * 4096 + pw + (((2 * w + (G & 1)) ^ (i16 & 7)) << 4)) = f32x4{bflo(res[g].x), bfhi(res[g].x), bflo(res[g].y), bfhi(res[g].y)};
  };
  auto fin = [&](int g) AI {
    if ((w >> 2) == (g & 1)) {
      const u32 a0 = tf * 256 + ((i16 ^ (tf & 7)) << 4), a = opq(((g & 1) ? L_Q : L_P) + a0);
      f32x4 sum = *(const f32x4*)(lds + L_RES + (g & 1) * 4096 + a0);
#pragma unroll
      for (int n = 0; n < 8; ++n) sum += *(const f32x4*)(lds + a + n * 4096);
      const int tok = 32 * (32 * (g >> 1) + (g & 1) + 2 * ri) + 16 * rq2 + tf;
      __builtin_amdgcn_raw_buffer_store_b64(u32x2{pk(sum.x, sum.y), pk(sum.z, sum.w)}, ro, (u32)((tok * D_ + 64 * n2 + 4 * i16) * 2), 0, 0);
    }
  };
  u32 chk;
  auto stepc = [&](auto gc, auto kc, auto nc) AI {
    constexpr int g = decltype(gc)::value, k = decltype(kc)::value;
    step(gc, kc, nc);
    const u32x4 v = Hf[g % 3][k];
    chk = k == 0 ? max4(v) : max(chk, max4(v));
    PIN(chk);
  };
  auto redo = [&](auto gc) AI {
    constexpr int g = decltype(gc)::value;
#pragma unroll 1
    for (int it = 0; it < 4000 && __any(chk == EMPTY); ++it) {
      h_load(g, 0, 7);
      stepc(gc, IC<0>{}, IC<6>{}); stepc(gc, IC<1>{}, IC<5>{}); stepc(gc, IC<2>{}, IC<4>{}); stepc(gc, IC<3>{}, IC<3>{});
      stepc(gc, IC<4>{}, IC<2>{}); stepc(gc, IC<5>{}, IC<1>{}); stepc(gc, IC<6>{}, IC<0>{});
    }
  };
  VMWP(6, fl);
  if (!__all(fl == tag)) poll(ws, f3o, true, tag);
  wd_dma(5, 0); wd_dma(5, 1); wd_dma(5, 2); wd_dma(5, 3);
  h_load(0, 0, 3);
  fl = flag_ld(dw, f3o + 14 * 128);
  h_load(0, 3, 7);
  const u32 hro = WS_H + (u32)((32 * (G >> 1) + (G & 1) + 2 * ri) * TB + (64 * n2 + 8 * w + (i16 >> 1)) * 64 + rq2 * 32 + (i16 & 1) * 16);
  u32x4 hr = ldg(dw, hro, 0);
  wd_dma(6, 0); wd_dma(6, 1); wd_dma(6, 2); wd_dma(6, 3);
  VMWP(9, fl);
  if (!__all(fl == tag)) poll(ws, f3o + 14 * 128, true, tag);
  wd_read(4); wd_read(5);
  SB();
  STAMP(23);
  h_load(1, 0, 1); step(IC<0>{}, IC<0>{}, IC<10>{});
  h_load(1, 1, 2); step(IC<0>{}, IC<1>{}, IC<11>{});
  h_load(1, 2, 3); step(IC<0>{}, IC<2>{}, IC<12>{});
  h_load(1, 3, 4); step(IC<0>{}, IC<3>{}, IC<12>{});
  h_load(1, 4, 5); step(IC<0>{}, IC<4>{}, IC<12>{});
  h_load(1, 5, 6); step(IC<0>{}, IC<5>{}, IC<12>{});
  h_load(1, 6, 7);
  vmw<7>(hr);
#pragma unroll 1
  for (int it = 0; it < 4000 && __any(max4(hr) == EMPTY); ++it) { hr = ldg(dw, hro, 0); vmw<0>(hr); }
  wd_read(6);
  *(u32x4*)(lds + blkR + l * 16) = hr;
#pragma unroll
  for (int g = 0; g < 4; ++g) res[g] = trd(lds, blkR + g * 256 + (4 * (G & 1) + q4) * 32 + p4 * 8);
  h_load(2, 0, 2);
  step(IC<0>{}, IC<6>{}, IC<9>{});
  put(0);
  STAMP(24);
  h_load(2, 2, 3); step(IC<1>{}, IC<0>{}, IC<9>{});
  h_load(2, 3, 5); step(IC<1>{}, IC<1>{}, IC<10>{});
  h_load(2, 5, 7); step(IC<1>{}, IC<2>{}, IC<11>{});
  h_load(3, 0, 2); step(IC<1>{}, IC<3>{}, IC<12>{});
  h_load(3, 2, 4); step(IC<1>{}, IC<4>{}, IC<13>{});
  h_load(3, 4, 6); step(IC<1>{}, IC<5>{}, IC<14>{});
  h_load(3, 6, 7); step(IC<1>{}, IC<6>{}, IC<14>{});
  put(1);
  STAMP(25);
  bar();
  fin(0); fin(1);
  SB();
  stepc(IC<2>{}, IC<0>{}, IC<13>{}); stepc(IC<2>{}, IC<1>{}, IC<12>{}); stepc(IC<2>{}, IC<2>{}, IC<11>{}); stepc(IC<2>{}, IC<3>{}, IC<10>{});
  stepc(IC<2>{}, IC<4>{}, IC<9>{}); stepc(IC<2>{}, IC<5>{}, IC<8>{}); stepc(IC<2>{}, IC<6>{}, IC<7>{});
  redo(IC<2>{});
  bar();
  put(2);
  SB();
  STAMP(26);
  stepc(IC<3>{}, IC<0>{}, IC<6>{}); stepc(IC<3>{}, IC<1>{}, IC<5>{}); stepc(IC<3>{}, IC<2>{}, IC<4>{}); stepc(IC<3>{}, IC<3>{}, IC<3>{});
  stepc(IC<3>{}, IC<4>{}, IC<2>{}); stepc(IC<3>{}, IC<5>{}, IC<1>{}); stepc(IC<3>{}, IC<6>{}, IC<0>{});
  redo(IC<3>{});
  put(3);
  bar();
  STAMP(27);
  fin(2); fin(3);
  STAMP(28);
  STAMP_OUT();
}

__global__ void k_pre(char* ws) { if (threadIdx.x == 0) __hip_atomic_store((GAS u64*)(ws + WS_DBG), (u64)__builtin_amdgcn_s_memrealtime(), RLX_AGENT); }
__global__ void k_post(char* ws, int mult) { if (threadIdx.x == 0) { const u64 t = __builtin_amdgcn_s_memrealtime(); const u64 e = __hip_atomic_load((GAS u64*)(ws + WS_DBG + 128), RLX_AGENT); u64 d = t - e; if (d > 5000) d = 5000; const u64 te = t + mult * d; while (__builtin_amdgcn_s_memrealtime() < te) __builtin_amdgcn_s_sleep(1); } }
#define R8(x) x x x x x x x x
__global__ __launch_bounds__(512, 2) void k_x(char* ws, int n, int m) {
  const u64 t = __builtin_amdgcn_s_memrealtime();
  __shared__ char lds[16];
  if (n < 0) lds[threadIdx.x] = n;

  if (blockIdx.x == 9 && threadIdx.x == 0) __hip_atomic_store((GAS u64*)(ws + WS_DBG + 256), t, RLX_AGENT);
  if (n < 0) ws[1] = lds[5];

}
}

extern "C" void kernel_launch(void* const* d_in, const int* in_sizes, int n_in, void* d_out, int out_size, void* d_ws, size_t ws_size,
                              hipStream_t stream) {
  k_pre<<<1, 64, 0, stream>>>((char*)d_ws);
  k_x<<<256, 512, 0, stream>>>((char*)d_ws, 1, 1);
  h3_ffn<<<256, 512, 0, stream>>>((const char*)d_in[0], (const char*)d_in[1], (const char*)d_in[2], (const char*)d_in[3], (const char*)d_in[4],
                                  (const char*)d_in[5], (const char*)d_in[7], (const char*)d_in[8], (const char*)d_in[9], (char*)d_out, (char*)d_ws);
}
```

```cpp
#include <hip/hip_runtime.h>
#include <stdint.h>

namespace {

#define LAS __attribute__((address_space(3)))
#define GAS __attribute__((address_space(1)))
#define DI __device__ __forceinline__
#define AI __attribute__((always_inline))
typedef uint32_t u32;
typedef unsigned long long u64;
typedef u32 u32x2 __attribute__((ext_vector_type(2)));
typedef u32 u32x4 __attribute__((ext_vector_type(4)));
typedef short s16x4 __attribute__((ext_vector_type(4)));
typedef short s16x8 __attribute__((ext_vector_type(8)));
typedef float f32x4 __attribute__((ext_vector_type(4)));
typedef float f32x16 __attribute__((ext_vector_type(16)));
typedef __bf16 bf2 __attribute__((ext_vector_type(2)));
typedef __amdgpu_buffer_rsrc_t rsrc_t;
#define RLX_AGENT __ATOMIC_RELAXED, __HIP_MEMORY_SCOPE_AGENT

constexpr int S_ = 2048, D_ = 448, F_ = 1792;
constexpr u32 TB = 448 * 64;
constexpr u32 WS_E  = 0;
constexpr u32 WS_F1 = 128;
constexpr u32 WS_F3 = WS_F1 + 256 * 128;
constexpr u32 WS_F2 = WS_F3 + 896 * 128;
constexpr u32 WS_S2 = 256 * 1024 + 16384;
constexpr u32 WS_CS = 224 * 1024;
constexpr u32 WS_ST = 256 * 1024;
constexpr u32 WS_U  = WS_S2 + 32768;
constexpr u32 WS_X  = WS_U + 64 * TB;
constexpr u32 WS_H  = WS_X + 64 * TB;
constexpr u32 WS_G  = WS_H + 64 * TB;
constexpr u32 WS_END = WS_G + 64 * 112 * 1024;
constexpr u32 WS_DBG = 16u << 20;
constexpr int SC1 = 16;
#define COLSUM_DELAY 16
constexpr u32 OOB = 0xFFFFFF00u;
constexpr u32 ONES = 0x3f803f80u;
constexpr u32 EMPTY = 0xffffffffu;

constexpr u32 L_K = 0, L_V = 9216, L_X = 17408, L_SK = 25600;
constexpr u32 L_RED = 0, L_FC = 32768, FCS = 4160, FCH = 4 * FCS;
constexpr u32 L_U = L_FC + 2 * FCH, UCH = 4608, L_RING = L_U + 2 * UCH;
constexpr u32 L_H0 = 32768, L_H1 = L_H0 + TB, L_H2 = 0, L_H3 = L_H1 + TB + 32768, L_SP = 155648;
constexpr u32 L_P = TB, L_Q = L_P + 32768, L_R = L_Q + 32768, L_R2 = L_H3 + TB;
constexpr u32 L_RES = 0;
constexpr u32 LDS_BYTES = 163840;

DI float bflo(u32 x) { return __uint_as_float(x << 16); }
DI float bfhi(u32 x) { return __uint_as_float(x & 0xffff0000u); }
DI u32 pk(float lo, float hi) { bf2 p; p.x = (__bf16)lo; p.y = (__bf16)hi; return __builtin_bit_cast(u32, p); }
DI rsrc_t mk(const void* p, u32 bytes) { return __builtin_amdgcn_make_buffer_rsrc((void*)p, 0, bytes, 0x00020000); }
DI u32x2 trd(const char* lds, u32 a) { return __builtin_bit_cast(u32x2, __builtin_amdgcn_ds_read_tr16_b64_v4i16((LAS s16x4*)(lds + a))); }
DI u32x4 cat(u32x2 a, u32x2 b) { return u32x4{a.x, a.y, b.x, b.y}; }
DI u32x4 srd(const void* p, u32 bytes) { const u64 a = (u64)p; return u32x4{(u32)a, (u32)(a >> 32) & 0xffffu, bytes, 0x00020000u}; }
template <int AUX = 0> DI void dma(u32x4 r, u32 ldsoff, u32 voff, u32 soff) {
  if (AUX) asm volatile("s_mov_b32 m0, %0\n\ts_nop 0\n\tbuffer_load_dwordx4 %1, %2, %3 offen sc1 lds" :: "s"(ldsoff), "v"(voff), "s"(r), "s"(soff) : "memory");
  else asm volatile("s_mov_b32 m0, %0\n\ts_nop 0\n\tbuffer_load_dwordx4 %1, %2, %3 offen lds" :: "s"(ldsoff), "v"(voff), "s"(r), "s"(soff) : "memory");
}
DI void dma_at(u32 ldsoff) { asm volatile("s_mov_b32 m0, %0\n\ts_nop 0" :: "s"(ldsoff) : "memory"); }
template <int OFF, int AUX> DI void dma_go(u32x4 r, u32 voff, u32 soff) {
  if (AUX) asm volatile("buffer_load_dwordx4 %0, %1, %2 offen offset:%3 sc1 lds" :: "v"(voff), "s"(r), "s"(soff), "n"(OFF) : "memory");
  else asm volatile("buffer_load_dwordx4 %0, %1, %2 offen offset:%3 lds" :: "v"(voff), "s"(r), "s"(soff), "n"(OFF) : "memory");
}
template <int AUX> DI void dma_i(int i, u32x4 r, u32 voff, u32 soff) {
  if (i == 0) dma_go<0, AUX>(r, voff, soff); else if (i == 1) dma_go<1024, AUX>(r, voff, soff); else if (i == 2) dma_go<2048, AUX>(r, voff, soff); else dma_go<3072, AUX>(r, voff, soff);
}
DI float fnma(float a, float b, float c) { float r; asm("v_fma_f32 %0, -%1, %2, %3" : "=v"(r) : "v"(a), "v"(b), "v"(c)); return r; }
DI float op(float x) { asm("" : "+v"(x)); return x; }
DI u32 opq(u32 x) { asm volatile("" : "+v"(x)); return x; }
#define PIN(x) asm volatile("" : "+v"(x))
DI f32x4 mfma16(u32x4 a, u32x4 b, f32x4 c) { return __builtin_amdgcn_mfma_f32_16x16x32_bf16(__builtin_bit_cast(s16x8, a), __builtin_bit_cast(s16x8, b), c, 0, 0, 0); }
DI f32x16 mfma32(u32x4 a, u32x4 b, f32x16 c) { return __builtin_amdgcn_mfma_f32_32x32x16_bf16(__builtin_bit_cast(s16x8, a), __builtin_bit_cast(s16x8, b), c, 0, 0, 0); }
DI f32x4 mfma4(u32x2 a, u32x2 b, f32x4 c) { return __builtin_amdgcn_mfma_f32_4x4x4bf16_1k(__builtin_bit_cast(s16x4, a), __builtin_bit_cast(s16x4, b), c, 0, 0, 0); }
DI float dot2(u32 a, u32 b, float c) { return __builtin_amdgcn_fdot2_f32_bf16(__builtin_bit_cast(bf2, a), __builtin_bit_cast(bf2, b), c, false); }
#define VMW(n) asm volatile("s_waitcnt vmcnt(" #n ")" ::: "memory")
#define LG0() asm volatile("s_waitcnt lgkmcnt(0)" ::: "memory")
#define SB() __builtin_amdgcn_sched_barrier(0)
DI void bar() { asm volatile("s_waitcnt lgkmcnt(0)\n\ts_barrier" ::: "memory"); }
#define RBAR() asm volatile("s_barrier" ::: "memory")
DI float wsum(float v) {
  v += __builtin_amdgcn_update_dpp(0.f, v, 0x111, 0xf, 0xf, true);
  v += __builtin_amdgcn_update_dpp(0.f, v, 0x112, 0xf, 0xf, true);
  v += __builtin_amdgcn_update_dpp(0.f, v, 0x114, 0xf, 0xf, true);
  v += __builtin_amdgcn_update_dpp(0.f, v, 0x118, 0xf, 0xf, true);
  v += __builtin_amdgcn_update_dpp(0.f, v, 0x142, 0xa, 0xf, true);
  v += __builtin_amdgcn_update_dpp(0.f, v, 0x143, 0xc, 0xf, true);
  return __uint_as_float(__builtin_amdgcn_readlane(__float_as_uint(v), 63));
}
DI float hsum(float v) {
  const auto r = __builtin_amdgcn_permlane32_swap(__float_as_uint(v), __float_as_uint(v), false, false);
  return __uint_as_float(r[0]) + __uint_as_float(r[1]);
}
template <int SLEEP = 1> DI void poll(char* ws, u32 off, bool act, u64 tag) {
  bool ok = !act;
#pragma unroll 1
  for (int it = 0; it < 20000; ++it) {
    u64 x = tag;
    if (!ok) x = __hip_atomic_load((GAS u64*)(ws + off), RLX_AGENT);
    ok = (x == tag);
    if (__all(ok)) break;
    __builtin_amdgcn_s_sleep(SLEEP);
  }
  asm volatile("" ::: "memory");
}
DI u32 max4(u32x4 v) { return max(max(v.x, v.y), max(v.z, v.w)); }
DI u64 flag_ld(u32x4 r, u32 voff) { u64 v; asm volatile("buffer_load_dwordx2 %0, %1, %2, 0 offen sc1" : "=v"(v) : "v"(voff), "s"(r) : "memory"); return v; }
DI u32x4 ldg(u32x4 r, u32 voff, u32 soff) { u32x4 v; asm volatile("buffer_load_dwordx4 %0, %1, %2, %3 offen sc1" : "=v"(v) : "v"(voff), "s"(r), "s"(soff) : "memory"); return v; }
template <int N> DI void vmw(u32x4& x) { asm volatile("s_waitcnt vmcnt(%1)" : "+v"(x) : "n"(N) : "memory"); }
#define VMWP(n, x) asm volatile("s_waitcnt vmcnt(" #n ")" : "+v"(x) :: "memory")
DI void setflag(char* ws, u32 off, u64 tag) { __hip_atomic_store((GAS u64*)(ws + off), tag, RLX_AGENT); }

#define STAMP(i)
#define STAMPC(i)
#define REDO_COUNT()
#define STAMP_OUT()

DI float xsum(float a, float c) {
  const auto r = __builtin_amdgcn_permlane32_swap(__float_as_uint(a), __float_as_uint(c), false, false);
  return __uint_as_float(r[0]) + __uint_as_float(r[1]);
}
DI f32x4 ln2_terms(float t) {
  const auto r = __builtin_amdgcn_permlane32_swap(__float_as_uint(t), __float_as_uint(t), false, false);
  const float mean = __uint_as_float(r[0]) * (1.f / D_);
  const float V = fmaxf(__uint_as_float(r[1]) * (1.f / D_) - mean * mean, 0.f) + 1e-5f;
  const float rs = __builtin_amdgcn_rsqf(V);
  const float K1 = -2.f * 0.7978845608028654f * 1.4426950408889634f * rs;
  const float lo = -mean - bflo(pk(-mean, 0.f));
  return f32x4{__uint_as_float(pk(-mean, lo)), V, K1, (K1 * rs) * (0.044715f * rs)};
}
template <int N> struct IC { static constexpr int value = N; };
template <int N, int I = 0, class F> DI void sfor(F&& f) { if constexpr (I < N) { f(IC<I>{}); sfor<N, I + 1>(f); } }
struct W1 { u32x4 rg, ru; u32 vg, vu, ra, lb; };
template <int T> DI void w1_g(const W1& c, u32 base) { dma(c.rg, c.lb + base, c.vg, T * (32 * F_ * 2)); }
template <int T> DI void w1_u(const W1& c, u32 base) { dma(c.ru, c.lb + base + 8192, c.vu, T * (32 * F_ * 2)); }
template <int T> DI void w1_issue(const W1& c, u32 base) { w1_g<T>(c, base); w1_u<T>(c, base); }
template <int T> DI void w1_read(const char* lds, u32 a, u32x4 (&W)[28]) {
#pragma unroll
  for (int kk = 0; kk < 2; ++kk) W[2 * T + kk] = cat(trd(lds, a + kk * 4096), trd(lds, a + kk * 4096 + 1024));
}
DI float x8sum(float v) {
  v += __builtin_amdgcn_update_dpp(0.f, v, 0xb1, 0xf, 0xf, true);
  v += __builtin_amdgcn_update_dpp(0.f, v, 0x4e, 0xf, 0xf, true);
  v += __builtin_amdgcn_update_dpp(0.f, v, 0x141, 0xf, 0xf, true);
  return v;
}

__global__ __launch_bounds__(512, 2) void h3_ffn(const char* __restrict__ gq, const char* __restrict__ gk, const char* __restrict__ gv,
                                                 const char* __restrict__ gsk, const char* __restrict__ gf, const char* __restrict__ gD,
                                                 const char* __restrict__ gwg, const char* __restrict__ gwu, const char* __restrict__ gwd,
                                                 char* __restrict__ gout, char* ws) {
  __shared__ __attribute__((aligned(16))) char lds[LDS_BYTES];
  const int tid = threadIdx.x, l = tid & 63, w = __builtin_amdgcn_readfirstlane(tid >> 6);
  const int b = blockIdx.x, xc = b & 7, sl = b >> 3;
  const int tb64 = 4 * xc + (sl >> 3), role = sl & 7;
  const int sc = (sl >> 3) * 7 + role;
  asm volatile("" :: "s"(gq), "s"(gk), "s"(gv), "s"(gsk), "s"(gf), "s"(gD), "s"(gwg), "s"(gwu), "s"(gwd), "s"(gout), "s"(ws));
  STAMP(0);
  const rsrc_t rw = mk(ws, WS_END);
  const int G = l >> 4, i16 = l & 15, q4 = i16 >> 2, p4 = l & 3, r8 = l >> 3;
  auto mktag = [&](u64 e0) AI { return ((u64)__builtin_amdgcn_readfirstlane((u32)e0) | ((u64)__builtin_amdgcn_readfirstlane((u32)(e0 >> 32)) << 32)) * 6364136223846793005ull + 1442695040888963407ull; };
  if (role == 7) {
    const rsrc_t rq = mk(gq, S_ * D_ * 2);
    const int row = 64 * tb64 + 8 * w + r8;
    u32x4 xs[7];
#pragma unroll
    for (int i = 0; i < 7; ++i) xs[i] = __builtin_amdgcn_raw_buffer_load_b128(rq, (u32)(row * 896 + (l & 7) * 16 + i * 128), 0, 0);
    const u64 e0 = __hip_atomic_load((GAS u64*)(ws + WS_E), RLX_AGENT);
    __builtin_amdgcn_raw_buffer_store_b128(u32x4{EMPTY, EMPTY, EMPTY, EMPTY}, rw, tid < 64 ? WS_S2 + (u32)(tb64 * 1024 + tid * 16) : tid < 120 ? WS_CS + (u32)(tb64 * 896 + (tid - 64) * 16) : OOB, 0, SC1);
    SB();
    const u64 tag = mktag(e0);
    float s[4] = {};
#pragma unroll
    for (int i = 0; i < 7; ++i)
#pragma unroll
      for (int e = 0; e < 4; ++e) s[e] += bflo(xs[i][e]) + bfhi(xs[i][e]);
    const float mean = x8sum((s[0] + s[1]) + (s[2] + s[3])) * (1.f / D_);
    float v[4] = {};
#pragma unroll
    for (int i = 0; i < 7; ++i)
#pragma unroll
      for (int e = 0; e < 4; ++e) { const float d0 = bflo(xs[i][e]) - mean, d1 = bfhi(xs[i][e]) - mean; v[e] += d0 * d0; v[e] += d1 * d1; }
    const float rstd = __builtin_amdgcn_rsqf(x8sum((v[0] + v[1]) + (v[2] + v[3])) * (1.f / D_) + 1e-5f);
    const u32 so = WS_ST + (row >> 8) * 2048 + ((row >> 1) & 3) * 512 + ((row >> 5) & 7) * 64 + ((row >> 3) & 3) * 16 + (row & 1) * 8;
    __builtin_amdgcn_raw_buffer_store_b64(u32x2{__float_as_uint(mean), __float_as_uint(rstd)}, rw, (l & 7) == 0 ? so : OOB, 0, SC1);
    VMW(0); bar();
    if (tid == 0) setflag(ws, WS_F1 + (tb64 * 8 + 7) * 128, tag);
    STAMP(1);
    __builtin_amdgcn_s_sleep(COLSUM_DELAY);
    {
      const int mat = tb64 >= 14, j = tb64 < 28 ? tb64 - 14 * mat : 0;
      const rsrc_t rm = mk(mat ? gwu : gwg, D_ * F_ * 2);
      u32x4 wv[14];
#pragma unroll
      for (int i = 0; i < 14; ++i) wv[i] = __builtin_amdgcn_raw_buffer_load_b128(rm, tb64 < 28 ? (u32)(((4 * w + G + 32 * i) * F_ + 128 * j) * 2 + i16 * 16) : OOB, 0, 0);
      float c[8] = {};
#pragma unroll
      for (int i = 0; i < 14; ++i)
#pragma unroll
        for (int e = 0; e < 4; ++e) { c[2 * e] += bflo(wv[i][e]); c[2 * e + 1] += bfhi(wv[i][e]); }
      *(f32x4*)(lds + (4 * w + G) * 512 + i16 * 32) = f32x4{c[0], c[1], c[2], c[3]};
      *(f32x4*)(lds + (4 * w + G) * 512 + i16 * 32 + 16) = f32x4{c[4], c[5], c[6], c[7]};
      poll(ws, WS_F1 + (u32)((l & 31) * 8 + 7) * 128, true, tag);
      bar();
      if (tid < 128 && tb64 < 28) {
        float t = 0.f;
#pragma unroll
        for (int n = 0; n < 32; ++n) t += *(const float*)(lds + n * 512 + tid * 4);
        const u32 hi = pk(t, t), lo = pk(t - bflo(hi), 0.f);
        __builtin_amdgcn_raw_buffer_store_b64(u32x2{hi, lo}, rw, WS_CS + (u32)(((j * 8 + (tid >> 4)) * 32 + mat * 16 + (tid & 15)) * 8), 0, SC1);
      }
    }
    {
      const rsrc_t rdw = mk(gwd, D_ * F_ * 2);
      const u32 t = __builtin_amdgcn_raw_buffer_load_b32(rdw, tid < 392 ? (u32)((tb64 * 392 + tid) * 128) : OOB, 0, 0);
      asm volatile("" :: "v"(t));
    }
    STAMP(2);
    const u32x4 dw = srd(ws, WS_END);
    const u32 lb = (u32)(size_t)(LAS char*)lds;
    if (w == 0) poll<8>(ws, WS_F2 + (u32)(tb64 * 8) * 128, l == 0, tag);
    bar();
    poll<2>(ws, WS_F2 + (u32)(32 * w + (l & 31)) * 128, (l & 7) != 7, tag);
    bar();
#pragma unroll 1
    for (int half = 0; half < 2; ++half) {
      const int tb = 32 * half + tb64;
#pragma unroll 1
      for (int it = 0; it < 4000; ++it) {
#pragma unroll
        for (int k = 0; k < 4; ++k) if (k < 3 || l < 32) dma<SC1>(dw, lb + 32768 + w * 3584 + k * 1024, l * 16, WS_H + (u32)(tb * TB + w * 3584 + k * 1024));
        VMW(0);
        u32 m = 0;
#pragma unroll
        for (int k = 0; k < 4; ++k) m = max(m, max4(*(const u32x4*)(lds + 32768 + w * 3584 + (k < 3 ? k * 1024 + l * 16 : 3072 + (l & 31) * 16))));
        if (!__any(m == EMPTY)) break;
        __builtin_amdgcn_s_sleep(3);
      }
      float s1 = 0.f, s2 = 0.f;
#pragma unroll
      for (int i = 0; i < 7; ++i) {
        const u32x2 t = trd(lds, 32768 + (u32)((56 * w + 4 * (G >> 1) + q4) * 64 + (G & 1) * 32 + p4 * 8) + i * 512);
        s1 = dot2(t.x, ONES, s1); s2 = dot2(t.x, t.x, s2); s1 = dot2(t.y, ONES, s1); s2 = dot2(t.y, t.y, s2);
      }
      *(float*)(lds + 20480 + half * 2048 + l * 32 + w * 4) = xsum(s1, s2);
      bar();
      if (w == 0) {
        const f32x4 a = *(const f32x4*)(lds + 20480 + half * 2048 + l * 32), c = *(const f32x4*)(lds + 20480 + half * 2048 + l * 32 + 16);
        __builtin_amdgcn_raw_buffer_store_b128(__builtin_bit_cast(u32x4, ln2_terms(((a.x + a.y) + (a.z + a.w)) + ((c.x + c.y) + (c.z + c.w)))), rw, l < 32 ? WS_S2 + (u32)(tb * 512 + l * 16) : OOB, 0, SC1);
      }
    }
    STAMP(3);
    STAMP_OUT();
    return;
  }
  const int cp = xc * 28 + sc, cch = w & 1, vp = w >> 1;
  const int ech = (l >> 2) & 1;
  const u32 eo = (u32)((8 * w + r8) * TB + (2 * cp + ech) * 64 + p4 * 16);
  u32x4 fhi; u32x2 flo; u32 Dh; u64 e0;
  const u32 lb = (u32)(size_t)(LAS char*)lds;
  const u32x4 dw = srd(ws, WS_END);
  {
    const u32x4 rk = srd(gk, S_ * D_ * 2), rv = srd(gv, S_ * D_ * 2);
    const int c8 = l & 7, s0 = 64 * tb64;
    const int swv = ((r8 >> 1) & 1) | ((w & 1) << 1), swk = swv ^ 2;
    const u32 g0 = (u32)((s0 + 8 * w + r8) * 896 + role * 128);
    if (w == 0) dma(rk, lb + L_K, g0 - 8 * 896 + ((c8 ^ (swv << 1)) << 4), 0);
    if (w == 1) dma(srd(gsk, D_ * 8), lb + L_SK, (u32)(512 * role + 16 * l), 0);
    dma(rk, lb + L_K + 1024 + w * 1024, g0 + ((c8 ^ (swk << 1)) << 4), 0);
    dma(rv, lb + L_V + w * 1024, g0 + ((c8 ^ (swv << 1)) << 4), 0);
    dma(srd(gq, S_ * D_ * 2), lb + L_X + w * 1024, g0 + ((c8 ^ (swv << 1)) << 4), 0);
    const u32x4 rf = srd(gf, S_ * D_ * 2);
    const int fq = vp * 64 + l;
    const u32 fo = (u32)((2 * cp + cch) * 4096 + 16 * fq);
    asm volatile("buffer_load_dwordx4 %0, %2, %4, 0 offen\n\tbuffer_load_dwordx2 %1, %3, %4, 0 offen" : "=&v"(fhi), "=&v"(flo) : "v"(fo), "v"(fq > 0 ? fo - 8 : OOB), "s"(rf) : "memory");
    asm volatile("buffer_load_ushort %0, %1, %2, 0 offen" : "=v"(Dh) : "v"((u32)((2 * cp + ((l >> 2) & 1)) * 2)), "s"(srd(gD, D_ * 2)) : "memory");
    e0 = flag_ld(dw, WS_E);
    SB();
  }
  const int gx = (xc >> 1) * 28 + sc, gi = 8 * (xc & 1) + (gx & 7), gj = gx >> 3;
  W1 w1;
  {
    const u32 v0 = (u32)(((4 * w + G) * F_ + 128 * gj) * 2);
    w1.rg = srd(gwg, D_ * F_ * 2); w1.ru = srd(gwu, D_ * F_ * 2); w1.lb = lb + w * 1024;
    w1.vg = v0 + ((i16 ^ (4 * G)) << 4); w1.vu = v0 + ((i16 ^ (4 * G + 2)) << 4);
    w1.ra = (u32)((G & 1) * 8192 + (8 * (G >> 1) + q4) * 256 + ((w ^ (2 * q4 + (G & 1))) * 32) + p4 * 8);
  }
  w1_issue<0>(w1, L_FC); w1_issue<1>(w1, L_FC + 16384);
  VMW(9); bar();
  STAMP(1);
  {
    const int tbl = w >> 2, chg = w & 3;
    const u32 rb = (u32)((32 * tbl + 8 * G + q4) * 128 + p4 * 8);
    const u32 sv = (u32)((chg ^ ((q4 >> 1) | ((G & 1) << 1))) * 32), sk2 = sv ^ 64;
    const u32x2 km = trd(lds, L_K + rb + 512 + sv), k0 = trd(lds, L_K + rb + 1024 + sk2), k1 = trd(lds, L_K + rb + 1536 + sk2);
    const u32x2 v0 = trd(lds, L_V + rb + sv), v1 = trd(lds, L_V + rb + 512 + sv);
    const u32x2 skv = *(const u32x2*)(lds + L_SK + (16 * chg + i16) * 8);
    const float kk[11] = {bfhi(km.x), bflo(km.y), bfhi(km.y), bflo(k0.x), bfhi(k0.x), bflo(k0.y), bfhi(k0.y), bflo(k1.x), bfhi(k1.x), bflo(k1.y), bfhi(k1.y)};
    const float vv[8] = {bflo(v0.x), bfhi(v0.x), bflo(v0.y), bfhi(v0.y), bflo(v1.x), bfhi(v1.x), bflo(v1.y), bfhi(v1.y)};
    const float s0 = bflo(skv.x), s1 = bfhi(skv.x), s2 = bflo(skv.y), s3 = bfhi(skv.y);
    float u[8];
#pragma unroll
    for (int e = 0; e < 8; ++e) u[e] = (kk[e + 3] * s0 + kk[e + 2] * s1 + kk[e + 1] * s2 + kk[e] * s3) * vv[e];
    const u32 o = (u32)((2 * tb64 + tbl) * TB + (64 * role + 16 * chg + i16) * 64 + G * 16);
    __builtin_amdgcn_raw_buffer_store_b128(u32x4{pk(u[0], u[1]), pk(u[2], u[3]), pk(u[4], u[5]), pk(u[6], u[7])}, rw, WS_U + o, 0, SC1);
    SB();
    STAMP(2);
    VMW(9); bar();
    const u32x2 x0 = trd(lds, L_X + rb + sv), x1 = trd(lds, L_X + rb + 512 + sv);
    __builtin_amdgcn_raw_buffer_store_b128(cat(x0, x1), rw, WS_X + o, 0, SC1);
    VMW(6); PIN(fhi); PIN(flo); PIN(Dh); PIN(e0);
    SB();
  }
  STAMP(3);
  const u64 tag = mktag(e0);
  const float Dv = bflo(Dh);
  SB();
  w1_issue<2>(w1, L_RING); w1_issue<3>(w1, L_RING + 16384);
  STAMP(4);
  VMW(4); bar();
  if (tid == 0) setflag(ws, WS_F1 + (tb64 * 8 + role) * 128, tag);
  STAMP(5);
  const int hh = l >> 5, tk = l & 31;
  const u32 gso = WS_G + (u32)(((2 * gi * 2 + (tk >> 4)) * 56 + 4 * gj + (w >> 1)) * 1024 + (2 * (w & 1) + hh) * 256 + i16 * 16);
  auto gofs = [](int g) AI { return (u32)((32 * (g >> 1) + (g & 1)) * 2 * 56 * 1024); };
  u32x4 W[28];
  const u32 ra0 = opq(w1.ra), ra1 = opq(w1.ra + L_RING), ra2 = opq(w1.ra + L_RING + 49152);
  w1_read<0>(lds, ra0 + L_FC, W); w1_read<1>(lds, ra0 + L_FC + 16384, W);
  w1_issue<7>(w1, L_RED); w1_issue<8>(w1, L_RED + 16384);
  bar();
  {
    const u32 a0 = __builtin_amdgcn_alignbit(flo.y, flo.x, 16), a1 = __builtin_amdgcn_alignbit(fhi.x, flo.y, 16), a2 = __builtin_amdgcn_alignbit(fhi.y, fhi.x, 16),
              a3 = __builtin_amdgcn_alignbit(fhi.z, fhi.y, 16), a4 = __builtin_amdgcn_alignbit(fhi.w, fhi.z, 16);
    char* fc = lds + L_FC + cch * FCH + (vp * 64 + l) * 16 + 32;
    *(u32x4*)(fc) = fhi;
    *(u32x4*)(fc + FCS) = u32x4{a1, a2, a3, a4};
    *(u32x4*)(fc + 2 * FCS) = u32x4{flo.y, fhi.x, fhi.y, fhi.z};
    *(u32x4*)(fc + 3 * FCS) = u32x4{a0, a1, a2, a3};
    if (tid >= 128 && tid < 144) *(u32x4*)(lds + L_FC + (tid & 7) * FCS + ((tid >> 3) & 1) * 16) = u32x4{0, 0, 0, 0};
    if (tid >= 192 && tid < 252) { const int z = tid - 192; *(u32x4*)(lds + L_U + (z / 30) * UCH + (z % 30) * 16) = u32x4{0, 0, 0, 0}; }
  }
  SB();
  w1_issue<4>(w1, L_RING + 32768); w1_issue<5>(w1, L_RING + 49152); w1_issue<6>(w1, L_RING + 65536);
  SB();

  STAMP(6);
  poll(ws, WS_F1 + (u32)(32 * w + 8 * (l & 3) + ((l & 4) ? 7 : (cp >> 5))) * 128, l < 8, tag);
  STAMP(7);
  u32x4 uu = ldg(dw, eo, WS_U), ex = ldg(dw, eo, WS_X);
  u32x4 est[4];
#pragma unroll
  for (int i = 0; i < 4; ++i) est[i] = ldg(dw, (u32)(r8 * 64 + p4 * 16), WS_ST + (u32)(w * 2048 + i * 512));
  bar();
  w1_read<7>(lds, ra0 + L_RED, W); w1_read<8>(lds, ra0 + L_RED + 16384, W);
  w1_read<2>(lds, ra1, W); w1_read<3>(lds, ra1 + 16384, W); w1_read<4>(lds, ra1 + 32768, W); w1_read<5>(lds, ra2, W); w1_read<6>(lds, ra2 + 16384, W);
  const u32 aA = opq(L_FC + cch * FCH + q4 * FCS + 64 - 16 * G + 8 * p4 - 8 + 128 * vp);
  const u32 aB = opq(L_U + cch * UCH + 32 * i16 + 16 * G + 448 - 128 * vp - 64);
  const u32 rwr = opq(L_RED + w * 4096 + G * 256 + ((i16 ^ ((G >> 1) << 3)) << 4));
  const u32 rr = opq(L_RED + ech * 4096 + (w & 3) * 1024 + (p4 & 1) * 512 + (((2 * r8 + (p4 >> 1)) ^ ((p4 & 1) << 3)) << 4));
  SB();
  vmw<5>(uu);
  *(u32x4*)(lds + L_U + ech * UCH + 480 + (8 * w + r8) * 64 + p4 * 16) = uu;
  bar();
  STAMP(8);
  auto rdA = [&](int v, int m) AI { const u32 o = 64 * v + 512 * m; return cat(trd(lds, aA + o + 8), trd(lds, aA + o)); };
  auto rdB = [&](int v, int m) AI { return *(const u32x4*)(lds + aB + 64 * (1 - v) + 512 * m); };
  float ex8[8], eq8[8], eu8[8];
  auto terms = [&]() AI {
#pragma unroll
    for (int i = 0; i < 4; ++i) {
      ex8[2 * i] = bflo(ex[i]); ex8[2 * i + 1] = bfhi(ex[i]);
      eu8[2 * i] = bflo(uu[i]) * Dv; eu8[2 * i + 1] = op(bfhi(uu[i]) * Dv);
      eq8[2 * i] = (ex8[2 * i] - __uint_as_float(est[i].x)) * __uint_as_float(est[i].y);
      eq8[2 * i + 1] = op(op(ex8[2 * i + 1] - __uint_as_float(est[i].z)) * __uint_as_float(est[i].w));
    }
#pragma unroll
    for (int i = 0; i < 8; ++i) { PIN(ex8[i]); PIN(eq8[i]); PIN(eu8[i]); }
  };
  auto epilogue = [&]() AI {
    f32x4 p[8];
#pragma unroll
    for (int n = 0; n < 4; ++n) { p[2 * n] = *(const f32x4*)(lds + rr + n * 8192); p[2 * n + 1] = *(const f32x4*)(lds + rr + n * 8192 + 256); }
    float h[8];
#pragma unroll
    for (int i = 0; i < 8; ++i) {
      const float y = op(op(eu8[i] + p[i >> 2][i & 3]) + p[2 + (i >> 2)][i & 3]) + op(p[4 + (i >> 2)][i & 3] + p[6 + (i >> 2)][i & 3]);
      h[i] = op(ex8[i] + eq8[i] * op(y));
    }
    __builtin_amdgcn_raw_buffer_store_b128(u32x4{pk(h[0], h[1]), pk(h[2], h[3]), pk(h[4], h[5]), pk(h[6], h[7])}, rw, WS_H + eo, 0, SC1);
  };
  u64 fl = tag;
  const u32 f2o = WS_F2 + (u32)(64 * (w & 3) + (l & 56) + ((l & 7) == 7 ? 0 : (l & 7))) * 128;
  {
    f32x4 acc[4];
    u32x4 A[2][4], B[2][4];
#pragma unroll
    for (int v = 0; v < 2; ++v) {
      A[v][0] = rdA(v, 0); SB();
#pragma unroll
      for (int m = 0; m < 4; ++m) { B[v][m] = rdB(v, m); SB(); }
#pragma unroll
      for (int m = 1; m < 4; ++m) { A[v][m] = rdA(v, m); SB(); }
      if (v == 0) {
        vmw<0>(ex); PIN(est[0]); PIN(est[1]); PIN(est[2]); PIN(est[3]);
        __builtin_amdgcn_raw_buffer_store_b128(u32x4{EMPTY, EMPTY, EMPTY, EMPTY}, rw, eo, WS_H, SC1);
        if (w < 4) terms();
        SB();
      }
    }
#pragma unroll
    for (int v = 0; v < 2; ++v)
#pragma unroll
      for (int m = 0; m < 4; ++m) {
#pragma unroll
        for (int c = m; c < 4; ++c) acc[c] = mfma16(A[v][m], B[v][c - m], (v == 0 && m == 0) ? f32x4{} : acc[c]);
        SB();
        if (v == 0 && m == 0) w1_g<9>(w1, L_RING);
        if (v == 0 && m == 2) w1_u<9>(w1, L_RING);
        if (v == 1 && m == 0) w1_g<10>(w1, L_RING + 16384);
        SB();
      }
#pragma unroll
    for (int c = 0; c < 4; ++c) *(f32x4*)(lds + rwr + c * 1024) = acc[c];
  }
  VMW(3); bar();
  if (tid == 256) setflag(ws, WS_F2 + (tb64 * 8 + role) * 128, tag);
  STAMP(9);
  if (w < 4) epilogue();
  SB();
  STAMP(10);
  {
    f32x4 acc[4];
    u32x4 A[2][8], B[2][8];
    auto rd = [&](int v, int m) AI {
      if (m == 0) {
        A[v][0] = rdA(v, 0);
#pragma unroll
        for (int d = 4; d < 8; ++d) B[v][d] = rdB(v, d);
      } else { A[v][m] = rdA(v, m); if (m <= 4) B[v][4 - m] = rdB(v, 4 - m); }
      SB();
    };
    rd(0, 0); rd(0, 1); rd(0, 2); rd(0, 3);
#pragma unroll
    for (int v = 0; v < 2; ++v) {
#pragma unroll
      for (int m = 0; m < 8; ++m) {
#pragma unroll
        for (int c = 4; c < 8; ++c) if (m <= c) acc[c - 4] = mfma16(A[v][m], B[v][c - m], (v == 0 && m == 0) ? f32x4{} : acc[c - 4]);
        SB();
        if (m < 4) rd(v, m + 4); else if (v == 0) rd(1, m - 4);
        if (v == 1 && m == 0 && w < 4) fl = flag_ld(dw, f2o);
        if (v == 0) {
          if (m == 0) w1_u<10>(w1, L_RING + 16384);
          if (m == 1) w1_g<11>(w1, L_RING + 32768);
          if (m == 2) w1_u<11>(w1, L_RING + 32768);
          if (m == 3) w1_g<12>(w1, L_RING + 49152);
          if (m == 4) w1_u<12>(w1, L_RING + 49152);
          if (m == 6) w1_g<13>(w1, L_RING + 65536);
          if (m == 7) w1_u<13>(w1, L_RING + 65536);
        }
        SB();
      }
    }
    if (w >= 4) terms();
    STAMP(11);
    VMWP(0, fl);
    if (!__all(fl == tag)) poll<2>(ws, f2o, true, tag);
    bar();
#pragma unroll
    for (int c = 0; c < 4; ++c) *(f32x4*)(lds + rwr + c * 1024) = acc[c];
  }
  const u32 lo16 = l * 16;
  if (w < 4) {
#pragma unroll
    for (int k = 0; k < 7; ++k) dma<SC1>(dw, lb + L_H0 + (7 * w + k) * 1024, lo16, WS_H + (u32)(2 * gi * TB + (7 * w + k) * 1024));
  }
  w1_read<9>(lds, ra1, W); w1_read<10>(lds, ra1 + 16384, W); w1_read<11>(lds, ra1 + 32768, W); w1_read<12>(lds, ra2, W); w1_read<13>(lds, ra2 + 16384, W);
  bar();
  STAMP(12);

  u64 csv;
  const u32 bta = (u32)((8 * (G >> 1) + q4) * 64 + (G & 1) * 32 + p4 * 8);
  auto hbase = [](int g) AI { return g == 0 ? L_H0 : g == 1 ? L_H1 : g == 2 ? L_H2 : L_H3; };
  f32x4 tm; float gu[8], gt[8]; u32 od[4];
  auto center = [&](f32x16& acc, float m) AI {
    const u32 d = __float_as_uint(m);
    acc = mfma32(u32x4{(u32)csv, (u32)(csv >> 32), 0, 0}, u32x4{d, d & 0xffffu, 0, 0}, acc);
  };
  auto geglu_step = [&](int g, f32x16& acc, int i) AI {
#pragma unroll
    for (int e = 0; e < 8; ++e) {
      const int s = i - (2 + (18 * e) / 7);
      if (s == 0) { gt[e] = acc[e] * acc[e]; gu[e] = op(acc[e] * acc[e + 8]); PIN(gt[e]); PIN(gu[e]); }
      if (s == 1) { gt[e] = tm.w * gt[e] + tm.z; PIN(gt[e]); }
      if (s == 2) { gt[e] = acc[e] * gt[e]; PIN(gt[e]); }
      if (s == 3) { gt[e] = __builtin_amdgcn_exp2f(gt[e]); PIN(gt[e]); }
      if (s == 4) { gt[e] = gt[e] * tm.y + tm.y; PIN(gt[e]); }
      if (s == 5) { gt[e] = __builtin_amdgcn_rcpf(gt[e]); PIN(gt[e]); }
      if (s == 6) { gu[e] = gu[e] * gt[e]; PIN(gu[e]); }
      if (s == 7 && (e & 1)) { od[e >> 1] = pk(gu[e - 1], gu[e]); PIN(od[e >> 1]); }
    }
    if (i == 28) {
      __builtin_amdgcn_raw_buffer_store_b128(u32x4{od[0], od[1], od[2], od[3]}, rw, gso, gofs(g), SC1);
    }
  };
  constexpr int PF = 4;
  u32x4 B[PF];
  u32 ba = opq(hbase(0) + bta);
  auto chain = [&](int g, f32x16& acc, auto side) AI {
    const u32 bn = opq(hbase(g < 3 ? g + 1 : g) + bta);
    sfor<28>([&](auto kc) AI {
      constexpr int k = decltype(kc)::value;
      acc = mfma32(W[k], B[k % PF], k == 0 ? f32x16{} : acc);
      SB();
      side(k);
      if (k + PF < 28) B[k % PF] = cat(trd(lds, ba + (k + PF) * 1024), trd(lds, ba + (k + PF) * 1024 + 256));
      else if (g < 3) B[k % PF] = cat(trd(lds, bn + (k + PF - 28) * 1024), trd(lds, bn + (k + PF - 28) * 1024 + 256));
      SB();
    });
    ba = bn;
  };
  auto flag3 = [&](int g) AI { setflag(ws, WS_F3 + (u32)(((gi * 4 + g) * 14) + gj) * 128, tag); };
  const int ri = 2 * xc + sc / 14, rq2 = (sc / 7) & 1, n2 = sc % 7;
  const u32x4 rd = srd(gwd, D_ * F_ * 2);
  const u32 wdv = (u32)(((224 * w + r8) * D_ + 64 * n2) * 2 + (((l & 7) ^ (((r8 >> 1) & 3) << 1)) << 4));
  const u32 blkR = w < 7 ? L_R + w * 4096 : L_R2;
  u32 wda[4], wdr[4];
#pragma unroll
  for (int nt = 0; nt < 4; ++nt) {
    const u32 a = (16 * (G >> 1) + 4 * (G & 1) + q4) * 128 + ((nt ^ (2 * (G & 1) + (q4 >> 1))) * 32) + p4 * 8;
    wda[nt] = opq(a + L_P + w * 4096); wdr[nt] = opq(a + blkR);
  }
  u32x4 Wd[7][4];
  auto wd_dma = [&](int k, int i) AI {
    if (i == 0) dma_at(lb + (k % 3 == 1 ? blkR : (k % 3 == 2 ? L_Q : L_P) + w * 4096));
    dma_i<0>(i, rd, wdv, 4 * k * (8 * D_ * 2) + i * (8 * D_ * 2 - 1024));
  };
  auto wd_rd = [&](int k, int nt) AI {
    const u32 a = k % 3 == 1 ? wdr[nt] : wda[nt] + (k % 3 == 2 ? L_Q - L_P : 0);
    Wd[k][nt] = cat(trd(lds, a), trd(lds, a + 1024));
  };
  auto wd_read = [&](int k) AI { wd_rd(k, 0); wd_rd(k, 1); wd_rd(k, 2); wd_rd(k, 3); };
  const int j0 = (14 * w) >> 3, jn = ((14 * w + 13) >> 3) - j0;
  const u32 f3o = WS_F3 + (u32)(ri * 56 + j0 + ((l & 3) < jn ? (l & 3) : jn)) * 128;

  f32x16 accA, accB;
  u32x4 tm0, tm1, tm2, tm3;
  const u32 s2o = WS_S2 + (u32)(2 * gi * 512 + tk * 16);
  const u32 hp = (u32)(w < 4 ? 4 * w : 4 + 3 * w) * 1024;
  auto htb = [&](int g) AI { return WS_H + (u32)((32 * (g >> 1) + 2 * gi + (g & 1)) * TB); };
  auto h_dma = [&](int g, int i) AI {
    if (i == 0) dma_at(lb + hbase(g) + hp);
    if (i < 3 || w < 4) dma_i<SC1>(i, dw, lo16, htb(g) + hp);
  };
  auto redo1 = [&](int g, f32x16& acc, u32 m) AI {
    if (__any(acc[0] != acc[0])) {
      REDO_COUNT();
      const u32 sc0 = L_SP + w * 1024;
      acc = f32x16{};
      const rsrc_t rg = mk(gwg, D_ * F_ * 2), ru = mk(gwu, D_ * F_ * 2);
#pragma unroll 1
      for (int k = 0; k < 28; ++k) {
        const u32 wo = (u32)(((16 * k + ((l >> 1) & 15)) * F_ + 128 * gj + 16 * w) * 2 + (l & 1) * 16);
        const u32x4 wg = __builtin_amdgcn_raw_buffer_load_b128(rg, wo, 0, 0), wu = __builtin_amdgcn_raw_buffer_load_b128(ru, wo, 0, 0);
        *(u32x4*)(lds + sc0 + lo16) = l < 32 ? wg : wu;
        const u32 wa = sc0 + (G & 1) * 512 + (8 * (G >> 1) + q4) * 32 + p4 * 8;
        const u32x4 a = cat(trd(lds, wa), trd(lds, wa + 128));
        u32x4 v;
#pragma unroll 1
        for (int it = 0; it < 400; ++it) { v = ldg(dw, lo16, htb(g) + k * 1024); vmw<0>(v); if (!__any(max4(v) == EMPTY)) break; }
        *(u32x4*)(lds + sc0 + lo16) = v;
        acc = mfma32(a, cat(trd(lds, sc0 + bta), trd(lds, sc0 + bta + 256)), acc);
      }
      center(acc, __uint_as_float(m));
    }
  };
  const u32 cso = opq(l < 32 ? WS_CS + (u32)(((gj * 8 + w) * 32 + l) * 8) : OOB);
  auto tm_fix = [&](u32x4& t, u32 so) AI {
#pragma unroll 1
    for (int it = 0; it < 4000 && __any(max4(t) == EMPTY); ++it) { t = ldg(dw, s2o, so); vmw<0>(t); }
  };
  SB();
  if (w >= 4) {
    epilogue();
  } else {
    const u32 ha = opq(L_H0 + 7 * w * 1024 + lo16);
#pragma unroll 1
    for (int it = 0; it < 4000; ++it) {
      VMW(0);
      u32 m = 0;
#pragma unroll
      for (int k = 0; k < 7; ++k) { const u32x4 v = *(const u32x4*)(lds + ha + k * 1024); m = max(m, max4(v)); }
      if (!__any(m == EMPTY)) break;
#pragma unroll
      for (int k = 0; k < 7; ++k) dma<SC1>(dw, lb + L_H0 + (7 * w + k) * 1024, lo16, WS_H + (u32)(2 * gi * TB + (7 * w + k) * 1024));
    }
    VMW(0);
    STAMP(13);
  }
  __builtin_amdgcn_s_setprio(1);
  bar();
  STAMP(14);
#pragma unroll
  for (int k = 0; k < PF; ++k) B[k] = cat(trd(lds, ba + k * 1024), trd(lds, ba + k * 1024 + 256));
  SB();
  chain(0, accA, [&](int k) AI {
    if (k <= 6 && !(k & 1)) h_dma(1, k >> 1);
    if (k == 8) csv = flag_ld(dw, cso);
    if (k == 9) tm0 = ldg(dw, s2o, 0);
    if (k == 12 || k == 16) __builtin_amdgcn_raw_buffer_store_b128(u32x4{EMPTY, EMPTY, EMPTY, EMPTY}, rw, gso, gofs(k == 12 ? 2 : 3), SC1);
    if (k == 22) { VMW(4); RBAR(); if (tid == 0 && b == 0) setflag(ws, WS_E, tag); }
    if (k == 25) {
      vmw<2>(tm0); PIN(csv);
#pragma unroll 1
      for (int it = 0; it < 4000 && __any(max((u32)csv, (u32)(csv >> 32)) == EMPTY); ++it) { csv = flag_ld(dw, cso); VMWP(0, csv); }
    }
    if (k == 26) tm_fix(tm0, 0);
  });
  center(accA, __uint_as_float(tm0.x));
  tm = __builtin_bit_cast(f32x4, tm0);
  SB();
  STAMP(16);
  chain(1, accB, [&](int k) AI {
    if (k == 1) RBAR();
    geglu_step(0, accA, k);
    if (k >= 1 && k <= 7 && (k & 1)) h_dma(2, k >> 1);
    if (k == 8) tm1 = ldg(dw, s2o, 512);
    if (k >= 10 && k <= 16 && !(k & 1)) wd_dma(0, (k - 10) >> 1);
    if (k >= 18 && k <= 27 && k % 3 == 0) h_dma(3, (k - 18) / 3);
    if (k == 23) { vmw<6>(tm1); RBAR(); }
    if (k == 25) tm_fix(tm1, 512);
  });
  center(accB, __uint_as_float(tm1.x));
  geglu_step(0, accA, 28);
  tm = __builtin_bit_cast(f32x4, tm1);
  SB();
  STAMP(18);
  chain(2, accA, [&](int k) AI {
    if (k == 1) RBAR();
    if (k == 2) redo1(1, accB, tm1.x);
    geglu_step(1, accB, k);
    if (k <= 6 && !(k & 1)) wd_dma(1, k >> 1);
    if (k == 5) VMW(7);
    if (k >= 5 && k <= 11 && (k & 1)) wd_rd(0, (k - 5) >> 1);
    if (k == 8) { tm2 = ldg(dw, s2o, 32 * 512); tm3 = ldg(dw, s2o, 33 * 512); }
    if (k >= 10 && k <= 24 && !(k & 1)) wd_dma(2 + ((k - 10) >> 3), ((k - 10) >> 1) & 3);
    if (k == 23) { vmw<7>(tm2); PIN(tm3); RBAR(); if (tid == 0) flag3(0); }
    if (k == 25) tm_fix(tm2, 32 * 512);
    if (k == 26) tm_fix(tm3, 33 * 512);
  });
  center(accA, __uint_as_float(tm2.x));
  geglu_step(1, accB, 28);
  tm = __builtin_bit_cast(f32x4, tm2);
  SB();
  STAMP(20);
  chain(3, accB, [&](int k) AI {
    if (k == 2) redo1(2, accA, tm2.x);
    geglu_step(2, accA, k);
    if (k == 12) fl = flag_ld(dw, f3o);
    if (k == 13) { VMW(1); RBAR(); if (tid == 0) flag3(1); }
    if (k >= 13 && k < 25) wd_rd(1 + (k - 13) / 4, (k - 13) & 3);
    if (k >= 20 && k < 28 && !(k & 1)) wd_dma(4, (k - 20) >> 1);
  });
  center(accB, __uint_as_float(tm3.x));
  geglu_step(2, accA, 28);
  tm = __builtin_bit_cast(f32x4, tm3);
  redo1(3, accB, tm3.x);
  STAMP(21);
#pragma unroll
  for (int e = 0; e < 8; ++e) {
    const float a = accB[e], u = accB[e + 8];
    gu[e] = (a * u) * __builtin_amdgcn_rcpf(__builtin_amdgcn_exp2f(a * (tm.w * (a * a) + tm.z)) * tm.y + tm.y);
  }
#pragma unroll
  for (int e = 0; e < 4; ++e) od[e] = pk(gu[2 * e], gu[2 * e + 1]);
  geglu_step(3, accB, 28);
  __builtin_amdgcn_s_setprio(0);
  SB();
  STAMP(22);
  const u32 gb = WS_G + (u32)((2 * ri * 2 + rq2) * 56 + 7 * w) * 1024;
  u32x4 Hf[3][7];
  auto h_load = [&](int g, int k0, int k1) AI {
#pragma unroll
    for (int k = k0; k < k1; ++k) Hf[g % 3][k] = ldg(dw, lo16, gb + gofs(g) + k * 1024);
  };
  const rsrc_t ro = mk(gout, S_ * D_ * 2);
  const u32 pw = (u32)(i16 * 256), tf = 4 * (w & 3) + G;
  f32x4 acc[4];
  u32x2 res[4];
  auto step = [&](auto gc, auto kc, auto nc) AI {
    constexpr int g = decltype(gc)::value, k = decltype(kc)::value;
    vmw<decltype(nc)::value>(Hf[g % 3][k]);
#pragma unroll
    for (int nt = 0; nt < 4; ++nt) acc[nt] = mfma16(Wd[k][nt], Hf[g % 3][k], k == 0 ? f32x4{} : acc[nt]);
    SB();
  };
  auto put = [&](int g) AI {
    const u32 rb = opq(((g & 1) ? L_Q : L_P) + w * 4096 + pw);
#pragma unroll
    for (int nt = 0; nt < 4; ++nt) *(f32x4*)(lds + rb + (((4 * nt + G) ^ (i16 & 7)) << 4)) = acc[nt];
    *(f32x4*)(lds + L_RES + (g & 1) * 4096 + pw + (((2 * w + (G & 1)) ^ (i16 & 7)) << 4)) = f32x4{bflo(res[g].x), bfhi(res[g].x), bflo(res[g].y), bfhi(res[g].y)};
  };
  auto fin = [&](int g) AI {
    if ((w >> 2) == (g & 1)) {
      const u32 a0 = tf * 256 + ((i16 ^ (tf & 7)) << 4), a = opq(((g & 1) ? L_Q : L_P) + a0);
      f32x4 sum = *(const f32x4*)(lds + L_RES + (g & 1) * 4096 + a0);
#pragma unroll
      for (int n = 0; n < 8; ++n) sum += *(const f32x4*)(lds + a + n * 4096);
      const int tok = 32 * (32 * (g >> 1) + (g & 1) + 2 * ri) + 16 * rq2 + tf;
      __builtin_amdgcn_raw_buffer_store_b64(u32x2{pk(sum.x, sum.y), pk(sum.z, sum.w)}, ro, (u32)((tok * D_ + 64 * n2 + 4 * i16) * 2), 0, 0);
    }
  };
  auto redo = [&](auto gc) AI {
    constexpr int g = decltype(gc)::value;
    if (__any(acc[0].x != acc[0].x)) {
#pragma unroll 1
      for (int it = 0; it < 4000; ++it) {
        h_load(g, 0, 7);
        step(gc, IC<0>{}, IC<6>{}); step(gc, IC<1>{}, IC<5>{}); step(gc, IC<2>{}, IC<4>{}); step(gc, IC<3>{}, IC<3>{});
        step(gc, IC<4>{}, IC<2>{}); step(gc, IC<5>{}, IC<1>{}); step(gc, IC<6>{}, IC<0>{});
        u32 m = 0;
#pragma unroll
        for (int k = 0; k < 7; ++k) m = max(m, max4(Hf[g % 3][k]));
        REDO_COUNT();
        if (!__any(m == EMPTY)) break;
      }
    }
  };
  VMWP(6, fl);
  if (!__all(fl == tag)) poll(ws, f3o, true, tag);
  LG0();
  wd_dma(5, 0); wd_dma(5, 1); wd_dma(5, 2); wd_dma(5, 3);
  h_load(0, 0, 3);
  fl = flag_ld(dw, f3o + 14 * 128);
  h_load(0, 3, 7);
  const u32 hro = WS_H + (u32)((32 * (G >> 1) + (G & 1) + 2 * ri) * TB + (64 * n2 + 8 * w + (i16 >> 1)) * 64 + rq2 * 32 + (i16 & 1) * 16);
  u32x4 hr = ldg(dw, hro, 0);
  wd_dma(6, 0); wd_dma(6, 1); wd_dma(6, 2); wd_dma(6, 3);
  VMWP(9, fl);
  if (!__all(fl == tag)) poll(ws, f3o + 14 * 128, true, tag);
  wd_read(4); wd_read(5);
  SB();
  STAMP(23);
  h_load(1, 0, 1); step(IC<0>{}, IC<0>{}, IC<10>{});
  h_load(1, 1, 2); step(IC<0>{}, IC<1>{}, IC<11>{});
  h_load(1, 2, 3); step(IC<0>{}, IC<2>{}, IC<12>{});
  h_load(1, 3, 4); step(IC<0>{}, IC<3>{}, IC<12>{});
  h_load(1, 4, 5); step(IC<0>{}, IC<4>{}, IC<12>{});
  h_load(1, 5, 6); step(IC<0>{}, IC<5>{}, IC<12>{});
  h_load(1, 6, 7);
  vmw<7>(hr);
#pragma unroll 1
  for (int it = 0; it < 4000 && __any(max4(hr) == EMPTY); ++it) { hr = ldg(dw, hro, 0); vmw<0>(hr); }
  wd_read(6);
  *(u32x4*)(lds + blkR + l * 16) = hr;
#pragma unroll
  for (int g = 0; g < 4; ++g) res[g] = trd(lds, blkR + g * 256 + (4 * (G & 1) + q4) * 32 + p4 * 8);
  h_load(2, 0, 2);
  step(IC<0>{}, IC<6>{}, IC<9>{});
  put(0);
  STAMP(24);
  h_load(2, 2, 3); step(IC<1>{}, IC<0>{}, IC<9>{});
  h_load(2, 3, 5); step(IC<1>{}, IC<1>{}, IC<10>{});
  h_load(2, 5, 7); step(IC<1>{}, IC<2>{}, IC<11>{});
  h_load(3, 0, 2); step(IC<1>{}, IC<3>{}, IC<12>{});
  h_load(3, 2, 4); step(IC<1>{}, IC<4>{}, IC<13>{});
  h_load(3, 4, 6); step(IC<1>{}, IC<5>{}, IC<14>{});
  h_load(3, 6, 7); step(IC<1>{}, IC<6>{}, IC<14>{});
  put(1);
  STAMP(25);
  bar();
  fin(0); fin(1);
  SB();
  step(IC<2>{}, IC<0>{}, IC<13>{}); step(IC<2>{}, IC<1>{}, IC<12>{}); step(IC<2>{}, IC<2>{}, IC<11>{}); step(IC<2>{}, IC<3>{}, IC<10>{});
  step(IC<2>{}, IC<4>{}, IC<9>{}); step(IC<2>{}, IC<5>{}, IC<8>{}); step(IC<2>{}, IC<6>{}, IC<7>{});
  redo(IC<2>{});
  bar();
  put(2);
  SB();
  STAMP(26);
  step(IC<3>{}, IC<0>{}, IC<6>{}); step(IC<3>{}, IC<1>{}, IC<5>{}); step(IC<3>{}, IC<2>{}, IC<4>{}); step(IC<3>{}, IC<3>{}, IC<3>{});
  step(IC<3>{}, IC<4>{}, IC<2>{}); step(IC<3>{}, IC<5>{}, IC<1>{}); step(IC<3>{}, IC<6>{}, IC<0>{});
  redo(IC<3>{});
  put(3);
  bar();
  STAMP(27);
  fin(2); fin(3);
  STAMP(28);
  STAMP_OUT();
}

}

extern "C" void kernel_launch(void* const* d_in, const int* in_sizes, int n_in, void* d_out, int out_size, void* d_ws, size_t ws_size,
                              hipStream_t stream) {
  h3_ffn<<<256, 512, 0, stream>>>((const char*)d_in[0], (const char*)d_in[1], (const char*)d_in[2], (const char*)d_in[3], (const char*)d_in[4],
                                  (const char*)d_in[5], (const char*)d_in[7], (const char*)d_in[8], (const char*)d_in[9], (char*)d_out, (char*)d_ws);
}
```

```cpp
#include <hip/hip_runtime.h>
#include <stdint.h>

namespace {

#define LAS __attribute__((address_space(3)))
#define GAS __attribute__((address_space(1)))
#define DI __device__ __forceinline__
#define AI __attribute__((always_inline))
#define RARE(x) __builtin_expect(!!(x), 0)
typedef uint32_t u32;
typedef unsigned long long u64;
typedef u32 u32x2 __attribute__((ext_vector_type(2)));
typedef u32 u32x4 __attribute__((ext_vector_type(4)));
typedef short s16x4 __attribute__((ext_vector_type(4)));
typedef short s16x8 __attribute__((ext_vector_type(8)));
typedef float f32x4 __attribute__((ext_vector_type(4)));
typedef float f32x16 __attribute__((ext_vector_type(16)));
typedef __bf16 bf2 __attribute__((ext_vector_type(2)));
typedef __amdgpu_buffer_rsrc_t rsrc_t;
#define RLX_AGENT __ATOMIC_RELAXED, __HIP_MEMORY_SCOPE_AGENT

constexpr int S_ = 2048, D_ = 448, F_ = 1792;
constexpr u32 TB = 448 * 64;
constexpr u32 WS_E  = 0;
constexpr u32 WS_F1 = 128;
constexpr u32 WS_F3 = WS_F1 + 256 * 128;
constexpr u32 WS_F2 = WS_F3 + 896 * 128;
constexpr u32 WS_S2 = 256 * 1024 + 16384;
constexpr u32 WS_ST = 256 * 1024;
constexpr u32 WS_U  = WS_S2 + 32768;
constexpr u32 WS_X  = WS_U + 64 * TB;
constexpr u32 WS_H  = WS_X + 64 * TB;
constexpr u32 WS_G  = WS_H + 64 * TB;
constexpr u32 WS_END = WS_G + 64 * 112 * 1024;
constexpr u32 WS_DBG = 16u << 20;
constexpr int SC1 = 16;
#define HELP_DELAY 8
#define TM0K 8
#define ROT1 0
#define PRETOUCH 0
constexpr u32 OOB = 0xFFFFFF00u;
constexpr u32 ONES = 0x3f803f80u;
constexpr u32 EMPTY = 0xffffffffu;

constexpr u32 L_K = 0, L_V = 9216, L_X = 17408, L_SK = 25600;
constexpr u32 L_RED = 0, L_FC = 32768, FCS = 4160, FCH = 4 * FCS;
constexpr u32 L_U = L_FC + 2 * FCH, UCH = 4608, L_RING = L_U + 2 * UCH;
constexpr u32 L_H0 = 32768, L_H1 = L_H0 + TB, L_H2 = 0, L_H3 = L_H1 + TB + 32768, L_SP = 155648;
constexpr u32 L_P = TB, L_Q = L_P + 32768, L_R = L_Q + 32768, L_R2 = L_H3 + TB;
constexpr u32 L_RES = 0, L_T = L_H3;
constexpr u32 LDS_BYTES = 163840;

DI float bflo(u32 x) { return __uint_as_float(x << 16); }
DI float bfhi(u32 x) { return __uint_as_float(x & 0xffff0000u); }
DI u32 pk(float lo, float hi) { bf2 p; p.x = (__bf16)lo; p.y = (__bf16)hi; return __builtin_bit_cast(u32, p); }
DI rsrc_t mk(const void* p, u32 bytes) { return __builtin_amdgcn_make_buffer_rsrc((void*)p, 0, bytes, 0x00020000); }
DI u32x2 trd(const char* lds, u32 a) { return __builtin_bit_cast(u32x2, __builtin_amdgcn_ds_read_tr16_b64_v4i16((LAS s16x4*)(lds + a))); }
DI u32x4 cat(u32x2 a, u32x2 b) { return u32x4{a.x, a.y, b.x, b.y}; }
DI u32x4 srd(const void* p, u32 bytes) { const u64 a = (u64)p; return u32x4{(u32)a, (u32)(a >> 32) & 0xffffu, bytes, 0x00020000u}; }
template <int AUX = 0> DI void dma(u32x4 r, u32 ldsoff, u32 voff, u32 soff) {
  if (AUX) asm volatile("s_mov_b32 m0, %0\n\ts_nop 0\n\tbuffer_load_dwordx4 %1, %2, %3 offen sc1 lds" :: "s"(ldsoff), "v"(voff), "s"(r), "s"(soff) : "memory");
  else asm volatile("s_mov_b32 m0, %0\n\ts_nop 0\n\tbuffer_load_dwordx4 %1, %2, %3 offen lds" :: "s"(ldsoff), "v"(voff), "s"(r), "s"(soff) : "memory");
}
DI void dma_at(u32 ldsoff) { asm volatile("s_mov_b32 m0, %0\n\ts_nop 0" :: "s"(ldsoff) : "memory"); }
template <int OFF, int AUX> DI void dma_go(u32x4 r, u32 voff, u32 soff) {
  if (AUX) asm volatile("buffer_load_dwordx4 %0, %1, %2 offen offset:%3 sc1 lds" :: "v"(voff), "s"(r), "s"(soff), "n"(OFF) : "memory");
  else asm volatile("buffer_load_dwordx4 %0, %1, %2 offen offset:%3 lds" :: "v"(voff), "s"(r), "s"(soff), "n"(OFF) : "memory");
}
template <int AUX> DI void dma_i(int i, u32x4 r, u32 voff, u32 soff) {
  if (i == 0) dma_go<0, AUX>(r, voff, soff); else if (i == 1) dma_go<1024, AUX>(r, voff, soff); else if (i == 2) dma_go<2048, AUX>(r, voff, soff); else dma_go<3072, AUX>(r, voff, soff);
}
DI float fnma(float a, float b, float c) { float r; asm("v_fma_f32 %0, -%1, %2, %3" : "=v"(r) : "v"(a), "v"(b), "v"(c)); return r; }
DI float op(float x) { asm("" : "+v"(x)); return x; }
DI u32 opq(u32 x) { asm volatile("" : "+v"(x)); return x; }
#define PIN(x) asm volatile("" : "+v"(x))
DI f32x4 mfma16(u32x4 a, u32x4 b, f32x4 c) { return __builtin_amdgcn_mfma_f32_16x16x32_bf16(__builtin_bit_cast(s16x8, a), __builtin_bit_cast(s16x8, b), c, 0, 0, 0); }
DI f32x16 mfma32(u32x4 a, u32x4 b, f32x16 c) { return __builtin_amdgcn_mfma_f32_32x32x16_bf16(__builtin_bit_cast(s16x8, a), __builtin_bit_cast(s16x8, b), c, 0, 0, 0); }
DI f32x4 mfma4(u32x2 a, u32x2 b, f32x4 c) { return __builtin_amdgcn_mfma_f32_4x4x4bf16_1k(__builtin_bit_cast(s16x4, a), __builtin_bit_cast(s16x4, b), c, 0, 0, 0); }
DI float dot2(u32 a, u32 b, float c) { return __builtin_amdgcn_fdot2_f32_bf16(__builtin_bit_cast(bf2, a), __builtin_bit_cast(bf2, b), c, false); }
#define VMW(n) asm volatile("s_waitcnt vmcnt(%0)" :: "n"(n) : "memory")
#define LG0() asm volatile("s_waitcnt lgkmcnt(0)" ::: "memory")
#define SB() __builtin_amdgcn_sched_barrier(0)
DI void bar() { asm volatile("s_waitcnt lgkmcnt(0)\n\ts_barrier" ::: "memory"); }
#define RBAR() asm volatile("s_barrier" ::: "memory")
DI float wsum(float v) {
  v += __builtin_amdgcn_update_dpp(0.f, v, 0x111, 0xf, 0xf, true);
  v += __builtin_amdgcn_update_dpp(0.f, v, 0x112, 0xf, 0xf, true);
  v += __builtin_amdgcn_update_dpp(0.f, v, 0x114, 0xf, 0xf, true);
  v += __builtin_amdgcn_update_dpp(0.f, v, 0x118, 0xf, 0xf, true);
  v += __builtin_amdgcn_update_dpp(0.f, v, 0x142, 0xa, 0xf, true);
  v += __builtin_amdgcn_update_dpp(0.f, v, 0x143, 0xc, 0xf, true);
  return __uint_as_float(__builtin_amdgcn_readlane(__float_as_uint(v), 63));
}
DI float hsum(float v) {
  const auto r = __builtin_amdgcn_permlane32_swap(__float_as_uint(v), __float_as_uint(v), false, false);
  return __uint_as_float(r[0]) + __uint_as_float(r[1]);
}
template <int SLEEP = 1> DI void poll(char* ws, u32 off, bool act, u64 tag) {
  bool ok = !act;
#pragma unroll 1
  for (int it = 0; it < 20000; ++it) {
    u64 x = tag;
    if (!ok) x = __hip_atomic_load((GAS u64*)(ws + off), RLX_AGENT);
    ok = (x == tag);
    if (__all(ok)) break;
    __builtin_amdgcn_s_sleep(SLEEP);
  }
  asm volatile("" ::: "memory");
}
DI u32 max4(u32x4 v) { return max(max(v.x, v.y), max(v.z, v.w)); }
DI u64 flag_ld(u32x4 r, u32 voff) { u64 v; asm volatile("buffer_load_dwordx2 %0, %1, %2, 0 offen sc1" : "=v"(v) : "v"(voff), "s"(r) : "memory"); return v; }
DI u32x4 ldg(u32x4 r, u32 voff, u32 soff) { u32x4 v; asm volatile("buffer_load_dwordx4 %0, %1, %2, %3 offen sc1" : "=v"(v) : "v"(voff), "s"(r), "s"(soff) : "memory"); return v; }
template <int N> DI void vmw(u32x4& x) { asm volatile("s_waitcnt vmcnt(%1)" : "+v"(x) : "n"(N) : "memory"); }
#define VMWP(n, x) asm volatile("s_waitcnt vmcnt(" #n ")" : "+v"(x) :: "memory")
DI void setflag(char* ws, u32 off, u64 tag) { __hip_atomic_store((GAS u64*)(ws + off), tag, RLX_AGENT); }

#define STAMP(i)
#define STAMPC(i)
#define REDO_COUNT()
#define FIX_COUNT()
#define STAMP_OUT()

DI float xsum(float a, float c) {
  const auto r = __builtin_amdgcn_permlane32_swap(__float_as_uint(a), __float_as_uint(c), false, false);
  return __uint_as_float(r[0]) + __uint_as_float(r[1]);
}
DI f32x4 ln2_terms(float t) {
  const auto r = __builtin_amdgcn_permlane32_swap(__float_as_uint(t), __float_as_uint(t), false, false);
  const float mean = __uint_as_float(r[0]) * (1.f / D_);
  const float V = fmaxf(__uint_as_float(r[1]) * (1.f / D_) - mean * mean, 0.f) + 1e-5f;
  const float rs = __builtin_amdgcn_rsqf(V);
  const float K1 = -2.f * 0.7978845608028654f * 1.4426950408889634f * rs;
  const float lo = -mean - bflo(pk(-mean, 0.f));
  return f32x4{__uint_as_float(pk(-mean, lo)), V, K1, (K1 * rs) * (0.044715f * rs)};
}
template <int N> struct IC { static constexpr int value = N; };
template <int N, int I = 0, class F> DI void sfor(F&& f) { if constexpr (I < N) { f(IC<I>{}); sfor<N, I + 1>(f); } }
struct W1 { u32x4 rg, ru; u32 vg, vu, ra, lb, rot; };
DI u32 wrap(u32 x, u32 n) { return x >= n ? x - n : x; }
template <int T> DI void w1_g(const W1& c, u32 base) { dma(c.rg, c.lb + base, c.vg, wrap(T + c.rot, 14) * (32 * F_ * 2)); }
template <int T> DI void w1_u(const W1& c, u32 base) { dma(c.ru, c.lb + base + 8192, c.vu, wrap(T + c.rot, 14) * (32 * F_ * 2)); }
template <int T> DI void w1_issue(const W1& c, u32 base) { w1_g<T>(c, base); w1_u<T>(c, base); }
template <int T> DI void w1_read(const char* lds, u32 a, u32x4 (&W)[28]) {
#pragma unroll
  for (int kk = 0; kk < 2; ++kk) W[2 * T + kk] = cat(trd(lds, a + kk * 4096), trd(lds, a + kk * 4096 + 1024));
}
DI float x8sum(float v) {
  v += __builtin_amdgcn_update_dpp(0.f, v, 0xb1, 0xf, 0xf, true);
  v += __builtin_amdgcn_update_dpp(0.f, v, 0x4e, 0xf, 0xf, true);
  v += __builtin_amdgcn_update_dpp(0.f, v, 0x141, 0xf, 0xf, true);
  return v;
}

__global__ __launch_bounds__(512, 2) void h3_ffn(const char* __restrict__ gq, const char* __restrict__ gk, const char* __restrict__ gv,
                                                 const char* __restrict__ gsk, const char* __restrict__ gf, const char* __restrict__ gD,
                                                 const char* __restrict__ gwg, const char* __restrict__ gwu, const char* __restrict__ gwd,
                                                 char* __restrict__ gout, char* ws) {
  __shared__ __attribute__((aligned(16))) char lds[LDS_BYTES];
  const int tid = threadIdx.x, l = tid & 63, w = __builtin_amdgcn_readfirstlane(tid >> 6);
  const int b = blockIdx.x, xc = b & 7, sl = b >> 3;
  const int tb64 = 4 * xc + (sl >> 3), role = sl & 7;
  const int sc = (sl >> 3) * 7 + role;
  asm volatile("" :: "s"(gq), "s"(gk), "s"(gv), "s"(gsk), "s"(gf), "s"(gD), "s"(gwg), "s"(gwu), "s"(gwd), "s"(gout), "s"(ws));
  STAMP(0);
  const rsrc_t rw = mk(ws, WS_END);
  const int G = l >> 4, i16 = l & 15, q4 = i16 >> 2, p4 = l & 3, r8 = l >> 3;
  auto mktag = [&](u64 e0) AI { return ((u64)__builtin_amdgcn_readfirstlane((u32)e0) | ((u64)__builtin_amdgcn_readfirstlane((u32)(e0 >> 32)) << 32)) * 6364136223846793005ull + 1442695040888963407ull; };
  if (RARE(role == 7)) {
    const rsrc_t rq = mk(gq, S_ * D_ * 2);
    const int row = 64 * tb64 + 8 * w + r8;
    u32x4 xs[7];
#pragma unroll
    for (int i = 0; i < 7; ++i) xs[i] = __builtin_amdgcn_raw_buffer_load_b128(rq, (u32)(row * 896 + (l & 7) * 16 + i * 128), 0, 0);
    const u64 e0 = __hip_atomic_load((GAS u64*)(ws + WS_E), RLX_AGENT);
    __builtin_amdgcn_raw_buffer_store_b128(u32x4{EMPTY, EMPTY, EMPTY, EMPTY}, rw, tid < 64 ? WS_S2 + (u32)((tb64 + (tid & 32)) * 512 + (tid & 31) * 16) : OOB, 0, SC1);
    SB();
    const u64 tag = mktag(e0);
    float s[4] = {};
#pragma unroll
    for (int i = 0; i < 7; ++i)
#pragma unroll
      for (int e = 0; e < 4; ++e) s[e] += bflo(xs[i][e]) + bfhi(xs[i][e]);
    const float mean = x8sum((s[0] + s[1]) + (s[2] + s[3])) * (1.f / D_);
    float v[4] = {};
#pragma unroll
    for (int i = 0; i < 7; ++i)
#pragma unroll
      for (int e = 0; e < 4; ++e) { const float d0 = bflo(xs[i][e]) - mean, d1 = bfhi(xs[i][e]) - mean; v[e] += d0 * d0; v[e] += d1 * d1; }
    const float rstd = __builtin_amdgcn_rsqf(x8sum((v[0] + v[1]) + (v[2] + v[3])) * (1.f / D_) + 1e-5f);
    const u32 so = WS_ST + (row >> 8) * 2048 + ((row >> 1) & 3) * 512 + ((row >> 5) & 7) * 64 + ((row >> 3) & 3) * 16 + (row & 1) * 8;
    __builtin_amdgcn_raw_buffer_store_b64(u32x2{__float_as_uint(mean), __float_as_uint(rstd)}, rw, (l & 7) == 0 ? so : OOB, 0, SC1);
    VMW(0); bar();
    if (tid == 0) setflag(ws, WS_F1 + (tb64 * 8 + 7) * 128, tag);
#pragma unroll
    for (int i = 0; i < 7; ++i) {
      const u32 o = (u32)(i * 8192 + tid * 16);
      __builtin_amdgcn_raw_buffer_store_b128(u32x4{EMPTY, EMPTY, EMPTY, EMPTY}, rw, o < TB ? o : o + 31 * TB, WS_H + (u32)tb64 * TB, SC1);
    }
    VMW(0); bar();
    if (tid == 0) setflag(ws, WS_F2 + tb64 * 128, tag);
    STAMP(1);
    {
      const rsrc_t rdw = mk(gwd, D_ * F_ * 2);
      const u32 t = __builtin_amdgcn_raw_buffer_load_b32(rdw, tid < 392 ? (u32)((tb64 * 392 + tid) * 128) : OOB, 0, 0);
      asm volatile("" :: "v"(t));
    }
    STAMP(2);
    const u32x4 dw = srd(ws, WS_END);
    const u32 lb = (u32)(size_t)(LAS char*)lds;
    __builtin_amdgcn_s_sleep(HELP_DELAY);
#pragma unroll 1
    for (int half = 0; half < 2; ++half) {
      const int tb = 32 * half + tb64;
#pragma unroll 1
      for (int it = 0; it < 4000; ++it) {
#pragma unroll
        for (int k = 0; k < 4; ++k) if (k < 3 || l < 32) dma<SC1>(dw, lb + 32768 + w * 3584 + k * 1024, l * 16, WS_H + (u32)(tb * TB + w * 3584 + k * 1024));
        VMW(0);
        u32 m = 0;
#pragma unroll
        for (int k = 0; k < 4; ++k) m = max(m, max4(*(const u32x4*)(lds + 32768 + w * 3584 + (k < 3 ? k * 1024 + l * 16 : 3072 + (l & 31) * 16))));
        if (!__any(m == EMPTY)) break;
        __builtin_amdgcn_s_sleep(3);
      }
      if (half == 0) { STAMP(4); } else { STAMP(6); }
      float s1 = 0.f, s2 = 0.f;
#pragma unroll
      for (int i = 0; i < 7; ++i) {
        const u32x2 t = trd(lds, 32768 + (u32)((56 * w + 4 * (G >> 1) + q4) * 64 + (G & 1) * 32 + p4 * 8) + i * 512);
        s1 = dot2(t.x, ONES, s1); s2 = dot2(t.x, t.x, s2); s1 = dot2(t.y, ONES, s1); s2 = dot2(t.y, t.y, s2);
      }
      *(float*)(lds + 20480 + half * 2048 + l * 32 + w * 4) = xsum(s1, s2);
      bar();
      if (w == 0) {
        const f32x4 a = *(const f32x4*)(lds + 20480 + half * 2048 + l * 32), c = *(const f32x4*)(lds + 20480 + half * 2048 + l * 32 + 16);
        __builtin_amdgcn_raw_buffer_store_b128(__builtin_bit_cast(u32x4, ln2_terms(((a.x + a.y) + (a.z + a.w)) + ((c.x + c.y) + (c.z + c.w)))), rw, l < 32 ? WS_S2 + (u32)(tb * 512 + l * 16) : OOB, 0, SC1);
      }
      if (half == 0) { STAMP(5); }
    }
    STAMP(3);
    STAMP_OUT();
    return;
  }
  const int cp = xc * 28 + sc, cch = w & 1, vp = w >> 1;
  const int ech = (l >> 2) & 1;
  const u32 eo = (u32)((8 * w + r8) * TB + (2 * cp + ech) * 64 + p4 * 16);
  u32x4 fhi; u32x2 flo; u32 Dh; u64 e0;
  const u32 lb = (u32)(size_t)(LAS char*)lds;
  const u32x4 dw = srd(ws, WS_END);
  {
    const u32x4 rk = srd(gk, S_ * D_ * 2), rv = srd(gv, S_ * D_ * 2);
    const int c8 = l & 7, s0 = 64 * tb64;
    const int swv = ((r8 >> 1) & 1) | ((w & 1) << 1), swk = swv ^ 2;
    const u32 g0 = (u32)((s0 + 8 * w + r8) * 896 + role * 128);
    if (w == 0) dma(rk, lb + L_K, g0 - 8 * 896 + ((c8 ^ (swv << 1)) << 4), 0);
    if (w == 1) dma(srd(gsk, D_ * 8), lb + L_SK, (u32)(512 * role + 16 * l), 0);
    dma(rk, lb + L_K + 1024 + w * 1024, g0 + ((c8 ^ (swk << 1)) << 4), 0);
    dma(rv, lb + L_V + w * 1024, g0 + ((c8 ^ (swv << 1)) << 4), 0);
    dma(srd(gq, S_ * D_ * 2), lb + L_X + w * 1024, g0 + ((c8 ^ (swv << 1)) << 4), 0);
    const u32x4 rf = srd(gf, S_ * D_ * 2);
    const int fq = vp * 64 + l;
    const u32 fo = (u32)((2 * cp + cch) * 4096 + 16 * fq);
    asm volatile("buffer_load_dwordx4 %0, %2, %4, 0 offen\n\tbuffer_load_dwordx2 %1, %3, %4, 0 offen" : "=&v"(fhi), "=&v"(flo) : "v"(fo), "v"(fq > 0 ? fo - 8 : OOB), "s"(rf) : "memory");
    asm volatile("buffer_load_ushort %0, %1, %2, 0 offen" : "=v"(Dh) : "v"((u32)((2 * cp + ((l >> 2) & 1)) * 2)), "s"(srd(gD, D_ * 2)) : "memory");
    e0 = flag_ld(dw, WS_E);
    SB();
  }
  const int gx = (xc >> 1) * 28 + sc, gi = 8 * (xc & 1) + (gx & 7), gj = gx >> 3;
  W1 w1;
  {
    const u32 v0 = (u32)(((4 * w + G) * F_ + 128 * gj) * 2);
    w1.rg = srd(gwg, D_ * F_ * 2); w1.ru = srd(gwu, D_ * F_ * 2); w1.lb = lb + w * 1024;
    w1.rot = ROT1;
    w1.vg = v0 + ((i16 ^ (4 * G)) << 4); w1.vu = v0 + ((i16 ^ (4 * G + 2)) << 4);
    w1.ra = (u32)((G & 1) * 8192 + (8 * (G >> 1) + q4) * 256 + ((w ^ (2 * q4 + (G & 1))) * 32) + p4 * 8);
  }
  w1_issue<0>(w1, L_FC); w1_issue<1>(w1, L_FC + 16384);
  VMW(9); bar();
  STAMP(1);
  {
    const int tbl = w >> 2, chg = w & 3;
    const u32 rb = (u32)((32 * tbl + 8 * G + q4) * 128 + p4 * 8);
    const u32 sv = (u32)((chg ^ ((q4 >> 1) | ((G & 1) << 1))) * 32), sk2 = sv ^ 64;
    const u32x2 km = trd(lds, L_K + rb + 512 + sv), k0 = trd(lds, L_K + rb + 1024 + sk2), k1 = trd(lds, L_K + rb + 1536 + sk2);
    const u32x2 v0 = trd(lds, L_V + rb + sv), v1 = trd(lds, L_V + rb + 512 + sv);
    const u32x2 skv = *(const u32x2*)(lds + L_SK + (16 * chg + i16) * 8);
    const float kk[11] = {bfhi(km.x), bflo(km.y), bfhi(km.y), bflo(k0.x), bfhi(k0.x), bflo(k0.y), bfhi(k0.y), bflo(k1.x), bfhi(k1.x), bflo(k1.y), bfhi(k1.y)};
    const float vv[8] = {bflo(v0.x), bfhi(v0.x), bflo(v0.y), bfhi(v0.y), bflo(v1.x), bfhi(v1.x), bflo(v1.y), bfhi(v1.y)};
    const float s0 = bflo(skv.x), s1 = bfhi(skv.x), s2 = bflo(skv.y), s3 = bfhi(skv.y);
    float u[8];
#pragma unroll
    for (int e = 0; e < 8; ++e) u[e] = (kk[e + 3] * s0 + kk[e + 2] * s1 + kk[e + 1] * s2 + kk[e] * s3) * vv[e];
    const u32 o = (u32)((2 * tb64 + tbl) * TB + (64 * role + 16 * chg + i16) * 64 + G * 16);
    __builtin_amdgcn_raw_buffer_store_b128(u32x4{pk(u[0], u[1]), pk(u[2], u[3]), pk(u[4], u[5]), pk(u[6], u[7])}, rw, WS_U + o, 0, SC1);
    SB();
    STAMP(2);
    VMW(9); bar();
    const u32x2 x0 = trd(lds, L_X + rb + sv), x1 = trd(lds, L_X + rb + 512 + sv);
    __builtin_amdgcn_raw_buffer_store_b128(cat(x0, x1), rw, WS_X + o, 0, SC1);
    VMW(6); PIN(fhi); PIN(flo); PIN(Dh); PIN(e0);
    SB();
  }
  STAMP(3);
  const u64 tag = mktag(e0);
  const float Dv = bflo(Dh);
  SB();
  w1_issue<2>(w1, L_RING); w1_issue<3>(w1, L_RING + 16384);
  STAMP(4);
  VMW(4); bar();
  if (tid == 0) setflag(ws, WS_F1 + (tb64 * 8 + role) * 128, tag);
  STAMP(5);
  const int hh = l >> 5, tk = l & 31;
  const u32 gso = WS_G + (u32)(((2 * gi * 2 + (tk >> 4)) * 56 + 4 * gj + (w >> 1)) * 1024 + (2 * (w & 1) + hh) * 256 + i16 * 16);
  auto gofs = [](int g) AI { return (u32)((32 * (g >> 1) + (g & 1)) * 2 * 56 * 1024); };
  u32x4 W[28];
  f32x4 cs0 = {}, cs1 = {};
  auto colsum = [&](int k0, int k1) AI {
#pragma unroll
    for (int k = k0; k < k1; ++k) { cs0 = mfma4(u32x2{ONES, ONES}, u32x2{W[k].x, W[k].y}, cs0); cs1 = mfma4(u32x2{ONES, ONES}, u32x2{W[k].z, W[k].w}, cs1); }
  };
  const u32 ra0 = opq(w1.ra), ra1 = opq(w1.ra + L_RING), ra2 = opq(w1.ra + L_RING + 49152);
  w1_read<0>(lds, ra0 + L_FC, W); w1_read<1>(lds, ra0 + L_FC + 16384, W);
  w1_issue<7>(w1, L_RED); w1_issue<8>(w1, L_RED + 16384);
  bar();
  {
    const u32 a0 = __builtin_amdgcn_alignbit(flo.y, flo.x, 16), a1 = __builtin_amdgcn_alignbit(fhi.x, flo.y, 16), a2 = __builtin_amdgcn_alignbit(fhi.y, fhi.x, 16),
              a3 = __builtin_amdgcn_alignbit(fhi.z, fhi.y, 16), a4 = __builtin_amdgcn_alignbit(fhi.w, fhi.z, 16);
    char* fc = lds + L_FC + cch * FCH + (vp * 64 + l) * 16 + 32;
    *(u32x4*)(fc) = fhi;
    *(u32x4*)(fc + FCS) = u32x4{a1, a2, a3, a4};
    *(u32x4*)(fc + 2 * FCS) = u32x4{flo.y, fhi.x, fhi.y, fhi.z};
    *(u32x4*)(fc + 3 * FCS) = u32x4{a0, a1, a2, a3};
    if (tid >= 128 && tid < 144) *(u32x4*)(lds + L_FC + (tid & 7) * FCS + ((tid >> 3) & 1) * 16) = u32x4{0, 0, 0, 0};
    if (tid >= 192 && tid < 252) { const int z = tid - 192; *(u32x4*)(lds + L_U + (z / 30) * UCH + (z % 30) * 16) = u32x4{0, 0, 0, 0}; }
  }
  SB();
  colsum(0, 4);
  w1_issue<4>(w1, L_RING + 32768); w1_issue<5>(w1, L_RING + 49152); w1_issue<6>(w1, L_RING + 65536);
  SB();

  STAMP(6);
  poll(ws, WS_F1 + (u32)(32 * w + 8 * (l & 3) + ((l & 4) ? 7 : (cp >> 5))) * 128, l < 8, tag);
  STAMP(7);
  u32x4 uu = ldg(dw, eo, WS_U), ex = ldg(dw, eo, WS_X);
  u32x4 est[4];
#pragma unroll
  for (int i = 0; i < 4; ++i) est[i] = ldg(dw, (u32)(r8 * 64 + p4 * 16), WS_ST + (u32)(w * 2048 + i * 512));
  bar();
  w1_read<7>(lds, ra0 + L_RED, W); w1_read<8>(lds, ra0 + L_RED + 16384, W);
  w1_read<2>(lds, ra1, W); w1_read<3>(lds, ra1 + 16384, W); w1_read<4>(lds, ra1 + 32768, W); w1_read<5>(lds, ra2, W); w1_read<6>(lds, ra2 + 16384, W);
  const u32 aA = opq(L_FC + cch * FCH + q4 * FCS + 64 - 16 * G + 8 * p4 - 8 + 128 * vp);
  const u32 aB = opq(L_U + cch * UCH + 32 * i16 + 16 * G + 448 - 128 * vp - 64);
  const u32 rwr = opq(L_RED + w * 4096 + G * 256 + ((i16 ^ ((G >> 1) << 3)) << 4));
  const u32 rr = opq(L_RED + ech * 4096 + (w & 3) * 1024 + (p4 & 1) * 512 + (((2 * r8 + (p4 >> 1)) ^ ((p4 & 1) << 3)) << 4));
  colsum(4, 18);
  PIN(cs0); PIN(cs1);
  SB();
  vmw<5>(uu);
  *(u32x4*)(lds + L_U + ech * UCH + 480 + (8 * w + r8) * 64 + p4 * 16) = uu;
  bar();
  STAMP(8);
  auto rdA = [&](int v, int m) AI { const u32 o = 64 * v + 512 * m; return cat(trd(lds, aA + o + 8), trd(lds, aA + o)); };
  auto rdB = [&](int v, int m) AI { return *(const u32x4*)(lds + aB + 64 * (1 - v) + 512 * m); };
  float ex8[8], eq8[8], eu8[8];
  auto terms = [&]() AI {
#pragma unroll
    for (int i = 0; i < 4; ++i) {
      ex8[2 * i] = bflo(ex[i]); ex8[2 * i + 1] = bfhi(ex[i]);
      eu8[2 * i] = bflo(uu[i]) * Dv; eu8[2 * i + 1] = op(bfhi(uu[i]) * Dv);
      eq8[2 * i] = (ex8[2 * i] - __uint_as_float(est[i].x)) * __uint_as_float(est[i].y);
      eq8[2 * i + 1] = op(op(ex8[2 * i + 1] - __uint_as_float(est[i].z)) * __uint_as_float(est[i].w));
    }
#pragma unroll
    for (int i = 0; i < 8; ++i) { PIN(ex8[i]); PIN(eq8[i]); PIN(eu8[i]); }
  };
  auto epilogue = [&]() AI {
    f32x4 p[8];
#pragma unroll
    for (int n = 0; n < 4; ++n) { p[2 * n] = *(const f32x4*)(lds + rr + n * 8192); p[2 * n + 1] = *(const f32x4*)(lds + rr + n * 8192 + 256); }
    float h[8];
#pragma unroll
    for (int i = 0; i < 8; ++i) {
      const float y = op(op(eu8[i] + p[i >> 2][i & 3]) + p[2 + (i >> 2)][i & 3]) + op(p[4 + (i >> 2)][i & 3] + p[6 + (i >> 2)][i & 3]);
      h[i] = op(ex8[i] + eq8[i] * op(y));
    }
    __builtin_amdgcn_raw_buffer_store_b128(u32x4{pk(h[0], h[1]), pk(h[2], h[3]), pk(h[4], h[5]), pk(h[6], h[7])}, rw, WS_H + eo, 0, SC1);
  };
  u64 fl;
  const u32 f2o = WS_F2 + (u32)(8 * (w & 3) + (l & 7)) * 128;
  {
    f32x4 acc[4];
    u32x4 A[2][4], B[2][4];
#pragma unroll
    for (int v = 0; v < 2; ++v) {
      A[v][0] = rdA(v, 0); SB();
#pragma unroll
      for (int m = 0; m < 4; ++m) { B[v][m] = rdB(v, m); SB(); }
#pragma unroll
      for (int m = 1; m < 4; ++m) { A[v][m] = rdA(v, m); SB(); }
      if (v == 0) {
        vmw<0>(ex); PIN(est[0]); PIN(est[1]); PIN(est[2]); PIN(est[3]);
        fl = flag_ld(dw, f2o);
        if (w < 4) terms();
        SB();
      }
    }
#pragma unroll
    for (int v = 0; v < 2; ++v)
#pragma unroll
      for (int m = 0; m < 4; ++m) {
#pragma unroll
        for (int c = m; c < 4; ++c) acc[c] = mfma16(A[v][m], B[v][c - m], (v == 0 && m == 0) ? f32x4{} : acc[c]);
        SB();
        if (v == 0 && m == 0) w1_g<9>(w1, L_RING);
        if (v == 0 && m == 2) w1_u<9>(w1, L_RING);
        if (v == 1 && m == 0) w1_g<10>(w1, L_RING + 16384);
        SB();
      }
#pragma unroll
    for (int c = 0; c < 4; ++c) *(f32x4*)(lds + rwr + c * 1024) = acc[c];
  }
  VMWP(3, fl);
  if (RARE(!__all(fl == tag))) poll<2>(ws, f2o, true, tag);
  bar();
  STAMP(9);
  if (w < 4) epilogue();
  SB();
  STAMP(10);
  {
    f32x4 acc[4];
    u32x4 A[2][8], B[2][8];
    auto rd = [&](int v, int m) AI {
      if (m == 0) {
        A[v][0] = rdA(v, 0);
#pragma unroll
        for (int d = 4; d < 8; ++d) B[v][d] = rdB(v, d);
      } else { A[v][m] = rdA(v, m); if (m <= 4) B[v][4 - m] = rdB(v, 4 - m); }
      SB();
    };
    rd(0, 0); rd(0, 1); rd(0, 2); rd(0, 3);
#pragma unroll
    for (int v = 0; v < 2; ++v) {
#pragma unroll
      for (int m = 0; m < 8; ++m) {
#pragma unroll
        for (int c = 4; c < 8; ++c) if (m <= c) acc[c - 4] = mfma16(A[v][m], B[v][c - m], (v == 0 && m == 0) ? f32x4{} : acc[c - 4]);
        SB();
        if (m < 4) rd(v, m + 4); else if (v == 0) rd(1, m - 4);
        if (v == 0) {
          if (m == 0) w1_u<10>(w1, L_RING + 16384);
          if (m == 1) w1_g<11>(w1, L_RING + 32768);
          if (m == 2) w1_u<11>(w1, L_RING + 32768);
          if (m == 3) w1_g<12>(w1, L_RING + 49152);
          if (m == 4) w1_u<12>(w1, L_RING + 49152);
          if (m == 6) w1_g<13>(w1, L_RING + 65536);
          if (m == 7) w1_u<13>(w1, L_RING + 65536);
        }
        SB();
      }
    }
    if (w >= 4) terms();
    STAMP(11);
    VMW(0); bar();
#pragma unroll
    for (int c = 0; c < 4; ++c) *(f32x4*)(lds + rwr + c * 1024) = acc[c];
  }
  const u32 lo16 = l * 16;
  if (w < 4) {
#pragma unroll
    for (int k = 0; k < 7; ++k) dma<SC1>(dw, lb + L_H0 + (7 * w + k) * 1024, lo16, WS_H + (u32)(2 * gi * TB) + wrap(7 * w + k + 2 * w1.rot, 28) * 1024);
  }
  w1_read<9>(lds, ra1, W); w1_read<10>(lds, ra1 + 16384, W); w1_read<11>(lds, ra1 + 32768, W); w1_read<12>(lds, ra2, W); w1_read<13>(lds, ra2 + 16384, W);
  bar();
  STAMP(12);

  const u32 bta = (u32)((8 * (G >> 1) + q4) * 64 + (G & 1) * 32 + p4 * 8);
  auto hbase = [](int g) AI { return g == 0 ? L_H0 : g == 1 ? L_H1 : g == 2 ? L_H2 : L_H3; };
  u32 csh, csl;
  auto colfin = [&]() AI {
    colsum(18, 28);
    const float t = hsum(cs0.x + cs1.x);
    const u32 hi = pk(t, t);
    csh = hh ? 0u : hi; csl = hh ? 0u : pk(t - bflo(hi), 0.f);
    PIN(csh); PIN(csl);
  };
  f32x4 tm; float gu[8], gt[8]; u32 od[4];
  auto center = [&](f32x16& acc, float m) AI {
    const u32 d = __float_as_uint(m);
    acc = mfma32(u32x4{csh, csl, 0, 0}, u32x4{d, d & 0xffffu, 0, 0}, acc);
  };
  auto geglu_step = [&](int g, f32x16& acc, int i) AI {
#pragma unroll
    for (int e = 0; e < 8; ++e) {
      const int s = i - (2 + (18 * e) / 7);
      if (s == 0) { gt[e] = acc[e] * acc[e]; gu[e] = op(acc[e] * acc[e + 8]); PIN(gt[e]); PIN(gu[e]); }
      if (s == 1) { gt[e] = tm.w * gt[e] + tm.z; PIN(gt[e]); }
      if (s == 2) { gt[e] = acc[e] * gt[e]; PIN(gt[e]); }
      if (s == 3) { gt[e] = __builtin_amdgcn_exp2f(gt[e]); PIN(gt[e]); }
      if (s == 4) { gt[e] = gt[e] * tm.y + tm.y; PIN(gt[e]); }
      if (s == 5) { gt[e] = __builtin_amdgcn_rcpf(gt[e]); PIN(gt[e]); }
      if (s == 6) { gu[e] = gu[e] * gt[e]; PIN(gu[e]); }
      if (s == 7 && (e & 1)) { od[e >> 1] = pk(gu[e - 1], gu[e]); PIN(od[e >> 1]); }
    }
    if (i == 28) {
      __builtin_amdgcn_raw_buffer_store_b128(u32x4{od[0], od[1], od[2], od[3]}, rw, gso, gofs(g), SC1);
    }
  };
  constexpr int PF = 4;
  u32x4 B[PF];
  u32 ba = opq(hbase(0) + bta);
  auto chain = [&](int g, f32x16& acc, auto side) AI {
    const u32 bn = opq(hbase(g < 3 ? g + 1 : g) + bta);
    sfor<28>([&](auto kc) AI {
      constexpr int k = decltype(kc)::value;
      acc = mfma32(W[k], B[k % PF], k == 0 ? f32x16{} : acc);
      SB();
      side(k);
      if (k + PF < 28) B[k % PF] = cat(trd(lds, ba + (k + PF) * 1024), trd(lds, ba + (k + PF) * 1024 + 256));
      else if (g < 3) B[k % PF] = cat(trd(lds, bn + (k + PF - 28) * 1024), trd(lds, bn + (k + PF - 28) * 1024 + 256));
      SB();
    });
    ba = bn;
  };
  auto flag3 = [&](int g) AI { setflag(ws, WS_F3 + (u32)(((gi * 4 + g) * 14) + gj) * 128, tag); };
  const int ri = 2 * xc + sc / 14, rq2 = (sc / 7) & 1, n2 = sc % 7;
  const u32x4 rd = srd(gwd, D_ * F_ * 2);
  const u32 wdv = (u32)(((224 * w + r8) * D_ + 64 * n2) * 2 + (((l & 7) ^ (((r8 >> 1) & 3) << 1)) << 4));
  const u32 blkR = w < 7 ? L_R + w * 4096 : L_R2;
  u32 wda[4], wdr[4];
#pragma unroll
  for (int nt = 0; nt < 4; ++nt) {
    const u32 a = (16 * (G >> 1) + 4 * (G & 1) + q4) * 128 + ((nt ^ (2 * (G & 1) + (q4 >> 1))) * 32) + p4 * 8;
    wda[nt] = opq(a + L_P + w * 4096); wdr[nt] = opq(a + blkR);
  }
  u32x4 Wd[7][4];
  auto wblk = [](int k) AI { return (k == 1 || k == 4) ? 1 : (k == 3 || k == 6) ? 2 : 0; };
  auto wd_dma = [&](int k, int i) AI {
    if (i == 0) dma_at(lb + (wblk(k) == 0 ? blkR : (wblk(k) == 2 ? L_Q : L_P) + w * 4096));
    dma_i<0>(i, rd, wdv, 4 * k * (8 * D_ * 2) + i * (8 * D_ * 2 - 1024));
  };
  auto wd_rd = [&](int k, int nt) AI {
    const u32 a = wblk(k) == 0 ? wdr[nt] : wda[nt] + (wblk(k) == 2 ? L_Q - L_P : 0);
    Wd[k][nt] = cat(trd(lds, a), trd(lds, a + 1024));
  };
  auto wd_read = [&](int k) AI { wd_rd(k, 0); wd_rd(k, 1); wd_rd(k, 2); wd_rd(k, 3); };
  const int j0 = (14 * w) >> 3, jn = ((14 * w + 13) >> 3) - j0;
  const u32 f3o = WS_F3 + (u32)(ri * 56 + j0 + ((l & 3) < jn ? (l & 3) : jn)) * 128;

  f32x16 accA, accB;
  u32x4 tm0, tm1, tm2, tm3;
  const u32 s2o = WS_S2 + (u32)(2 * gi * 512 + tk * 16);
  const u32 hp = (u32)(w < 4 ? 4 * w : 4 + 3 * w) * 1024;
  u32 hs[4];
#pragma unroll
  for (int i = 0; i < 4; ++i) hs[i] = (wrap((hp >> 10) + i + 2 * w1.rot, 28) - i) * 1024;
  auto htb = [&](int g) AI { return WS_H + (u32)((32 * (g >> 1) + 2 * gi + (g & 1)) * TB); };
  auto h_dma = [&](int g, int i) AI {
    if (i == 0) dma_at(lb + hbase(g) + hp);
    if (i < 3 || w < 4) dma_i<SC1>(i, dw, lo16, htb(g) + hs[i]);
  };
  auto redo1 = [&](int g, f32x16& acc, u32 m) AI {
    if (RARE(__any(acc[0] != acc[0]))) {
      REDO_COUNT();
      const u32 sc0 = L_SP + w * 1024;
      acc = f32x16{};
      const rsrc_t rg = mk(gwg, D_ * F_ * 2), ru = mk(gwu, D_ * F_ * 2);
#pragma unroll 1
      for (int k = 0; k < 28; ++k) {
        const u32 wo = (u32)(((16 * k + ((l >> 1) & 15)) * F_ + 128 * gj + 16 * w) * 2 + (l & 1) * 16);
        const u32x4 wg = __builtin_amdgcn_raw_buffer_load_b128(rg, wo, 0, 0), wu = __builtin_amdgcn_raw_buffer_load_b128(ru, wo, 0, 0);
        *(u32x4*)(lds + sc0 + lo16) = l < 32 ? wg : wu;
        const u32 wa = sc0 + (G & 1) * 512 + (8 * (G >> 1) + q4) * 32 + p4 * 8;
        const u32x4 a = cat(trd(lds, wa), trd(lds, wa + 128));
        u32x4 v;
#pragma unroll 1
        for (int it = 0; it < 400; ++it) { v = ldg(dw, lo16, htb(g) + k * 1024); vmw<0>(v); if (!__any(max4(v) == EMPTY)) break; }
        *(u32x4*)(lds + sc0 + lo16) = v;
        acc = mfma32(a, cat(trd(lds, sc0 + bta), trd(lds, sc0 + bta + 256)), acc);
      }
      center(acc, __uint_as_float(m));
    }
  };
  auto tm_fix = [&](u32x4& t, u32 so) AI {
    if (RARE(__any(max4(t) == EMPTY))) {
#pragma unroll 1
      for (int it = 0; it < 4000; ++it) { FIX_COUNT(); t = ldg(dw, s2o, so); vmw<0>(t); if (!__any(max4(t) == EMPTY)) break; }
    }
  };
  SB();
  if (w >= 4) {
    epilogue();
    SB();
    colfin();
  } else {
    colfin();
    SB();
    const u32 ha = opq(L_H0 + 7 * w * 1024 + lo16);
#pragma unroll 1
    for (int it = 0; it < 4000; ++it) {
      VMW(0);
      u32 m = 0;
#pragma unroll
      for (int k = 0; k < 7; ++k) { const u32x4 v = *(const u32x4*)(lds + ha + k * 1024); m = max(m, max4(v)); }
      if (!__any(m == EMPTY)) break;
#pragma unroll
      for (int k = 0; k < 7; ++k) dma<SC1>(dw, lb + L_H0 + (7 * w + k) * 1024, lo16, WS_H + (u32)(2 * gi * TB) + wrap(7 * w + k + 2 * w1.rot, 28) * 1024);
    }
    VMW(0);
    STAMP(13);
  }
  __builtin_amdgcn_s_setprio(1);
  bar();
  STAMP(14);
#pragma unroll
  for (int k = 0; k < PF; ++k) B[k] = cat(trd(lds, ba + k * 1024), trd(lds, ba + k * 1024 + 256));
  SB();
  chain(0, accA, [&](int k) AI {
    if (k <= 6 && !(k & 1)) h_dma(1, k >> 1);
    if (k == TM0K) tm0 = ldg(dw, s2o, 0);
    if (k == 10 || k == 13) __builtin_amdgcn_raw_buffer_store_b128(u32x4{EMPTY, EMPTY, EMPTY, EMPTY}, rw, gso, gofs(k == 10 ? 2 : 3), SC1);
    if (k >= 16 && k <= 25 && k % 3 == 1) wd_dma(0, (k - 16) / 3);
    if (k == 23) { VMW(6); RBAR(); if (tid == 0 && b == 0) setflag(ws, WS_E, tag); }
    if (k == 27) { vmw<(TM0K > 13 ? 4 : 6)>(tm0); tm_fix(tm0, 0); }
  });
  center(accA, __uint_as_float(tm0.x));
  tm = __builtin_bit_cast(f32x4, tm0);
  SB();
  STAMP(16);
  chain(1, accB, [&](int k) AI {
    if (k == 1) RBAR();
    geglu_step(0, accA, k);
    if (k == 2) tm1 = ldg(dw, s2o, 512);
    if (k == 3 || k == 5) wd_dma(1, (k - 3) >> 1);
    if (k >= 7 && k <= 10) h_dma(2, k - 7);
    if (k == 12) { VMW(6); dma_at(lb + L_P + w * 4096); }
    if (k == 13 || k == 15) wd_dma(1, (k - 9) >> 1);
    if (k >= 12 && k <= 18 && !(k & 1)) wd_rd(0, (k - 12) >> 1);
    if (k == 17) { vmw<7>(tm1); tm_fix(tm1, 512); }
    if (k >= 21 && (k & 1)) wd_dma(2, (k - 21) >> 1);
    if (k == 23) { VMW(4); RBAR(); }
  });
  center(accB, __uint_as_float(tm1.x));
  geglu_step(0, accA, 28);
  tm = __builtin_bit_cast(f32x4, tm1);
  SB();
  STAMP(18);
  chain(2, accA, [&](int k) AI {
    if (k == 1) RBAR();
    if (k == 2) redo1(1, accB, tm1.x);
    geglu_step(1, accB, k);
    if (k <= 6 && !(k & 1)) h_dma(3, k >> 1);
    if (k >= 3 && k <= 9 && (k & 1)) { if (k == 3) VMW(7); wd_rd(1, (k - 3) >> 1); }
    if (k == 8) { tm2 = ldg(dw, s2o, 32 * 512); tm3 = ldg(dw, s2o, 33 * 512); }
    if (k >= 10 && k <= 16 && !(k & 1)) wd_dma(3, (k - 10) >> 1);
    if (k >= 18 && k <= 27 && k % 3 == 0) wd_dma(4, (k - 18) / 3);
    if (k == 23) { vmw<6>(tm2); PIN(tm3); RBAR(); if (tid == 0) flag3(0); }
    if (k == 25) tm_fix(tm2, 32 * 512);
    if (k == 26) tm_fix(tm3, 33 * 512);
  });
  center(accA, __uint_as_float(tm2.x));
  geglu_step(1, accB, 28);
  tm = __builtin_bit_cast(f32x4, tm2);
  SB();
  STAMP(20);
  const u32 gb = WS_G + (u32)((2 * ri * 2 + rq2) * 56 + 7 * w) * 1024;
  constexpr int NS = 9;
  u32x4 Hf[NS];
  auto hl = [&](int t) AI { Hf[t % NS] = ldg(dw, lo16, gb + gofs(t / 7) + (t % 7) * 1024); };
  chain(3, accB, [&](int k) AI {
    if (k == 2) redo1(2, accA, tm2.x);
    geglu_step(2, accA, k);
    if (k >= 5 && k < 13) { if (k == 5) VMW(5); wd_rd(2 + (k - 5) / 4, (k - 5) & 3); }
    if (k == 10) fl = flag_ld(dw, f3o);
    if (k >= 12 && k <= 18 && !(k & 1)) wd_dma(5, (k - 12) >> 1);
    if (k == 13) { VMW(2); RBAR(); if (tid == 0) flag3(1); }
    if (k >= 13 && k < 21 && (k & 1)) wd_rd(4, (k - 13) >> 1);
    if (k >= 19 && k <= 22) wd_dma(6, k - 19);
    if (k == 23) { VMWP(8, fl); if (RARE(!__all(fl == tag))) poll(ws, f3o, true, tag); }
    if (k >= 23) hl(k - 23);
  });
  center(accB, __uint_as_float(tm3.x));
  geglu_step(2, accA, 28);
  tm = __builtin_bit_cast(f32x4, tm3);
  redo1(3, accB, tm3.x);
  STAMP(21);
#pragma unroll
  for (int e = 0; e < 8; ++e) {
    const float a = accB[e], u = accB[e + 8];
    gu[e] = (a * u) * __builtin_amdgcn_rcpf(__builtin_amdgcn_exp2f(a * (tm.w * (a * a) + tm.z)) * tm.y + tm.y);
  }
#pragma unroll
  for (int e = 0; e < 4; ++e) od[e] = pk(gu[2 * e], gu[2 * e + 1]);
  geglu_step(3, accB, 28);
  __builtin_amdgcn_s_setprio(0);
  SB();
  STAMP(22);
  f32x4 acc[2][4];
  const rsrc_t ro = mk(gout, S_ * D_ * 2);
  const u32 pw = (u32)(i16 * 256), tf = 4 * (w & 3) + G;
  const u32 hro = WS_H + (u32)((32 * (G >> 1) + (G & 1) + 2 * ri) * TB + (64 * n2 + 8 * w + (i16 >> 1)) * 64 + rq2 * 32 + (i16 & 1) * 16);
  auto pbuf = [](int g) AI { return g == 3 ? L_T : g == 1 ? L_Q : L_P; };
  auto put = [&](int g) AI {
    const u32 rb = opq(pbuf(g) + w * 4096 + pw);
#pragma unroll
    for (int nt = 0; nt < 4; ++nt) *(f32x4*)(lds + rb + (((4 * nt + G) ^ (i16 & 7)) << 4)) = acc[g & 1][nt];
  };
  auto fin = [&](int g) AI {
    if ((w >> 2) == (g & 1)) {
      const u32 a0 = tf * 256 + ((i16 ^ (tf & 7)) << 4), a = opq(pbuf(g) + a0);
      f32x4 p[9];
      p[8] = *(const f32x4*)(lds + L_RES + g * 4096 + a0);
#pragma unroll
      for (int n = 0; n < 8; ++n) p[n] = *(const f32x4*)(lds + a + n * 4096);
      const f32x4 sum = (((p[0] + p[1]) + (p[2] + p[3])) + ((p[4] + p[5]) + (p[6] + p[7]))) + p[8];
      const int tok = 32 * (32 * (g >> 1) + (g & 1) + 2 * ri) + 16 * rq2 + tf;
      __builtin_amdgcn_raw_buffer_store_b64(u32x2{pk(sum.x, sum.y), pk(sum.z, sum.w)}, ro, (u32)((tok * D_ + 64 * n2 + 4 * i16) * 2), 0, 0);
    }
  };
  auto redo = [&](int g) AI {
    if (RARE(__any(acc[g & 1][0].x != acc[g & 1][0].x))) {
      REDO_COUNT();
      u32x4 v[7];
#pragma unroll 1
      for (int it = 0; it < 4000; ++it) {
        u32 m = 0;
#pragma unroll
        for (int k = 0; k < 7; ++k) v[k] = ldg(dw, lo16, gb + gofs(g) + k * 1024);
#pragma unroll
        for (int k = 0; k < 7; ++k) { vmw<0>(v[k]); m = max(m, max4(v[k])); }
        if (!__any(m == EMPTY)) break;
      }
#pragma unroll
      for (int k = 0; k < 7; ++k)
#pragma unroll
        for (int nt = 0; nt < 4; ++nt) acc[g & 1][nt] = mfma16(Wd[k][nt], v[k], k == 0 ? f32x4{} : acc[g & 1][nt]);
    }
  };
  fl = flag_ld(dw, f3o + 14 * 128);
  u32x4 hr = ldg(dw, hro, 0);
  hl(5); hl(6);
  VMW(11);
  wd_read(5); wd_read(6);
  SB();
  STAMP(23);
  sfor<28>([&](auto tc) AI {
    constexpr int t = decltype(tc)::value, g = t / 7, k = t % 7;
    constexpr auto pos = [](int h) { return h < 5 ? h : h + 4; };
    constexpr auto last = [](int u) { return u < 2 ? 6 : u == 2 ? 8 : u == 3 ? 10 : u < 21 ? u + 7 : 27; };
    if (t == 2) { VMWP(3, fl); if (RARE(!__all(fl == tag))) poll(ws, f3o + 14 * 128, true, tag); }
#pragma unroll
    for (int h = (t ? last(t - 1) : 6) + 1; h <= last(t); ++h) hl(h);
    vmw<pos(last(t)) - pos(t)>(Hf[t % NS]);
#pragma unroll
    for (int nt = 0; nt < 4; ++nt) acc[g & 1][nt] = mfma16(Wd[k][nt], Hf[t % NS], k == 0 ? f32x4{} : acc[g & 1][nt]);
    SB();
    if (t == 3) {
      vmw<6>(hr);
      if (RARE(__any(max4(hr) == EMPTY))) {
#pragma unroll 1
        for (int it = 0; it < 4000; ++it) { hr = ldg(dw, hro, 0); vmw<0>(hr); if (!__any(max4(hr) == EMPTY)) break; }
      }
      *(u32x4*)(lds + blkR + lo16) = hr;
#pragma unroll
      for (int q = 0; q < 4; ++q) {
        const u32x2 r = trd(lds, blkR + q * 256 + (4 * (G & 1) + q4) * 32 + p4 * 8);
        *(f32x4*)(lds + L_RES + q * 4096 + pw + (((2 * w + (G & 1)) ^ (i16 & 7)) << 4)) = f32x4{bflo(r.x), bfhi(r.x), bflo(r.y), bfhi(r.y)};
      }
    }
    if (t == 8) put(0);
    if (t == 12) { bar(); STAMP(24); fin(0); }
    if (t == 15) put(1);
    if (t == 19) { bar(); STAMP(25); fin(1); }
    if (t == 22) { redo(2); put(2); STAMP(26); }
    SB();
  });
  redo(3);
  put(3);
  bar();
  STAMP(27);
  fin(2); fin(3);
  STAMP(28);
  STAMP_OUT();
}

}

extern "C" void kernel_launch(void* const* d_in, const int* in_sizes, int n_in, void* d_out, int out_size, void* d_ws, size_t ws_size,
                              hipStream_t stream) {
  h3_ffn<<<256, 512, 0, stream>>>((const char*)d_in[0], (const char*)d_in[1], (const char*)d_in[2], (const char*)d_in[3], (const char*)d_in[4],
                                  (const char*)d_in[5], (const char*)d_in[7], (const char*)d_in[8], (const char*)d_in[9], (char*)d_out, (char*)d_ws);
}
```

```cpp
#include <hip/hip_runtime.h>
#include <stdint.h>

namespace {

#define LAS __attribute__((address_space(3)))
#define GAS __attribute__((address_space(1)))
#define DI __device__ __forceinline__
#define AI __attribute__((always_inline))
typedef uint32_t u32;
typedef unsigned long long u64;
typedef u32 u32x2 __attribute__((ext_vector_type(2)));
typedef u32 u32x4 __attribute__((ext_vector_type(4)));
typedef short s16x4 __attribute__((ext_vector_type(4)));
typedef short s16x8 __attribute__((ext_vector_type(8)));
typedef float f32x4 __attribute__((ext_vector_type(4)));
typedef float f32x16 __attribute__((ext_vector_type(16)));
typedef __bf16 bf2 __attribute__((ext_vector_type(2)));
typedef __amdgpu_buffer_rsrc_t rsrc_t;
#define RLX_AGENT __ATOMIC_RELAXED, __HIP_MEMORY_SCOPE_AGENT

constexpr int S_ = 2048, D_ = 448, F_ = 1792;
constexpr u32 TB = 448 * 64;
constexpr u32 WS_E  = 0;
constexpr u32 WS_F1 = 128;
constexpr u32 WS_F3 = WS_F1 + 256 * 128;
constexpr u32 WS_F2 = WS_F3 + 896 * 128;
constexpr u32 WS_S2 = 256 * 1024 + 16384;
constexpr u32 WS_ST = 256 * 1024;
constexpr u32 WS_U  = WS_S2 + 32768;
constexpr u32 WS_X  = WS_U + 64 * TB;
constexpr u32 WS_H  = WS_X + 64 * TB;
constexpr u32 WS_G  = WS_H + 64 * TB;
constexpr u32 WS_END = WS_G + 64 * 112 * 1024;
constexpr u32 WS_DBG = 16u << 20;
constexpr int SC1 = 16;
#define PRETOUCH 0
constexpr u32 OOB = 0xFFFFFF00u;
constexpr u32 ONES = 0x3f803f80u;
constexpr u32 EMPTY = 0xffffffffu;

constexpr u32 L_K = 0, L_V = 9216, L_X = 17408, L_SK = 25600;
constexpr u32 L_RED = 0, L_FC = 32768, FCS = 4160, FCH = 4 * FCS;
constexpr u32 L_U = L_FC + 2 * FCH, UCH = 4608, L_RING = L_U + 2 * UCH;
constexpr u32 L_H0 = 32768, L_H1 = L_H0 + TB, L_H2 = 0, L_H3 = L_H1 + TB + 32768, L_SP = 155648;
constexpr u32 L_P = TB, L_Q = L_P + 32768, L_R = L_Q + 32768, L_R2 = L_H3 + TB;
constexpr u32 L_RES = 0, L_T = L_H3;
constexpr u32 LDS_BYTES = 163840;

DI float bflo(u32 x) { return __uint_as_float(x << 16); }
DI float bfhi(u32 x) { return __uint_as_float(x & 0xffff0000u); }
DI u32 pk(float lo, float hi) { bf2 p; p.x = (__bf16)lo; p.y = (__bf16)hi; return __builtin_bit_cast(u32, p); }
DI rsrc_t mk(const void* p, u32 bytes) { return __builtin_amdgcn_make_buffer_rsrc((void*)p, 0, bytes, 0x00020000); }
DI u32x2 trd(const char* lds, u32 a) { return __builtin_bit_cast(u32x2, __builtin_amdgcn_ds_read_tr16_b64_v4i16((LAS s16x4*)(lds + a))); }
DI u32x4 cat(u32x2 a, u32x2 b) { return u32x4{a.x, a.y, b.x, b.y}; }
DI u32x4 srd(const void* p, u32 bytes) { const u64 a = (u64)p; return u32x4{(u32)a, (u32)(a >> 32) & 0xffffu, bytes, 0x00020000u}; }
template <int AUX = 0> DI void dma(u32x4 r, u32 ldsoff, u32 voff, u32 soff) {
  if (AUX) asm volatile("s_mov_b32 m0, %0\n\ts_nop 0\n\tbuffer_load_dwordx4 %1, %2, %3 offen sc1 lds" :: "s"(ldsoff), "v"(voff), "s"(r), "s"(soff) : "memory");
  else asm volatile("s_mov_b32 m0, %0\n\ts_nop 0\n\tbuffer_load_dwordx4 %1, %2, %3 offen lds" :: "s"(ldsoff), "v"(voff), "s"(r), "s"(soff) : "memory");
}
DI void dma_at(u32 ldsoff) { asm volatile("s_mov_b32 m0, %0\n\ts_nop 0" :: "s"(ldsoff) : "memory"); }
template <int OFF, int AUX> DI void dma_go(u32x4 r, u32 voff, u32 soff) {
  if (AUX) asm volatile("buffer_load_dwordx4 %0, %1, %2 offen offset:%3 sc1 lds" :: "v"(voff), "s"(r), "s"(soff), "n"(OFF) : "memory");
  else asm volatile("buffer_load_dwordx4 %0, %1, %2 offen offset:%3 lds" :: "v"(voff), "s"(r), "s"(soff), "n"(OFF) : "memory");
}
template <int AUX> DI void dma_i(int i, u32x4 r, u32 voff, u32 soff) {
  if (i == 0) dma_go<0, AUX>(r, voff, soff); else if (i == 1) dma_go<1024, AUX>(r, voff, soff); else if (i == 2) dma_go<2048, AUX>(r, voff, soff); else dma_go<3072, AUX>(r, voff, soff);
}
DI float fnma(float a, float b, float c) { float r; asm("v_fma_f32 %0, -%1, %2, %3" : "=v"(r) : "v"(a), "v"(b), "v"(c)); return r; }
DI float op(float x) { asm("" : "+v"(x)); return x; }
DI u32 opq(u32 x) { asm volatile("" : "+v"(x)); return x; }
#define PIN(x) asm volatile("" : "+v"(x))
DI f32x4 mfma16(u32x4 a, u32x4 b, f32x4 c) { return __builtin_amdgcn_mfma_f32_16x16x32_bf16(__builtin_bit_cast(s16x8, a), __builtin_bit_cast(s16x8, b), c, 0, 0, 0); }
DI f32x16 mfma32(u32x4 a, u32x4 b, f32x16 c) { return __builtin_amdgcn_mfma_f32_32x32x16_bf16(__builtin_bit_cast(s16x8, a), __builtin_bit_cast(s16x8, b), c, 0, 0, 0); }
DI f32x4 mfma4(u32x2 a, u32x2 b, f32x4 c) { return __builtin_amdgcn_mfma_f32_4x4x4bf16_1k(__builtin_bit_cast(s16x4, a), __builtin_bit_cast(s16x4, b), c, 0, 0, 0); }
DI float dot2(u32 a, u32 b, float c) { return __builtin_amdgcn_fdot2_f32_bf16(__builtin_bit_cast(bf2, a), __builtin_bit_cast(bf2, b), c, false); }
#define VMW(n) asm volatile("s_waitcnt vmcnt(" #n ")" ::: "memory")
#define LG0() asm volatile("s_waitcnt lgkmcnt(0)" ::: "memory")
#define SB() __builtin_amdgcn_sched_barrier(0)
DI void bar() { asm volatile("s_waitcnt lgkmcnt(0)\n\ts_barrier" ::: "memory"); }
#define RBAR() asm volatile("s_barrier" ::: "memory")
DI float wsum(float v) {
  v += __builtin_amdgcn_update_dpp(0.f, v, 0x111, 0xf, 0xf, true);
  v += __builtin_amdgcn_update_dpp(0.f, v, 0x112, 0xf, 0xf, true);
  v += __builtin_amdgcn_update_dpp(0.f, v, 0x114, 0xf, 0xf, true);
  v += __builtin_amdgcn_update_dpp(0.f, v, 0x118, 0xf, 0xf, true);
  v += __builtin_amdgcn_update_dpp(0.f, v, 0x142, 0xa, 0xf, true);
  v += __builtin_amdgcn_update_dpp(0.f, v, 0x143, 0xc, 0xf, true);
  return __uint_as_float(__builtin_amdgcn_readlane(__float_as_uint(v), 63));
}
DI float hsum(float v) {
  const auto r = __builtin_amdgcn_permlane32_swap(__float_as_uint(v), __float_as_uint(v), false, false);
  return __uint_as_float(r[0]) + __uint_as_float(r[1]);
}
template <int SLEEP = 1> DI void poll(char* ws, u32 off, bool act, u64 tag) {
  bool ok = !act;
#pragma unroll 1
  for (int it = 0; it < 20000; ++it) {
    u64 x = tag;
    if (!ok) x = __hip_atomic_load((GAS u64*)(ws + off), RLX_AGENT);
    ok = (x == tag);
    if (__all(ok)) break;
    __builtin_amdgcn_s_sleep(SLEEP);
  }
  asm volatile("" ::: "memory");
}
DI u32 max4(u32x4 v) { return max(max(v.x, v.y), max(v.z, v.w)); }
DI u64 flag_ld(u32x4 r, u32 voff) { u64 v; asm volatile("buffer_load_dwordx2 %0, %1, %2, 0 offen sc1" : "=v"(v) : "v"(voff), "s"(r) : "memory"); return v; }
DI u32x4 ldg(u32x4 r, u32 voff, u32 soff) { u32x4 v; asm volatile("buffer_load_dwordx4 %0, %1, %2, %3 offen sc1" : "=v"(v) : "v"(voff), "s"(r), "s"(soff) : "memory"); return v; }
template <int N> DI void vmw(u32x4& x) { asm volatile("s_waitcnt vmcnt(%1)" : "+v"(x) : "n"(N) : "memory"); }
#define VMWP(n, x) asm volatile("s_waitcnt vmcnt(" #n ")" : "+v"(x) :: "memory")
DI void setflag(char* ws, u32 off, u64 tag) { __hip_atomic_store((GAS u64*)(ws + off), tag, RLX_AGENT); }

#define STAMP(i)
#define STAMPC(i)
#define REDO_COUNT()
#define STAMP_OUT()

DI float xsum(float a, float c) {
  const auto r = __builtin_amdgcn_permlane32_swap(__float_as_uint(a), __float_as_uint(c), false, false);
  return __uint_as_float(r[0]) + __uint_as_float(r[1]);
}
DI f32x4 ln2_terms(float t) {
  const auto r = __builtin_amdgcn_permlane32_swap(__float_as_uint(t), __float_as_uint(t), false, false);
  const float mean = __uint_as_float(r[0]) * (1.f / D_);
  const float V = fmaxf(__uint_as_float(r[1]) * (1.f / D_) - mean * mean, 0.f) + 1e-5f;
  const float rs = __builtin_amdgcn_rsqf(V);
  const float K1 = -2.f * 0.7978845608028654f * 1.4426950408889634f * rs;
  const float lo = -mean - bflo(pk(-mean, 0.f));
  return f32x4{__uint_as_float(pk(-mean, lo)), V, K1, (K1 * rs) * (0.044715f * rs)};
}
template <int N> struct IC { static constexpr int value = N; };
template <int N, int I = 0, class F> DI void sfor(F&& f) { if constexpr (I < N) { f(IC<I>{}); sfor<N, I + 1>(f); } }
struct W1 { u32x4 rg, ru; u32 vg, vu, ra, lb; };
template <int T> DI void w1_g(const W1& c, u32 base) { dma(c.rg, c.lb + base, c.vg, T * (32 * F_ * 2)); }
template <int T> DI void w1_u(const W1& c, u32 base) { dma(c.ru, c.lb + base + 8192, c.vu, T * (32 * F_ * 2)); }
template <int T> DI void w1_issue(const W1& c, u32 base) { w1_g<T>(c, base); w1_u<T>(c, base); }
template <int T> DI void w1_read(const char* lds, u32 a, u32x4 (&W)[28]) {
#pragma unroll
  for (int kk = 0; kk < 2; ++kk) W[2 * T + kk] = cat(trd(lds, a + kk * 4096), trd(lds, a + kk * 4096 + 1024));
}
DI float x8sum(float v) {
  v += __builtin_amdgcn_update_dpp(0.f, v, 0xb1, 0xf, 0xf, true);
  v += __builtin_amdgcn_update_dpp(0.f, v, 0x4e, 0xf, 0xf, true);
  v += __builtin_amdgcn_update_dpp(0.f, v, 0x141, 0xf, 0xf, true);
  return v;
}

__global__ __launch_bounds__(512, 2) void h3_ffn(const char* __restrict__ gq, const char* __restrict__ gk, const char* __restrict__ gv,
                                                 const char* __restrict__ gsk, const char* __restrict__ gf, const char* __restrict__ gD,
                                                 const char* __restrict__ gwg, const char* __restrict__ gwu, const char* __restrict__ gwd,
                                                 char* __restrict__ gout, char* ws) {
  __shared__ __attribute__((aligned(16))) char lds[LDS_BYTES];
  const int tid = threadIdx.x, l = tid & 63, w = __builtin_amdgcn_readfirstlane(tid >> 6);
  const int b = blockIdx.x, xc = b & 7, sl = b >> 3;
  const int tb64 = 4 * xc + (sl >> 3), role = sl & 7;
  const int sc = (sl >> 3) * 7 + role;
  asm volatile("" :: "s"(gq), "s"(gk), "s"(gv), "s"(gsk), "s"(gf), "s"(gD), "s"(gwg), "s"(gwu), "s"(gwd), "s"(gout), "s"(ws));
  STAMP(0);
  const rsrc_t rw = mk(ws, WS_END);
  const int G = l >> 4, i16 = l & 15, q4 = i16 >> 2, p4 = l & 3, r8 = l >> 3;
  auto mktag = [&](u64 e0) AI { return ((u64)__builtin_amdgcn_readfirstlane((u32)e0) | ((u64)__builtin_amdgcn_readfirstlane((u32)(e0 >> 32)) << 32)) * 6364136223846793005ull + 1442695040888963407ull; };
  if (role == 7) {
    const rsrc_t rq = mk(gq, S_ * D_ * 2);
    const int row = 64 * tb64 + 8 * w + r8;
    u32x4 xs[7];
#pragma unroll
    for (int i = 0; i < 7; ++i) xs[i] = __builtin_amdgcn_raw_buffer_load_b128(rq, (u32)(row * 896 + (l & 7) * 16 + i * 128), 0, 0);
    const u64 e0 = __hip_atomic_load((GAS u64*)(ws + WS_E), RLX_AGENT);
    __builtin_amdgcn_raw_buffer_store_b128(u32x4{EMPTY, EMPTY, EMPTY, EMPTY}, rw, tid < 64 ? WS_S2 + (u32)((tb64 + (tid & 32)) * 512 + (tid & 31) * 16) : OOB, 0, SC1);
    SB();
    const u64 tag = mktag(e0);
    float s[4] = {};
#pragma unroll
    for (int i = 0; i < 7; ++i)
#pragma unroll
      for (int e = 0; e < 4; ++e) s[e] += bflo(xs[i][e]) + bfhi(xs[i][e]);
    const float mean = x8sum((s[0] + s[1]) + (s[2] + s[3])) * (1.f / D_);
    float v[4] = {};
#pragma unroll
    for (int i = 0; i < 7; ++i)
#pragma unroll
      for (int e = 0; e < 4; ++e) { const float d0 = bflo(xs[i][e]) - mean, d1 = bfhi(xs[i][e]) - mean; v[e] += d0 * d0; v[e] += d1 * d1; }
    const float rstd = __builtin_amdgcn_rsqf(x8sum((v[0] + v[1]) + (v[2] + v[3])) * (1.f / D_) + 1e-5f);
    const u32 so = WS_ST + (row >> 8) * 2048 + ((row >> 1) & 3) * 512 + ((row >> 5) & 7) * 64 + ((row >> 3) & 3) * 16 + (row & 1) * 8;
    __builtin_amdgcn_raw_buffer_store_b64(u32x2{__float_as_uint(mean), __float_as_uint(rstd)}, rw, (l & 7) == 0 ? so : OOB, 0, SC1);
    VMW(0); bar();
    if (tid == 0) setflag(ws, WS_F1 + (tb64 * 8 + 7) * 128, tag);
    STAMP(1);
    {
      const rsrc_t rdw = mk(gwd, D_ * F_ * 2);
      const u32 t = __builtin_amdgcn_raw_buffer_load_b32(rdw, tid < 392 ? (u32)((tb64 * 392 + tid) * 128) : OOB, 0, 0);
      asm volatile("" :: "v"(t));
    }
    STAMP(2);
    const u32x4 dw = srd(ws, WS_END);
    const u32 lb = (u32)(size_t)(LAS char*)lds;
    if (w == 0) poll<8>(ws, WS_F2 + (u32)(tb64 * 8) * 128, l == 0, tag);
    bar();
    poll<2>(ws, WS_F2 + (u32)(32 * w + (l & 31)) * 128, (l & 7) != 7, tag);
    bar();
#pragma unroll 1
    for (int half = 0; half < 2; ++half) {
      const int tb = 32 * half + tb64;
#pragma unroll 1
      for (int it = 0; it < 4000; ++it) {
#pragma unroll
        for (int k = 0; k < 4; ++k) if (k < 3 || l < 32) dma<SC1>(dw, lb + 32768 + w * 3584 + k * 1024, l * 16, WS_H + (u32)(tb * TB + w * 3584 + k * 1024));
        VMW(0);
        u32 m = 0;
#pragma unroll
        for (int k = 0; k < 4; ++k) m = max(m, max4(*(const u32x4*)(lds + 32768 + w * 3584 + (k < 3 ? k * 1024 + l * 16 : 3072 + (l & 31) * 16))));
        if (!__any(m == EMPTY)) break;
        __builtin_amdgcn_s_sleep(3);
      }
      float s1 = 0.f, s2 = 0.f;
#pragma unroll
      for (int i = 0; i < 7; ++i) {
        const u32x2 t = trd(lds, 32768 + (u32)((56 * w + 4 * (G >> 1) + q4) * 64 + (G & 1) * 32 + p4 * 8) + i * 512);
        s1 = dot2(t.x, ONES, s1); s2 = dot2(t.x, t.x, s2); s1 = dot2(t.y, ONES, s1); s2 = dot2(t.y, t.y, s2);
      }
      *(float*)(lds + 20480 + half * 2048 + l * 32 + w * 4) = xsum(s1, s2);
      bar();
      if (w == 0) {
        const f32x4 a = *(const f32x4*)(lds + 20480 + half * 2048 + l * 32), c = *(const f32x4*)(lds + 20480 + half * 2048 + l * 32 + 16);
        __builtin_amdgcn_raw_buffer_store_b128(__builtin_bit_cast(u32x4, ln2_terms(((a.x + a.y) + (a.z + a.w)) + ((c.x + c.y) + (c.z + c.w)))), rw, l < 32 ? WS_S2 + (u32)(tb * 512 + l * 16) : OOB, 0, SC1);
      }
    }
    STAMP(3);
    STAMP_OUT();
    return;
  }
  const int cp = xc * 28 + sc, cch = w & 1, vp = w >> 1;
  const int ech = (l >> 2) & 1;
  const u32 eo = (u32)((8 * w + r8) * TB + (2 * cp + ech) * 64 + p4 * 16);
  u32x4 fhi; u32x2 flo; u32 Dh; u64 e0;
  const u32 lb = (u32)(size_t)(LAS char*)lds;
  const u32x4 dw = srd(ws, WS_END);
  {
    const u32x4 rk = srd(gk, S_ * D_ * 2), rv = srd(gv, S_ * D_ * 2);
    const int c8 = l & 7, s0 = 64 * tb64;
    const int swv = ((r8 >> 1) & 1) | ((w & 1) << 1), swk = swv ^ 2;
    const u32 g0 = (u32)((s0 + 8 * w + r8) * 896 + role * 128);
    if (w == 0) dma(rk, lb + L_K, g0 - 8 * 896 + ((c8 ^ (swv << 1)) << 4), 0);
    if (w == 1) dma(srd(gsk, D_ * 8), lb + L_SK, (u32)(512 * role + 16 * l), 0);
    dma(rk, lb + L_K + 1024 + w * 1024, g0 + ((c8 ^ (swk << 1)) << 4), 0);
    dma(rv, lb + L_V + w * 1024, g0 + ((c8 ^ (swv << 1)) << 4), 0);
    dma(srd(gq, S_ * D_ * 2), lb + L_X + w * 1024, g0 + ((c8 ^ (swv << 1)) << 4), 0);
    const u32x4 rf = srd(gf, S_ * D_ * 2);
    const int fq = vp * 64 + l;
    const u32 fo = (u32)((2 * cp + cch) * 4096 + 16 * fq);
    asm volatile("buffer_load_dwordx4 %0, %2, %4, 0 offen\n\tbuffer_load_dwordx2 %1, %3, %4, 0 offen" : "=&v"(fhi), "=&v"(flo) : "v"(fo), "v"(fq > 0 ? fo - 8 : OOB), "s"(rf) : "memory");
    asm volatile("buffer_load_ushort %0, %1, %2, 0 offen" : "=v"(Dh) : "v"((u32)((2 * cp + ((l >> 2) & 1)) * 2)), "s"(srd(gD, D_ * 2)) : "memory");
    e0 = flag_ld(dw, WS_E);
    SB();
  }
  const int gx = (xc >> 1) * 28 + sc, gi = 8 * (xc & 1) + (gx & 7), gj = gx >> 3;
  W1 w1;
  {
    const u32 v0 = (u32)(((4 * w + G) * F_ + 128 * gj) * 2);
    w1.rg = srd(gwg, D_ * F_ * 2); w1.ru = srd(gwu, D_ * F_ * 2); w1.lb = lb + w * 1024;
    w1.vg = v0 + ((i16 ^ (4 * G)) << 4); w1.vu = v0 + ((i16 ^ (4 * G + 2)) << 4);
    w1.ra = (u32)((G & 1) * 8192 + (8 * (G >> 1) + q4) * 256 + ((w ^ (2 * q4 + (G & 1))) * 32) + p4 * 8);
  }
  w1_issue<0>(w1, L_FC); w1_issue<1>(w1, L_FC + 16384);
  VMW(9); bar();
  STAMP(1);
  {
    const int tbl = w >> 2, chg = w & 3;
    const u32 rb = (u32)((32 * tbl + 8 * G + q4) * 128 + p4 * 8);
    const u32 sv = (u32)((chg ^ ((q4 >> 1) | ((G & 1) << 1))) * 32), sk2 = sv ^ 64;
    const u32x2 km = trd(lds, L_K + rb + 512 + sv), k0 = trd(lds, L_K + rb + 1024 + sk2), k1 = trd(lds, L_K + rb + 1536 + sk2);
    const u32x2 v0 = trd(lds, L_V + rb + sv), v1 = trd(lds, L_V + rb + 512 + sv);
    const u32x2 skv = *(const u32x2*)(lds + L_SK + (16 * chg + i16) * 8);
    const float kk[11] = {bfhi(km.x), bflo(km.y), bfhi(km.y), bflo(k0.x), bfhi(k0.x), bflo(k0.y), bfhi(k0.y), bflo(k1.x), bfhi(k1.x), bflo(k1.y), bfhi(k1.y)};
    const float vv[8] = {bflo(v0.x), bfhi(v0.x), bflo(v0.y), bfhi(v0.y), bflo(v1.x), bfhi(v1.x), bflo(v1.y), bfhi(v1.y)};
    const float s0 = bflo(skv.x), s1 = bfhi(skv.x), s2 = bflo(skv.y), s3 = bfhi(skv.y);
    float u[8];
#pragma unroll
    for (int e = 0; e < 8; ++e) u[e] = (kk[e + 3] * s0 + kk[e + 2] * s1 + kk[e + 1] * s2 + kk[e] * s3) * vv[e];
    const u32 o = (u32)((2 * tb64 + tbl) * TB + (64 * role + 16 * chg + i16) * 64 + G * 16);
    __builtin_amdgcn_raw_buffer_store_b128(u32x4{pk(u[0], u[1]), pk(u[2], u[3]), pk(u[4], u[5]), pk(u[6], u[7])}, rw, WS_U + o, 0, SC1);
    SB();
    STAMP(2);
    VMW(9); bar();
    const u32x2 x0 = trd(lds, L_X + rb + sv), x1 = trd(lds, L_X + rb + 512 + sv);
    __builtin_amdgcn_raw_buffer_store_b128(cat(x0, x1), rw, WS_X + o, 0, SC1);
    VMW(6); PIN(fhi); PIN(flo); PIN(Dh); PIN(e0);
    SB();
  }
  STAMP(3);
  const u64 tag = mktag(e0);
  const float Dv = bflo(Dh);
  SB();
  w1_issue<2>(w1, L_RING); w1_issue<3>(w1, L_RING + 16384);
  STAMP(4);
  VMW(4); bar();
  if (tid == 0) setflag(ws, WS_F1 + (tb64 * 8 + role) * 128, tag);
  STAMP(5);
  const int hh = l >> 5, tk = l & 31;
  const u32 gso = WS_G + (u32)(((2 * gi * 2 + (tk >> 4)) * 56 + 4 * gj + (w >> 1)) * 1024 + (2 * (w & 1) + hh) * 256 + i16 * 16);
  auto gofs = [](int g) AI { return (u32)((32 * (g >> 1) + (g & 1)) * 2 * 56 * 1024); };
  u32x4 W[28];
  f32x4 cs0 = {}, cs1 = {};
  auto colsum = [&](int k0, int k1) AI {
#pragma unroll
    for (int k = k0; k < k1; ++k) { cs0 = mfma4(u32x2{ONES, ONES}, u32x2{W[k].x, W[k].y}, cs0); cs1 = mfma4(u32x2{ONES, ONES}, u32x2{W[k].z, W[k].w}, cs1); }
  };
  const u32 ra0 = opq(w1.ra), ra1 = opq(w1.ra + L_RING), ra2 = opq(w1.ra + L_RING + 49152);
  w1_read<0>(lds, ra0 + L_FC, W); w1_read<1>(lds, ra0 + L_FC + 16384, W);
  w1_issue<7>(w1, L_RED); w1_issue<8>(w1, L_RED + 16384);
  bar();
  {
    const u32 a0 = __builtin_amdgcn_alignbit(flo.y, flo.x, 16), a1 = __builtin_amdgcn_alignbit(fhi.x, flo.y, 16), a2 = __builtin_amdgcn_alignbit(fhi.y, fhi.x, 16),
              a3 = __builtin_amdgcn_alignbit(fhi.z, fhi.y, 16), a4 = __builtin_amdgcn_alignbit(fhi.w, fhi.z, 16);
    char* fc = lds + L_FC + cch * FCH + (vp * 64 + l) * 16 + 32;
    *(u32x4*)(fc) = fhi;
    *(u32x4*)(fc + FCS) = u32x4{a1, a2, a3, a4};
    *(u32x4*)(fc + 2 * FCS) = u32x4{flo.y, fhi.x, fhi.y, fhi.z};
    *(u32x4*)(fc + 3 * FCS) = u32x4{a0, a1, a2, a3};
    if (tid >= 128 && tid < 144) *(u32x4*)(lds + L_FC + (tid & 7) * FCS + ((tid >> 3) & 1) * 16) = u32x4{0, 0, 0, 0};
    if (tid >= 192 && tid < 252) { const int z = tid - 192; *(u32x4*)(lds + L_U + (z / 30) * UCH + (z % 30) * 16) = u32x4{0, 0, 0, 0}; }
  }
  SB();
  colsum(0, 4);
  w1_issue<4>(w1, L_RING + 32768); w1_issue<5>(w1, L_RING + 49152); w1_issue<6>(w1, L_RING + 65536);
  SB();

  STAMP(6);
  poll(ws, WS_F1 + (u32)(32 * w + 8 * (l & 3) + ((l & 4) ? 7 : (cp >> 5))) * 128, l < 8, tag);
  STAMP(7);
  u32x4 uu = ldg(dw, eo, WS_U), ex = ldg(dw, eo, WS_X);
  u32x4 est[4];
#pragma unroll
  for (int i = 0; i < 4; ++i) est[i] = ldg(dw, (u32)(r8 * 64 + p4 * 16), WS_ST + (u32)(w * 2048 + i * 512));
  bar();
  w1_read<7>(lds, ra0 + L_RED, W); w1_read<8>(lds, ra0 + L_RED + 16384, W);
  w1_read<2>(lds, ra1, W); w1_read<3>(lds, ra1 + 16384, W); w1_read<4>(lds, ra1 + 32768, W); w1_read<5>(lds, ra2, W); w1_read<6>(lds, ra2 + 16384, W);
  const u32 aA = opq(L_FC + cch * FCH + q4 * FCS + 64 - 16 * G + 8 * p4 - 8 + 128 * vp);
  const u32 aB = opq(L_U + cch * UCH + 32 * i16 + 16 * G + 448 - 128 * vp - 64);
  const u32 rwr = opq(L_RED + w * 4096 + G * 256 + ((i16 ^ ((G >> 1) << 3)) << 4));
  const u32 rr = opq(L_RED + ech * 4096 + (w & 3) * 1024 + (p4 & 1) * 512 + (((2 * r8 + (p4 >> 1)) ^ ((p4 & 1) << 3)) << 4));
  colsum(4, 18);
  PIN(cs0); PIN(cs1);
  SB();
  vmw<5>(uu);
  *(u32x4*)(lds + L_U + ech * UCH + 480 + (8 * w + r8) * 64 + p4 * 16) = uu;
  bar();
  STAMP(8);
  auto rdA = [&](int v, int m) AI { const u32 o = 64 * v + 512 * m; return cat(trd(lds, aA + o + 8), trd(lds, aA + o)); };
  auto rdB = [&](int v, int m) AI { return *(const u32x4*)(lds + aB + 64 * (1 - v) + 512 * m); };
  float ex8[8], eq8[8], eu8[8];
  auto terms = [&]() AI {
#pragma unroll
    for (int i = 0; i < 4; ++i) {
      ex8[2 * i] = bflo(ex[i]); ex8[2 * i + 1] = bfhi(ex[i]);
      eu8[2 * i] = bflo(uu[i]) * Dv; eu8[2 * i + 1] = op(bfhi(uu[i]) * Dv);
      eq8[2 * i] = (ex8[2 * i] - __uint_as_float(est[i].x)) * __uint_as_float(est[i].y);
      eq8[2 * i + 1] = op(op(ex8[2 * i + 1] - __uint_as_float(est[i].z)) * __uint_as_float(est[i].w));
    }
#pragma unroll
    for (int i = 0; i < 8; ++i) { PIN(ex8[i]); PIN(eq8[i]); PIN(eu8[i]); }
  };
  auto epilogue = [&]() AI {
    f32x4 p[8];
#pragma unroll
    for (int n = 0; n < 4; ++n) { p[2 * n] = *(const f32x4*)(lds + rr + n * 8192); p[2 * n + 1] = *(const f32x4*)(lds + rr + n * 8192 + 256); }
    float h[8];
#pragma unroll
    for (int i = 0; i < 8; ++i) {
      const float y = op(op(eu8[i] + p[i >> 2][i & 3]) + p[2 + (i >> 2)][i & 3]) + op(p[4 + (i >> 2)][i & 3] + p[6 + (i >> 2)][i & 3]);
      h[i] = op(ex8[i] + eq8[i] * op(y));
    }
    __builtin_amdgcn_raw_buffer_store_b128(u32x4{pk(h[0], h[1]), pk(h[2], h[3]), pk(h[4], h[5]), pk(h[6], h[7])}, rw, WS_H + eo, 0, SC1);
  };
  u64 fl = tag;
  const u32 f2o = WS_F2 + (u32)(64 * (w & 3) + (l & 56) + ((l & 7) == 7 ? 0 : (l & 7))) * 128;
  {
    f32x4 acc[4];
    u32x4 A[2][4], B[2][4];
#pragma unroll
    for (int v = 0; v < 2; ++v) {
      A[v][0] = rdA(v, 0); SB();
#pragma unroll
      for (int m = 0; m < 4; ++m) { B[v][m] = rdB(v, m); SB(); }
#pragma unroll
      for (int m = 1; m < 4; ++m) { A[v][m] = rdA(v, m); SB(); }
      if (v == 0) {
        vmw<0>(ex); PIN(est[0]); PIN(est[1]); PIN(est[2]); PIN(est[3]);
        __builtin_amdgcn_raw_buffer_store_b128(u32x4{EMPTY, EMPTY, EMPTY, EMPTY}, rw, eo, WS_H, SC1);
        if (w < 4) terms();
        SB();
      }
    }
#pragma unroll
    for (int v = 0; v < 2; ++v)
#pragma unroll
      for (int m = 0; m < 4; ++m) {
#pragma unroll
        for (int c = m; c < 4; ++c) acc[c] = mfma16(A[v][m], B[v][c - m], (v == 0 && m == 0) ? f32x4{} : acc[c]);
        SB();
        if (v == 0 && m == 0) w1_g<9>(w1, L_RING);
        if (v == 0 && m == 2) w1_u<9>(w1, L_RING);
        if (v == 1 && m == 0) w1_g<10>(w1, L_RING + 16384);
        SB();
      }
#pragma unroll
    for (int c = 0; c < 4; ++c) *(f32x4*)(lds + rwr + c * 1024) = acc[c];
  }
  VMW(3); bar();
  if (tid == 256) setflag(ws, WS_F2 + (tb64 * 8 + role) * 128, tag);
  STAMP(9);
  if (w < 4) epilogue();
  SB();
  STAMP(10);
  {
    f32x4 acc[4];
    u32x4 A[2][8], B[2][8];
    auto rd = [&](int v, int m) AI {
      if (m == 0) {
        A[v][0] = rdA(v, 0);
#pragma unroll
        for (int d = 4; d < 8; ++d) B[v][d] = rdB(v, d);
      } else { A[v][m] = rdA(v, m); if (m <= 4) B[v][4 - m] = rdB(v, 4 - m); }
      SB();
    };
    rd(0, 0); rd(0, 1); rd(0, 2); rd(0, 3);
#pragma unroll
    for (int v = 0; v < 2; ++v) {
#pragma unroll
      for (int m = 0; m < 8; ++m) {
#pragma unroll
        for (int c = 4; c < 8; ++c) if (m <= c) acc[c - 4] = mfma16(A[v][m], B[v][c - m], (v == 0 && m == 0) ? f32x4{} : acc[c - 4]);
        SB();
        if (m < 4) rd(v, m + 4); else if (v == 0) rd(1, m - 4);
        if (v == 1 && m == 0 && w < 4) fl = flag_ld(dw, f2o);
        if (v == 0) {
          if (m == 0) w1_u<10>(w1, L_RING + 16384);
          if (m == 1) w1_g<11>(w1, L_RING + 32768);
          if (m == 2) w1_u<11>(w1, L_RING + 32768);
          if (m == 3) w1_g<12>(w1, L_RING + 49152);
          if (m == 4) w1_u<12>(w1, L_RING + 49152);
          if (m == 6) w1_g<13>(w1, L_RING + 65536);
          if (m == 7) w1_u<13>(w1, L_RING + 65536);
        }
        SB();
      }
    }
    if (w >= 4) terms();
    STAMP(11);
    VMWP(0, fl);
    if (!__all(fl == tag)) poll<2>(ws, f2o, true, tag);
    bar();
#pragma unroll
    for (int c = 0; c < 4; ++c) *(f32x4*)(lds + rwr + c * 1024) = acc[c];
  }
  const u32 lo16 = l * 16;
  if (w < 4) {
#pragma unroll
    for (int k = 0; k < 7; ++k) dma<SC1>(dw, lb + L_H0 + (7 * w + k) * 1024, lo16, WS_H + (u32)(2 * gi * TB + (7 * w + k) * 1024));
  }
  w1_read<9>(lds, ra1, W); w1_read<10>(lds, ra1 + 16384, W); w1_read<11>(lds, ra1 + 32768, W); w1_read<12>(lds, ra2, W); w1_read<13>(lds, ra2 + 16384, W);
  bar();
  STAMP(12);

  const u32 bta = (u32)((8 * (G >> 1) + q4) * 64 + (G & 1) * 32 + p4 * 8);
  auto hbase = [](int g) AI { return g == 0 ? L_H0 : g == 1 ? L_H1 : g == 2 ? L_H2 : L_H3; };
  u32 csh, csl;
  auto colfin = [&]() AI {
    colsum(18, 28);
    const float t = hsum(cs0.x + cs1.x);
    const u32 hi = pk(t, t);
    csh = hh ? 0u : hi; csl = hh ? 0u : pk(t - bflo(hi), 0.f);
    PIN(csh); PIN(csl);
  };
  f32x4 tm; float gu[8], gt[8]; u32 od[4];
  auto center = [&](f32x16& acc, float m) AI {
    const u32 d = __float_as_uint(m);
    acc = mfma32(u32x4{csh, csl, 0, 0}, u32x4{d, d & 0xffffu, 0, 0}, acc);
  };
  auto geglu_step = [&](int g, f32x16& acc, int i) AI {
#pragma unroll
    for (int e = 0; e < 8; ++e) {
      const int s = i - (2 + (18 * e) / 7);
      if (s == 0) { gt[e] = acc[e] * acc[e]; gu[e] = op(acc[e] * acc[e + 8]); PIN(gt[e]); PIN(gu[e]); }
      if (s == 1) { gt[e] = tm.w * gt[e] + tm.z; PIN(gt[e]); }
      if (s == 2) { gt[e] = acc[e] * gt[e]; PIN(gt[e]); }
      if (s == 3) { gt[e] = __builtin_amdgcn_exp2f(gt[e]); PIN(gt[e]); }
      if (s == 4) { gt[e] = gt[e] * tm.y + tm.y; PIN(gt[e]); }
      if (s == 5) { gt[e] = __builtin_amdgcn_rcpf(gt[e]); PIN(gt[e]); }
      if (s == 6) { gu[e] = gu[e] * gt[e]; PIN(gu[e]); }
      if (s == 7 && (e & 1)) { od[e >> 1] = pk(gu[e - 1], gu[e]); PIN(od[e >> 1]); }
    }
    if (i == 28) {
      __builtin_amdgcn_raw_buffer_store_b128(u32x4{od[0], od[1], od[2], od[3]}, rw, gso, gofs(g), SC1);
    }
  };
  constexpr int PF = 4;
  u32x4 B[PF];
  u32 ba = opq(hbase(0) + bta);
  auto chain = [&](int g, f32x16& acc, auto side) AI {
    const u32 bn = opq(hbase(g < 3 ? g + 1 : g) + bta);
    sfor<28>([&](auto kc) AI {
      constexpr int k = decltype(kc)::value;
      acc = mfma32(W[k], B[k % PF], k == 0 ? f32x16{} : acc);
      SB();
      side(k);
      if (k + PF < 28) B[k % PF] = cat(trd(lds, ba + (k + PF) * 1024), trd(lds, ba + (k + PF) * 1024 + 256));
      else if (g < 3) B[k % PF] = cat(trd(lds, bn + (k + PF - 28) * 1024), trd(lds, bn + (k + PF - 28) * 1024 + 256));
      SB();
    });
    ba = bn;
  };
  auto flag3 = [&](int g) AI { setflag(ws, WS_F3 + (u32)(((gi * 4 + g) * 14) + gj) * 128, tag); };
  const int ri = 2 * xc + sc / 14, rq2 = (sc / 7) & 1, n2 = sc % 7;
  const u32x4 rd = srd(gwd, D_ * F_ * 2);
  const u32 wdv = (u32)(((224 * w + r8) * D_ + 64 * n2) * 2 + (((l & 7) ^ (((r8 >> 1) & 3) << 1)) << 4));
  const u32 blkR = w < 7 ? L_R + w * 4096 : L_R2;
  u32 wda[4], wdr[4];
#pragma unroll
  for (int nt = 0; nt < 4; ++nt) {
    const u32 a = (16 * (G >> 1) + 4 * (G & 1) + q4) * 128 + ((nt ^ (2 * (G & 1) + (q4 >> 1))) * 32) + p4 * 8;
    wda[nt] = opq(a + L_P + w * 4096); wdr[nt] = opq(a + blkR);
  }
  u32x4 Wd[7][4];
  auto wblk = [](int k) AI { return (k == 1 || k == 4) ? 1 : (k == 3 || k == 6) ? 2 : 0; };
  auto wd_dma = [&](int k, int i) AI {
    if (i == 0) dma_at(lb + (wblk(k) == 0 ? blkR : (wblk(k) == 2 ? L_Q : L_P) + w * 4096));
    dma_i<0>(i, rd, wdv, 4 * k * (8 * D_ * 2) + i * (8 * D_ * 2 - 1024));
  };
  auto wd_rd = [&](int k, int nt) AI {
    const u32 a = wblk(k) == 0 ? wdr[nt] : wda[nt] + (wblk(k) == 2 ? L_Q - L_P : 0);
    Wd[k][nt] = cat(trd(lds, a), trd(lds, a + 1024));
  };
  auto wd_read = [&](int k) AI { wd_rd(k, 0); wd_rd(k, 1); wd_rd(k, 2); wd_rd(k, 3); };
  const int j0 = (14 * w) >> 3, jn = ((14 * w + 13) >> 3) - j0;
  const u32 f3o = WS_F3 + (u32)(ri * 56 + j0 + ((l & 3) < jn ? (l & 3) : jn)) * 128;

  f32x16 accA, accB;
  u32x4 tm0, tm1, tm2, tm3;
  const u32 s2o = WS_S2 + (u32)(2 * gi * 512 + tk * 16);
  const u32 hp = (u32)(w < 4 ? 4 * w : 4 + 3 * w) * 1024;
  auto htb = [&](int g) AI { return WS_H + (u32)((32 * (g >> 1) + 2 * gi + (g & 1)) * TB); };
  auto h_dma = [&](int g, int i) AI {
    if (i == 0) dma_at(lb + hbase(g) + hp);
    if (i < 3 || w < 4) dma_i<SC1>(i, dw, lo16, htb(g) + hp);
  };
  auto redo1 = [&](int g, f32x16& acc, u32 m) AI {
    if (__any(acc[0] != acc[0])) {
      REDO_COUNT();
      const u32 sc0 = L_SP + w * 1024;
      acc = f32x16{};
      const rsrc_t rg = mk(gwg, D_ * F_ * 2), ru = mk(gwu, D_ * F_ * 2);
#pragma unroll 1
      for (int k = 0; k < 28; ++k) {
        const u32 wo = (u32)(((16 * k + ((l >> 1) & 15)) * F_ + 128 * gj + 16 * w) * 2 + (l & 1) * 16);
        const u32x4 wg = __builtin_amdgcn_raw_buffer_load_b128(rg, wo, 0, 0), wu = __builtin_amdgcn_raw_buffer_load_b128(ru, wo, 0, 0);
        *(u32x4*)(lds + sc0 + lo16) = l < 32 ? wg : wu;
        const u32 wa = sc0 + (G & 1) * 512 + (8 * (G >> 1) + q4) * 32 + p4 * 8;
        const u32x4 a = cat(trd(lds, wa), trd(lds, wa + 128));
        u32x4 v;
#pragma unroll 1
        for (int it = 0; it < 400; ++it) { v = ldg(dw, lo16, htb(g) + k * 1024); vmw<0>(v); if (!__any(max4(v) == EMPTY)) break; }
        *(u32x4*)(lds + sc0 + lo16) = v;
        acc = mfma32(a, cat(trd(lds, sc0 + bta), trd(lds, sc0 + bta + 256)), acc);
      }
      center(acc, __uint_as_float(m));
    }
  };
  auto tm_fix = [&](u32x4& t, u32 so) AI {
#pragma unroll 1
    for (int it = 0; it < 4000 && __any(max4(t) == EMPTY); ++it) { t = ldg(dw, s2o, so); vmw<0>(t); }
  };
  SB();
  if (w >= 4) {
    epilogue();
    SB();
    colfin();
  } else {
    colfin();
    SB();
    const u32 ha = opq(L_H0 + 7 * w * 1024 + lo16);
#pragma unroll 1
    for (int it = 0; it < 4000; ++it) {
      VMW(0);
      u32 m = 0;
#pragma unroll
      for (int k = 0; k < 7; ++k) { const u32x4 v = *(const u32x4*)(lds + ha + k * 1024); m = max(m, max4(v)); }
      if (!__any(m == EMPTY)) break;
#pragma unroll
      for (int k = 0; k < 7; ++k) dma<SC1>(dw, lb + L_H0 + (7 * w + k) * 1024, lo16, WS_H + (u32)(2 * gi * TB + (7 * w + k) * 1024));
    }
    VMW(0);
    STAMP(13);
  }
  __builtin_amdgcn_s_setprio(1);
  bar();
  STAMP(14);
#pragma unroll
  for (int k = 0; k < PF; ++k) B[k] = cat(trd(lds, ba + k * 1024), trd(lds, ba + k * 1024 + 256));
  SB();
  chain(0, accA, [&](int k) AI {
    if (k <= 6 && !(k & 1)) h_dma(1, k >> 1);
    if (k == 8) tm0 = ldg(dw, s2o, 0);
    if (k == 10 || k == 13) __builtin_amdgcn_raw_buffer_store_b128(u32x4{EMPTY, EMPTY, EMPTY, EMPTY}, rw, gso, gofs(k == 10 ? 2 : 3), SC1);
    if (k >= 16 && k <= 25 && k % 3 == 1) wd_dma(0, (k - 16) / 3);
    if (k == 23) { VMW(6); RBAR(); if (tid == 0 && b == 0) setflag(ws, WS_E, tag); }
    if (k == 26) { vmw<6>(tm0); tm_fix(tm0, 0); }
  });
  center(accA, __uint_as_float(tm0.x));
  tm = __builtin_bit_cast(f32x4, tm0);
  SB();
  STAMP(16);
  chain(1, accB, [&](int k) AI {
    if (k == 1) RBAR();
    geglu_step(0, accA, k);
    if (k >= 1 && k <= 7 && (k & 1)) h_dma(2, k >> 1);
    if (k == 8) tm1 = ldg(dw, s2o, 512);
    if (k >= 9 && k <= 15 && (k & 1)) wd_dma(1, (k - 9) >> 1);
    if (k == 12) VMW(6);
    if (k >= 12 && k <= 18 && !(k & 1)) wd_rd(0, (k - 12) >> 1);
    if (k >= 17 && k <= 23 && (k & 1)) h_dma(3, (k - 17) >> 1);
    if (k == 22) { vmw<7>(tm1); RBAR(); }
    if (k == 24) tm_fix(tm1, 512);
    if (k == 25 || k == 27) wd_dma(2, (k - 25) >> 1);
  });
  center(accB, __uint_as_float(tm1.x));
  geglu_step(0, accA, 28);
  tm = __builtin_bit_cast(f32x4, tm1);
  SB();
  STAMP(18);
  chain(2, accA, [&](int k) AI {
    if (k == 1) RBAR();
    if (k == 2) redo1(1, accB, tm1.x);
    geglu_step(1, accB, k);
    if (k == 1 || k == 3) wd_dma(2, 2 + (k >> 1));
    if (k == 4) VMW(8);
    if (k >= 4 && k <= 10 && !(k & 1)) wd_rd(1, (k - 4) >> 1);
    if (k == 5) { tm2 = ldg(dw, s2o, 32 * 512); tm3 = ldg(dw, s2o, 33 * 512); }
    if (k >= 7 && k <= 13 && (k & 1)) wd_dma(3, (k - 7) >> 1);
    if (k >= 16 && k <= 25 && k % 3 == 1) wd_dma(4, (k - 16) / 3);
    if (k == 23) { vmw<7>(tm2); PIN(tm3); RBAR(); if (tid == 0) flag3(0); }
    if (k == 24) tm_fix(tm2, 32 * 512);
    if (k == 26) tm_fix(tm3, 33 * 512);
  });
  center(accA, __uint_as_float(tm2.x));
  geglu_step(1, accB, 28);
  tm = __builtin_bit_cast(f32x4, tm2);
  SB();
  STAMP(20);
  const u32 gb = WS_G + (u32)((2 * ri * 2 + rq2) * 56 + 7 * w) * 1024;
  constexpr int NS = 9;
  u32x4 Hf[NS];
  auto hl = [&](int t) AI { Hf[t % NS] = ldg(dw, lo16, gb + gofs(t / 7) + (t % 7) * 1024); };
  chain(3, accB, [&](int k) AI {
    if (k == 2) redo1(2, accA, tm2.x);
    geglu_step(2, accA, k);
    if (k >= 5 && k < 13) { if (k == 5) VMW(5); wd_rd(2 + (k - 5) / 4, (k - 5) & 3); }
    if (k == 10) fl = flag_ld(dw, f3o);
    if (k >= 12 && k <= 18 && !(k & 1)) wd_dma(5, (k - 12) >> 1);
    if (k == 13) { VMW(2); RBAR(); if (tid == 0) flag3(1); }
    if (k >= 13 && k < 21 && (k & 1)) wd_rd(4, (k - 13) >> 1);
    if (k >= 19 && k <= 22) wd_dma(6, k - 19);
    if (k == 23) { VMWP(8, fl); if (!__all(fl == tag)) poll(ws, f3o, true, tag); }
    if (k >= 23) hl(k - 23);
  });
  center(accB, __uint_as_float(tm3.x));
  geglu_step(2, accA, 28);
  tm = __builtin_bit_cast(f32x4, tm3);
  redo1(3, accB, tm3.x);
  STAMP(21);
#pragma unroll
  for (int e = 0; e < 8; ++e) {
    const float a = accB[e], u = accB[e + 8];
    gu[e] = (a * u) * __builtin_amdgcn_rcpf(__builtin_amdgcn_exp2f(a * (tm.w * (a * a) + tm.z)) * tm.y + tm.y);
  }
#pragma unroll
  for (int e = 0; e < 4; ++e) od[e] = pk(gu[2 * e], gu[2 * e + 1]);
  geglu_step(3, accB, 28);
  __builtin_amdgcn_s_setprio(0);
  SB();
  STAMP(22);
  f32x4 acc[2][4];
  const rsrc_t ro = mk(gout, S_ * D_ * 2);
  const u32 pw = (u32)(i16 * 256), tf = 4 * (w & 3) + G;
  const u32 hro = WS_H + (u32)((32 * (G >> 1) + (G & 1) + 2 * ri) * TB + (64 * n2 + 8 * w + (i16 >> 1)) * 64 + rq2 * 32 + (i16 & 1) * 16);
  auto pbuf = [](int g) AI { return g == 3 ? L_T : g == 1 ? L_Q : L_P; };
  auto put = [&](int g) AI {
    const u32 rb = opq(pbuf(g) + w * 4096 + pw);
#pragma unroll
    for (int nt = 0; nt < 4; ++nt) *(f32x4*)(lds + rb + (((4 * nt + G) ^ (i16 & 7)) << 4)) = acc[g & 1][nt];
  };
  auto fin = [&](int g) AI {
    if ((w >> 2) == (g & 1)) {
      const u32 a0 = tf * 256 + ((i16 ^ (tf & 7)) << 4), a = opq(pbuf(g) + a0);
      f32x4 p[9];
      p[8] = *(const f32x4*)(lds + L_RES + g * 4096 + a0);
#pragma unroll
      for (int n = 0; n < 8; ++n) p[n] = *(const f32x4*)(lds + a + n * 4096);
      const f32x4 sum = (((p[0] + p[1]) + (p[2] + p[3])) + ((p[4] + p[5]) + (p[6] + p[7]))) + p[8];
      const int tok = 32 * (32 * (g >> 1) + (g & 1) + 2 * ri) + 16 * rq2 + tf;
      __builtin_amdgcn_raw_buffer_store_b64(u32x2{pk(sum.x, sum.y), pk(sum.z, sum.w)}, ro, (u32)((tok * D_ + 64 * n2 + 4 * i16) * 2), 0, 0);
    }
  };
  auto redo = [&](int g) AI {
    if (__any(acc[g & 1][0].x != acc[g & 1][0].x)) {
      REDO_COUNT();
      u32x4 v[7];
#pragma unroll 1
      for (int it = 0; it < 4000; ++it) {
        u32 m = 0;
#pragma unroll
        for (int k = 0; k < 7; ++k) v[k] = ldg(dw, lo16, gb + gofs(g) + k * 1024);
#pragma unroll
        for (int k = 0; k < 7; ++k) { vmw<0>(v[k]); m = max(m, max4(v[k])); }
        if (!__any(m == EMPTY)) break;
      }
#pragma unroll
      for (int k = 0; k < 7; ++k)
#pragma unroll
        for (int nt = 0; nt < 4; ++nt) acc[g & 1][nt] = mfma16(Wd[k][nt], v[k], k == 0 ? f32x4{} : acc[g & 1][nt]);
    }
  };
  fl = flag_ld(dw, f3o + 14 * 128);
  u32x4 hr = ldg(dw, hro, 0);
  hl(5); hl(6);
  VMW(11);
  wd_read(5); wd_read(6);
  SB();
  STAMP(23);
  sfor<28>([&](auto tc) AI {
    constexpr int t = decltype(tc)::value, g = t / 7, k = t % 7;
    constexpr auto pos = [](int h) { return h < 5 ? h : h + 4; };
    constexpr auto last = [](int u) { return u < 2 ? 6 : u == 2 ? 8 : u == 3 ? 10 : u < 21 ? u + 7 : 27; };
    if (t == 2) { VMWP(3, fl); if (!__all(fl == tag)) poll(ws, f3o + 14 * 128, true, tag); }
#pragma unroll
    for (int h = (t ? last(t - 1) : 6) + 1; h <= last(t); ++h) hl(h);
    vmw<pos(last(t)) - pos(t)>(Hf[t % NS]);
#pragma unroll
    for (int nt = 0; nt < 4; ++nt) acc[g & 1][nt] = mfma16(Wd[k][nt], Hf[t % NS], k == 0 ? f32x4{} : acc[g & 1][nt]);
    SB();
    if (t == 3) {
      vmw<6>(hr);
#pragma unroll 1
      for (int it = 0; it < 4000 && __any(max4(hr) == EMPTY); ++it) { hr = ldg(dw, hro, 0); vmw<0>(hr); }
      *(u32x4*)(lds + blkR + lo16) = hr;
#pragma unroll
      for (int q = 0; q < 4; ++q) {
        const u32x2 r = trd(lds, blkR + q * 256 + (4 * (G & 1) + q4) * 32 + p4 * 8);
        *(f32x4*)(lds + L_RES + q * 4096 + pw + (((2 * w + (G & 1)) ^ (i16 & 7)) << 4)) = f32x4{bflo(r.x), bfhi(r.x), bflo(r.y), bfhi(r.y)};
      }
    }
    if (t == 8) put(0);
    if (t == 12) { bar(); STAMP(24); fin(0); }
    if (t == 15) put(1);
    if (t == 19) { bar(); STAMP(25); fin(1); }
    if (t == 22) { redo(2); put(2); STAMP(26); }
    SB();
  });
  redo(3);
  put(3);
  bar();
  STAMP(27);
  fin(2); fin(3);
  STAMP(28);
  STAMP_OUT();
}

}

extern "C" void kernel_launch(void* const* d_in, const int* in_sizes, int n_in, void* d_out, int out_size, void* d_ws, size_t ws_size,
                              hipStream_t stream) {
  h3_ffn<<<256, 512, 0, stream>>>((const char*)d_in[0], (const char*)d_in[1], (const char*)d_in[2], (const char*)d_in[3], (const char*)d_in[4],
                                  (const char*)d_in[5], (const char*)d_in[7], (const char*)d_in[8], (const char*)d_in[9], (char*)d_out, (char*)d_ws);
}
```

```cpp
#include <hip/hip_runtime.h>
#include <stdint.h>

namespace {

#define LAS __attribute__((address_space(3)))
#define GAS __attribute__((address_space(1)))
#define DI __device__ __forceinline__
#define AI __attribute__((always_inline))
#define RARE(x) __builtin_expect(!!(x), 0)
typedef uint32_t u32;
typedef unsigned long long u64;
typedef u32 u32x2 __attribute__((ext_vector_type(2)));
typedef u32 u32x4 __attribute__((ext_vector_type(4)));
typedef short s16x4 __attribute__((ext_vector_type(4)));
typedef short s16x8 __attribute__((ext_vector_type(8)));
typedef float f32x4 __attribute__((ext_vector_type(4)));
typedef float f32x16 __attribute__((ext_vector_type(16)));
typedef __bf16 bf2 __attribute__((ext_vector_type(2)));
typedef __amdgpu_buffer_rsrc_t rsrc_t;
#define RLX_AGENT __ATOMIC_RELAXED, __HIP_MEMORY_SCOPE_AGENT

constexpr int S_ = 2048, D_ = 448, F_ = 1792;
constexpr u32 TB = 448 * 64;
constexpr u32 WS_E  = 0;
constexpr u32 WS_F1 = 128;
constexpr u32 WS_F3 = WS_F1 + 256 * 128;
constexpr u32 WS_F2 = WS_F3 + 896 * 128;
constexpr u32 WS_S2 = 256 * 1024 + 16384;
constexpr u32 WS_ST = 256 * 1024;
constexpr u32 WS_U  = WS_S2 + 32768;
constexpr u32 WS_X  = WS_U + 64 * TB;
constexpr u32 WS_H  = WS_X + 64 * TB;
constexpr u32 WS_G  = WS_H + 64 * TB;
constexpr u32 WS_END = WS_G + 64 * 112 * 1024;
constexpr u32 WS_DBG = 16u << 20;
constexpr int SC1 = 16;
#define HELP_DELAY 8
#define TM0K 8
#define ROT1 0
#define PRETOUCH 0
constexpr u32 OOB = 0xFFFFFF00u;
constexpr u32 ONES = 0x3f803f80u;
constexpr u32 EMPTY = 0xffffffffu;

constexpr u32 L_K = 0, L_V = 9216, L_X = 17408, L_SK = 25600;
constexpr u32 L_RED = 0, L_FC = 32768, FCS = 4160, FCH = 4 * FCS;
constexpr u32 L_U = L_FC + 2 * FCH, UCH = 4608, L_RING = L_U + 2 * UCH;
constexpr u32 L_H0 = 32768, L_H1 = L_H0 + TB, L_H2 = 0, L_H3 = L_H1 + TB + 32768, L_SP = 155648;
constexpr u32 L_P = TB, L_Q = L_P + 32768, L_R = L_Q + 32768, L_R2 = L_H3 + TB;
constexpr u32 L_RES = 0, L_T = L_H3;
constexpr u32 LDS_BYTES = 163840;

DI float bflo(u32 x) { return __uint_as_float(x << 16); }
DI float bfhi(u32 x) { return __uint_as_float(x & 0xffff0000u); }
DI u32 pk(float lo, float hi) { bf2 p; p.x = (__bf16)lo; p.y = (__bf16)hi; return __builtin_bit_cast(u32, p); }
DI rsrc_t mk(const void* p, u32 bytes) { return __builtin_amdgcn_make_buffer_rsrc((void*)p, 0, bytes, 0x00020000); }
DI u32x2 trd(const char* lds, u32 a) { return __builtin_bit_cast(u32x2, __builtin_amdgcn_ds_read_tr16_b64_v4i16((LAS s16x4*)(lds + a))); }
DI u32x4 cat(u32x2 a, u32x2 b) { return u32x4{a.x, a.y, b.x, b.y}; }
DI u32x4 srd(const void* p, u32 bytes) { const u64 a = (u64)p; return u32x4{(u32)a, (u32)(a >> 32) & 0xffffu, bytes, 0x00020000u}; }
template <int AUX = 0> DI void dma(u32x4 r, u32 ldsoff, u32 voff, u32 soff) {
  if (AUX) asm volatile("s_mov_b32 m0, %0\n\ts_nop 0\n\tbuffer_load_dwordx4 %1, %2, %3 offen sc1 lds" :: "s"(ldsoff), "v"(voff), "s"(r), "s"(soff) : "memory");
  else asm volatile("s_mov_b32 m0, %0\n\ts_nop 0\n\tbuffer_load_dwordx4 %1, %2, %3 offen lds" :: "s"(ldsoff), "v"(voff), "s"(r), "s"(soff) : "memory");
}
DI void dma_at(u32 ldsoff) { asm volatile("s_mov_b32 m0, %0\n\ts_nop 0" :: "s"(ldsoff) : "memory"); }
template <int OFF, int AUX> DI void dma_go(u32x4 r, u32 voff, u32 soff) {
  if (AUX) asm volatile("buffer_load_dwordx4 %0, %1, %2 offen offset:%3 sc1 lds" :: "v"(voff), "s"(r), "s"(soff), "n"(OFF) : "memory");
  else asm volatile("buffer_load_dwordx4 %0, %1, %2 offen offset:%3 lds" :: "v"(voff), "s"(r), "s"(soff), "n"(OFF) : "memory");
}
template <int AUX> DI void dma_i(int i, u32x4 r, u32 voff, u32 soff) {
  if (i == 0) dma_go<0, AUX>(r, voff, soff); else if (i == 1) dma_go<1024, AUX>(r, voff, soff); else if (i == 2) dma_go<2048, AUX>(r, voff, soff); else dma_go<3072, AUX>(r, voff, soff);
}
DI float fnma(float a, float b, float c) { float r; asm("v_fma_f32 %0, -%1, %2, %3" : "=v"(r) : "v"(a), "v"(b), "v"(c)); return r; }
DI float op(float x) { asm("" : "+v"(x)); return x; }
DI u32 opq(u32 x) { asm volatile("" : "+v"(x)); return x; }
#define PIN(x) asm volatile("" : "+v"(x))
DI f32x4 mfma16(u32x4 a, u32x4 b, f32x4 c) { return __builtin_amdgcn_mfma_f32_16x16x32_bf16(__builtin_bit_cast(s16x8, a), __builtin_bit_cast(s16x8, b), c, 0, 0, 0); }
DI f32x16 mfma32(u32x4 a, u32x4 b, f32x16 c) { return __builtin_amdgcn_mfma_f32_32x32x16_bf16(__builtin_bit_cast(s16x8, a), __builtin_bit_cast(s16x8, b), c, 0, 0, 0); }
DI f32x4 mfma4(u32x2 a, u32x2 b, f32x4 c) { return __builtin_amdgcn_mfma_f32_4x4x4bf16_1k(__builtin_bit_cast(s16x4, a), __builtin_bit_cast(s16x4, b), c, 0, 0, 0); }
DI float dot2(u32 a, u32 b, float c) { return __builtin_amdgcn_fdot2_f32_bf16(__builtin_bit_cast(bf2, a), __builtin_bit_cast(bf2, b), c, false); }
#define VMW(n) asm volatile("s_waitcnt vmcnt(%0)" :: "n"(n) : "memory")
#define LG0() asm volatile("s_waitcnt lgkmcnt(0)" ::: "memory")
#define SB() __builtin_amdgcn_sched_barrier(0)
DI void bar() { asm volatile("s_waitcnt lgkmcnt(0)\n\ts_barrier" ::: "memory"); }
#define RBAR() asm volatile("s_barrier" ::: "memory")
DI float wsum(float v) {
  v += __builtin_amdgcn_update_dpp(0.f, v, 0x111, 0xf, 0xf, true);
  v += __builtin_amdgcn_update_dpp(0.f, v, 0x112, 0xf, 0xf, true);
  v += __builtin_amdgcn_update_dpp(0.f, v, 0x114, 0xf, 0xf, true);
  v += __builtin_amdgcn_update_dpp(0.f, v, 0x118, 0xf, 0xf, true);
  v += __builtin_amdgcn_update_dpp(0.f, v, 0x142, 0xa, 0xf, true);
  v += __builtin_amdgcn_update_dpp(0.f, v, 0x143, 0xc, 0xf, true);
  return __uint_as_float(__builtin_amdgcn_readlane(__float_as_uint(v), 63));
}
DI float hsum(float v) {
  const auto r = __builtin_amdgcn_permlane32_swap(__float_as_uint(v), __float_as_uint(v), false, false);
  return __uint_as_float(r[0]) + __uint_as_float(r[1]);
}
template <int SLEEP = 1> DI void poll(char* ws, u32 off, bool act, u64 tag) {
  bool ok = !act;
#pragma unroll 1
  for (int it = 0; it < 20000; ++it) {
    u64 x = tag;
    if (!ok) x = __hip_atomic_load((GAS u64*)(ws + off), RLX_AGENT);
    ok = (x == tag);
    if (__all(ok)) break;
    __builtin_amdgcn_s_sleep(SLEEP);
  }
  asm volatile("" ::: "memory");
}
DI u32 max4(u32x4 v) { return max(max(v.x, v.y), max(v.z, v.w)); }
DI u64 flag_ld(u32x4 r, u32 voff) { u64 v; asm volatile("buffer_load_dwordx2 %0, %1, %2, 0 offen sc1" : "=v"(v) : "v"(voff), "s"(r) : "memory"); return v; }
DI u32x4 ldg(u32x4 r, u32 voff, u32 soff) { u32x4 v; asm volatile("buffer_load_dwordx4 %0, %1, %2, %3 offen sc1" : "=v"(v) : "v"(voff), "s"(r), "s"(soff) : "memory"); return v; }
template <int N> DI void vmw(u32x4& x) { asm volatile("s_waitcnt vmcnt(%1)" : "+v"(x) : "n"(N) : "memory"); }
#define VMWP(n, x) asm volatile("s_waitcnt vmcnt(" #n ")" : "+v"(x) :: "memory")
DI void setflag(char* ws, u32 off, u64 tag) { __hip_atomic_store((GAS u64*)(ws + off), tag, RLX_AGENT); }

#define STAMP(i) do { if ((i) == 0) t_0 = __builtin_amdgcn_s_memrealtime(); if ((i) == 14) t_j = __builtin_amdgcn_s_memrealtime(); } while (0)
#define STAMPC(i)
#define REDO_COUNT()
#define FIX_COUNT()
#define STAMP_OUT() do { if (b == 9 && w == 0) { const u64 te = __builtin_amdgcn_s_memrealtime() + 8 * (t_j - t_0); while (__builtin_amdgcn_s_memrealtime() < te) __builtin_amdgcn_s_sleep(1); } } while (0)

DI float xsum(float a, float c) {
  const auto r = __builtin_amdgcn_permlane32_swap(__float_as_uint(a), __float_as_uint(c), false, false);
  return __uint_as_float(r[0]) + __uint_as_float(r[1]);
}
DI f32x4 ln2_terms(float t) {
  const auto r = __builtin_amdgcn_permlane32_swap(__float_as_uint(t), __float_as_uint(t), false, false);
  const float mean = __uint_as_float(r[0]) * (1.f / D_);
  const float V = fmaxf(__uint_as_float(r[1]) * (1.f / D_) - mean * mean, 0.f) + 1e-5f;
  const float rs = __builtin_amdgcn_rsqf(V);
  const float K1 = -2.f * 0.7978845608028654f * 1.4426950408889634f * rs;
  const float lo = -mean - bflo(pk(-mean, 0.f));
  return f32x4{__uint_as_float(pk(-mean, lo)), V, K1, (K1 * rs) * (0.044715f * rs)};
}
template <int N> struct IC { static constexpr int value = N; };
template <int N, int I = 0, class F> DI void sfor(F&& f) { if constexpr (I < N) { f(IC<I>{}); sfor<N, I + 1>(f); } }
struct W1 { u32x4 rg, ru; u32 vg, vu, ra, lb, rot; };
DI u32 wrap(u32 x, u32 n) { return x >= n ? x - n : x; }
template <int T> DI void w1_g(const W1& c, u32 base) { dma(c.rg, c.lb + base, c.vg, wrap(T + c.rot, 14) * (32 * F_ * 2)); }
template <int T> DI void w1_u(const W1& c, u32 base) { dma(c.ru, c.lb + base + 8192, c.vu, wrap(T + c.rot, 14) * (32 * F_ * 2)); }
template <int T> DI void w1_issue(const W1& c, u32 base) { w1_g<T>(c, base); w1_u<T>(c, base); }
template <int T> DI void w1_read(const char* lds, u32 a, u32x4 (&W)[28]) {
#pragma unroll
  for (int kk = 0; kk < 2; ++kk) W[2 * T + kk] = cat(trd(lds, a + kk * 4096), trd(lds, a + kk * 4096 + 1024));
}
DI float x8sum(float v) {
  v += __builtin_amdgcn_update_dpp(0.f, v, 0xb1, 0xf, 0xf, true);
  v += __builtin_amdgcn_update_dpp(0.f, v, 0x4e, 0xf, 0xf, true);
  v += __builtin_amdgcn_update_dpp(0.f, v, 0x141, 0xf, 0xf, true);
  return v;
}

__global__ __launch_bounds__(512, 2) void h3_ffn(const char* __restrict__ gq, const char* __restrict__ gk, const char* __restrict__ gv,
                                                 const char* __restrict__ gsk, const char* __restrict__ gf, const char* __restrict__ gD,
                                                 const char* __restrict__ gwg, const char* __restrict__ gwu, const char* __restrict__ gwd,
                                                 char* __restrict__ gout, char* ws) {
  __shared__ __attribute__((aligned(16))) char lds[LDS_BYTES];
  const int tid = threadIdx.x, l = tid & 63, w = __builtin_amdgcn_readfirstlane(tid >> 6);
  const int b = blockIdx.x, xc = b & 7, sl = b >> 3;
  const int tb64 = 4 * xc + (sl >> 3), role = sl & 7;
  const int sc = (sl >> 3) * 7 + role;
  u64 t_0 = 0, t_j = 0;
  asm volatile("" :: "s"(gq), "s"(gk), "s"(gv), "s"(gsk), "s"(gf), "s"(gD), "s"(gwg), "s"(gwu), "s"(gwd), "s"(gout), "s"(ws));
  STAMP(0);
  const rsrc_t rw = mk(ws, WS_END);
  const int G = l >> 4, i16 = l & 15, q4 = i16 >> 2, p4 = l & 3, r8 = l >> 3;
  auto mktag = [&](u64 e0) AI { return ((u64)__builtin_amdgcn_readfirstlane((u32)e0) | ((u64)__builtin_amdgcn_readfirstlane((u32)(e0 >> 32)) << 32)) * 6364136223846793005ull + 1442695040888963407ull; };
  if (RARE(role == 7)) {
    const rsrc_t rq = mk(gq, S_ * D_ * 2);
    const int row = 64 * tb64 + 8 * w + r8;
    u32x4 xs[7];
#pragma unroll
    for (int i = 0; i < 7; ++i) xs[i] = __builtin_amdgcn_raw_buffer_load_b128(rq, (u32)(row * 896 + (l & 7) * 16 + i * 128), 0, 0);
    const u64 e0 = __hip_atomic_load((GAS u64*)(ws + WS_E), RLX_AGENT);
    __builtin_amdgcn_raw_buffer_store_b128(u32x4{EMPTY, EMPTY, EMPTY, EMPTY}, rw, tid < 64 ? WS_S2 + (u32)((tb64 + (tid & 32)) * 512 + (tid & 31) * 16) : OOB, 0, SC1);
    SB();
    const u64 tag = mktag(e0);
    float s[4] = {};
#pragma unroll
    for (int i = 0; i < 7; ++i)
#pragma unroll
      for (int e = 0; e < 4; ++e) s[e] += bflo(xs[i][e]) + bfhi(xs[i][e]);
    const float mean = x8sum((s[0] + s[1]) + (s[2] + s[3])) * (1.f / D_);
    float v[4] = {};
#pragma unroll
    for (int i = 0; i < 7; ++i)
#pragma unroll
      for (int e = 0; e < 4; ++e) { const float d0 = bflo(xs[i][e]) - mean, d1 = bfhi(xs[i][e]) - mean; v[e] += d0 * d0; v[e] += d1 * d1; }
    const float rstd = __builtin_amdgcn_rsqf(x8sum((v[0] + v[1]) + (v[2] + v[3])) * (1.f / D_) + 1e-5f);
    const u32 so = WS_ST + (row >> 8) * 2048 + ((row >> 1) & 3) * 512 + ((row >> 5) & 7) * 64 + ((row >> 3) & 3) * 16 + (row & 1) * 8;
    __builtin_amdgcn_raw_buffer_store_b64(u32x2{__float_as_uint(mean), __float_as_uint(rstd)}, rw, (l & 7) == 0 ? so : OOB, 0, SC1);
    VMW(0); bar();
    if (tid == 0) setflag(ws, WS_F1 + (tb64 * 8 + 7) * 128, tag);
#pragma unroll
    for (int i = 0; i < 7; ++i) {
      const u32 o = (u32)(i * 8192 + tid * 16);
      __builtin_amdgcn_raw_buffer_store_b128(u32x4{EMPTY, EMPTY, EMPTY, EMPTY}, rw, o < TB ? o : o + 31 * TB, WS_H + (u32)tb64 * TB, SC1);
    }
    VMW(0); bar();
    if (tid == 0) setflag(ws, WS_F2 + tb64 * 128, tag);
    STAMP(1);
    {
      const rsrc_t rdw = mk(gwd, D_ * F_ * 2);
      const u32 t = __builtin_amdgcn_raw_buffer_load_b32(rdw, tid < 392 ? (u32)((tb64 * 392 + tid) * 128) : OOB, 0, 0);
      asm volatile("" :: "v"(t));
    }
    STAMP(2);
    const u32x4 dw = srd(ws, WS_END);
    const u32 lb = (u32)(size_t)(LAS char*)lds;
    __builtin_amdgcn_s_sleep(HELP_DELAY);
#pragma unroll 1
    for (int half = 0; half < 2; ++half) {
      const int tb = 32 * half + tb64;
#pragma unroll 1
      for (int it = 0; it < 4000; ++it) {
#pragma unroll
        for (int k = 0; k < 4; ++k) if (k < 3 || l < 32) dma<SC1>(dw, lb + 32768 + w * 3584 + k * 1024, l * 16, WS_H + (u32)(tb * TB + w * 3584 + k * 1024));
        VMW(0);
        u32 m = 0;
#pragma unroll
        for (int k = 0; k < 4; ++k) m = max(m, max4(*(const u32x4*)(lds + 32768 + w * 3584 + (k < 3 ? k * 1024 + l * 16 : 3072 + (l & 31) * 16))));
        if (!__any(m == EMPTY)) break;
        __builtin_amdgcn_s_sleep(3);
      }
      if (half == 0) { STAMP(4); } else { STAMP(6); }
      float s1 = 0.f, s2 = 0.f;
#pragma unroll
      for (int i = 0; i < 7; ++i) {
        const u32x2 t = trd(lds, 32768 + (u32)((56 * w + 4 * (G >> 1) + q4) * 64 + (G & 1) * 32 + p4 * 8) + i * 512);
        s1 = dot2(t.x, ONES, s1); s2 = dot2(t.x, t.x, s2); s1 = dot2(t.y, ONES, s1); s2 = dot2(t.y, t.y, s2);
      }
      *(float*)(lds + 20480 + half * 2048 + l * 32 + w * 4) = xsum(s1, s2);
      bar();
      if (w == 0) {
        const f32x4 a = *(const f32x4*)(lds + 20480 + half * 2048 + l * 32), c = *(const f32x4*)(lds + 20480 + half * 2048 + l * 32 + 16);
        __builtin_amdgcn_raw_buffer_store_b128(__builtin_bit_cast(u32x4, ln2_terms(((a.x + a.y) + (a.z + a.w)) + ((c.x + c.y) + (c.z + c.w)))), rw, l < 32 ? WS_S2 + (u32)(tb * 512 + l * 16) : OOB, 0, SC1);
      }
      if (half == 0) { STAMP(5); }
    }
    STAMP(3);
    STAMP_OUT();
    return;
  }
  const int cp = xc * 28 + sc, cch = w & 1, vp = w >> 1;
  const int ech = (l >> 2) & 1;
  const u32 eo = (u32)((8 * w + r8) * TB + (2 * cp + ech) * 64 + p4 * 16);
  u32x4 fhi; u32x2 flo; u32 Dh; u64 e0;
  const u32 lb = (u32)(size_t)(LAS char*)lds;
  const u32x4 dw = srd(ws, WS_END);
  {
    const u32x4 rk = srd(gk, S_ * D_ * 2), rv = srd(gv, S_ * D_ * 2);
    const int c8 = l & 7, s0 = 64 * tb64;
    const int swv = ((r8 >> 1) & 1) | ((w & 1) << 1), swk = swv ^ 2;
    const u32 g0 = (u32)((s0 + 8 * w + r8) * 896 + role * 128);
    if (w == 0) dma(rk, lb + L_K, g0 - 8 * 896 + ((c8 ^ (swv << 1)) << 4), 0);
    if (w == 1) dma(srd(gsk, D_ * 8), lb + L_SK, (u32)(512 * role + 16 * l), 0);
    dma(rk, lb + L_K + 1024 + w * 1024, g0 + ((c8 ^ (swk << 1)) << 4), 0);
    dma(rv, lb + L_V + w * 1024, g0 + ((c8 ^ (swv << 1)) << 4), 0);
    dma(srd(gq, S_ * D_ * 2), lb + L_X + w * 1024, g0 + ((c8 ^ (swv << 1)) << 4), 0);
    const u32x4 rf = srd(gf, S_ * D_ * 2);
    const int fq = vp * 64 + l;
    const u32 fo = (u32)((2 * cp + cch) * 4096 + 16 * fq);
    asm volatile("buffer_load_dwordx4 %0, %2, %4, 0 offen\n\tbuffer_load_dwordx2 %1, %3, %4, 0 offen" : "=&v"(fhi), "=&v"(flo) : "v"(fo), "v"(fq > 0 ? fo - 8 : OOB), "s"(rf) : "memory");
    asm volatile("buffer_load_ushort %0, %1, %2, 0 offen" : "=v"(Dh) : "v"((u32)((2 * cp + ((l >> 2) & 1)) * 2)), "s"(srd(gD, D_ * 2)) : "memory");
    e0 = flag_ld(dw, WS_E);
    SB();
  }
  const int gx = (xc >> 1) * 28 + sc, gi = 8 * (xc & 1) + (gx & 7), gj = gx >> 3;
  W1 w1;
  {
    const u32 v0 = (u32)(((4 * w + G) * F_ + 128 * gj) * 2);
    w1.rg = srd(gwg, D_ * F_ * 2); w1.ru = srd(gwu, D_ * F_ * 2); w1.lb = lb + w * 1024;
    w1.rot = ROT1;
    w1.vg = v0 + ((i16 ^ (4 * G)) << 4); w1.vu = v0 + ((i16 ^ (4 * G + 2)) << 4);
    w1.ra = (u32)((G & 1) * 8192 + (8 * (G >> 1) + q4) * 256 + ((w ^ (2 * q4 + (G & 1))) * 32) + p4 * 8);
  }
  w1_issue<0>(w1, L_FC); w1_issue<1>(w1, L_FC + 16384);
  VMW(9); bar();
  STAMP(1);
  {
    const int tbl = w >> 2, chg = w & 3;
    const u32 rb = (u32)((32 * tbl + 8 * G + q4) * 128 + p4 * 8);
    const u32 sv = (u32)((chg ^ ((q4 >> 1) | ((G & 1) << 1))) * 32), sk2 = sv ^ 64;
    const u32x2 km = trd(lds, L_K + rb + 512 + sv), k0 = trd(lds, L_K + rb + 1024 + sk2), k1 = trd(lds, L_K + rb + 1536 + sk2);
    const u32x2 v0 = trd(lds, L_V + rb + sv), v1 = trd(lds, L_V + rb + 512 + sv);
    const u32x2 skv = *(const u32x2*)(lds + L_SK + (16 * chg + i16) * 8);
    const float kk[11] = {bfhi(km.x), bflo(km.y), bfhi(km.y), bflo(k0.x), bfhi(k0.x), bflo(k0.y), bfhi(k0.y), bflo(k1.x), bfhi(k1.x), bflo(k1.y), bfhi(k1.y)};
    const float vv[8] = {bflo(v0.x), bfhi(v0.x), bflo(v0.y), bfhi(v0.y), bflo(v1.x), bfhi(v1.x), bflo(v1.y), bfhi(v1.y)};
    const float s0 = bflo(skv.x), s1 = bfhi(skv.x), s2 = bflo(skv.y), s3 = bfhi(skv.y);
    float u[8];
#pragma unroll
    for (int e = 0; e < 8; ++e) u[e] = (kk[e + 3] * s0 + kk[e + 2] * s1 + kk[e + 1] * s2 + kk[e] * s3) * vv[e];
    const u32 o = (u32)((2 * tb64 + tbl) * TB + (64 * role + 16 * chg + i16) * 64 + G * 16);
    __builtin_amdgcn_raw_buffer_store_b128(u32x4{pk(u[0], u[1]), pk(u[2], u[3]), pk(u[4], u[5]), pk(u[6], u[7])}, rw, WS_U + o, 0, SC1);
    SB();
    STAMP(2);
    VMW(9); bar();
    const u32x2 x0 = trd(lds, L_X + rb + sv), x1 = trd(lds, L_X + rb + 512 + sv);
    __builtin_amdgcn_raw_buffer_store_b128(cat(x0, x1), rw, WS_X + o, 0, SC1);
    VMW(6); PIN(fhi); PIN(flo); PIN(Dh); PIN(e0);
    SB();
  }
  STAMP(3);
  const u64 tag = mktag(e0);
  const float Dv = bflo(Dh);
  SB();
  w1_issue<2>(w1, L_RING); w1_issue<3>(w1, L_RING + 16384);
  STAMP(4);
  VMW(4); bar();
  if (tid == 0) setflag(ws, WS_F1 + (tb64 * 8 + role) * 128, tag);
  STAMP(5);
  const int hh = l >> 5, tk = l & 31;
  const u32 gso = WS_G + (u32)(((2 * gi * 2 + (tk >> 4)) * 56 + 4 * gj + (w >> 1)) * 1024 + (2 * (w & 1) + hh) * 256 + i16 * 16);
  auto gofs = [](int g) AI { return (u32)((32 * (g >> 1) + (g & 1)) * 2 * 56 * 1024); };
  u32x4 W[28];
  f32x4 cs0 = {}, cs1 = {};
  auto colsum = [&](int k0, int k1) AI {
#pragma unroll
    for (int k = k0; k < k1; ++k) { cs0 = mfma4(u32x2{ONES, ONES}, u32x2{W[k].x, W[k].y}, cs0); cs1 = mfma4(u32x2{ONES, ONES}, u32x2{W[k].z, W[k].w}, cs1); }
  };
  const u32 ra0 = opq(w1.ra), ra1 = opq(w1.ra + L_RING), ra2 = opq(w1.ra + L_RING + 49152);
  w1_read<0>(lds, ra0 + L_FC, W); w1_read<1>(lds, ra0 + L_FC + 16384, W);
  w1_issue<7>(w1, L_RED); w1_issue<8>(w1, L_RED + 16384);
  bar();
  {
    const u32 a0 = __builtin_amdgcn_alignbit(flo.y, flo.x, 16), a1 = __builtin_amdgcn_alignbit(fhi.x, flo.y, 16), a2 = __builtin_amdgcn_alignbit(fhi.y, fhi.x, 16),
              a3 = __builtin_amdgcn_alignbit(fhi.z, fhi.y, 16), a4 = __builtin_amdgcn_alignbit(fhi.w, fhi.z, 16);
    char* fc = lds + L_FC + cch * FCH + (vp * 64 + l) * 16 + 32;
    *(u32x4*)(fc) = fhi;
    *(u32x4*)(fc + FCS) = u32x4{a1, a2, a3, a4};
    *(u32x4*)(fc + 2 * FCS) = u32x4{flo.y, fhi.x, fhi.y, fhi.z};
    *(u32x4*)(fc + 3 * FCS) = u32x4{a0, a1, a2, a3};
    if (tid >= 128 && tid < 144) *(u32x4*)(lds + L_FC + (tid & 7) * FCS + ((tid >> 3) & 1) * 16) = u32x4{0, 0, 0, 0};
    if (tid >= 192 && tid < 252) { const int z = tid - 192; *(u32x4*)(lds + L_U + (z / 30) * UCH + (z % 30) * 16) = u32x4{0, 0, 0, 0}; }
  }
  SB();
  colsum(0, 4);
  w1_issue<4>(w1, L_RING + 32768); w1_issue<5>(w1, L_RING + 49152); w1_issue<6>(w1, L_RING + 65536);
  SB();

  STAMP(6);
  poll(ws, WS_F1 + (u32)(32 * w + 8 * (l & 3) + ((l & 4) ? 7 : (cp >> 5))) * 128, l < 8, tag);
  STAMP(7);
  u32x4 uu = ldg(dw, eo, WS_U), ex = ldg(dw, eo, WS_X);
  u32x4 est[4];
#pragma unroll
  for (int i = 0; i < 4; ++i) est[i] = ldg(dw, (u32)(r8 * 64 + p4 * 16), WS_ST + (u32)(w * 2048 + i * 512));
  bar();
  w1_read<7>(lds, ra0 + L_RED, W); w1_read<8>(lds, ra0 + L_RED + 16384, W);
  w1_read<2>(lds, ra1, W); w1_read<3>(lds, ra1 + 16384, W); w1_read<4>(lds, ra1 + 32768, W); w1_read<5>(lds, ra2, W); w1_read<6>(lds, ra2 + 16384, W);
  const u32 aA = opq(L_FC + cch * FCH + q4 * FCS + 64 - 16 * G + 8 * p4 - 8 + 128 * vp);
  const u32 aB = opq(L_U + cch * UCH + 32 * i16 + 16 * G + 448 - 128 * vp - 64);
  const u32 rwr = opq(L_RED + w * 4096 + G * 256 + ((i16 ^ ((G >> 1) << 3)) << 4));
  const u32 rr = opq(L_RED + ech * 4096 + (w & 3) * 1024 + (p4 & 1) * 512 + (((2 * r8 + (p4 >> 1)) ^ ((p4 & 1) << 3)) << 4));
  colsum(4, 18);
  PIN(cs0); PIN(cs1);
  SB();
  vmw<5>(uu);
  *(u32x4*)(lds + L_U + ech * UCH + 480 + (8 * w + r8) * 64 + p4 * 16) = uu;
  bar();
  STAMP(8);
  auto rdA = [&](int v, int m) AI { const u32 o = 64 * v + 512 * m; return cat(trd(lds, aA + o + 8), trd(lds, aA + o)); };
  auto rdB = [&](int v, int m) AI { return *(const u32x4*)(lds + aB + 64 * (1 - v) + 512 * m); };
  float ex8[8], eq8[8], eu8[8];
  auto terms = [&]() AI {
#pragma unroll
    for (int i = 0; i < 4; ++i) {
      ex8[2 * i] = bflo(ex[i]); ex8[2 * i + 1] = bfhi(ex[i]);
      eu8[2 * i] = bflo(uu[i]) * Dv; eu8[2 * i + 1] = op(bfhi(uu[i]) * Dv);
      eq8[2 * i] = (ex8[2 * i] - __uint_as_float(est[i].x)) * __uint_as_float(est[i].y);
      eq8[2 * i + 1] = op(op(ex8[2 * i + 1] - __uint_as_float(est[i].z)) * __uint_as_float(est[i].w));
    }
#pragma unroll
    for (int i = 0; i < 8; ++i) { PIN(ex8[i]); PIN(eq8[i]); PIN(eu8[i]); }
  };
  auto epilogue = [&]() AI {
    f32x4 p[8];
#pragma unroll
    for (int n = 0; n < 4; ++n) { p[2 * n] = *(const f32x4*)(lds + rr + n * 8192); p[2 * n + 1] = *(const f32x4*)(lds + rr + n * 8192 + 256); }
    float h[8];
#pragma unroll
    for (int i = 0; i < 8; ++i) {
      const float y = op(op(eu8[i] + p[i >> 2][i & 3]) + p[2 + (i >> 2)][i & 3]) + op(p[4 + (i >> 2)][i & 3] + p[6 + (i >> 2)][i & 3]);
      h[i] = op(ex8[i] + eq8[i] * op(y));
    }
    __builtin_amdgcn_raw_buffer_store_b128(u32x4{pk(h[0], h[1]), pk(h[2], h[3]), pk(h[4], h[5]), pk(h[6], h[7])}, rw, WS_H + eo, 0, SC1);
  };
  u64 fl;
  const u32 f2o = WS_F2 + (u32)(8 * (w & 3) + (l & 7)) * 128;
  {
    f32x4 acc[4];
    u32x4 A[2][4], B[2][4];
#pragma unroll
    for (int v = 0; v < 2; ++v) {
      A[v][0] = rdA(v, 0); SB();
#pragma unroll
      for (int m = 0; m < 4; ++m) { B[v][m] = rdB(v, m); SB(); }
#pragma unroll
      for (int m = 1; m < 4; ++m) { A[v][m] = rdA(v, m); SB(); }
      if (v == 0) {
        vmw<0>(ex); PIN(est[0]); PIN(est[1]); PIN(est[2]); PIN(est[3]);
        fl = flag_ld(dw, f2o);
        if (w < 4) terms();
        SB();
      }
    }
#pragma unroll
    for (int v = 0; v < 2; ++v)
#pragma unroll
      for (int m = 0; m < 4; ++m) {
#pragma unroll
        for (int c = m; c < 4; ++c) acc[c] = mfma16(A[v][m], B[v][c - m], (v == 0 && m == 0) ? f32x4{} : acc[c]);
        SB();
        if (v == 0 && m == 0) w1_g<9>(w1, L_RING);
        if (v == 0 && m == 2) w1_u<9>(w1, L_RING);
        if (v == 1 && m == 0) w1_g<10>(w1, L_RING + 16384);
        SB();
      }
#pragma unroll
    for (int c = 0; c < 4; ++c) *(f32x4*)(lds + rwr + c * 1024) = acc[c];
  }
  VMWP(3, fl);
  if (RARE(!__all(fl == tag))) poll<2>(ws, f2o, true, tag);
  bar();
  STAMP(9);
  if (w < 4) epilogue();
  SB();
  STAMP(10);
  {
    f32x4 acc[4];
    u32x4 A[2][8], B[2][8];
    auto rd = [&](int v, int m) AI {
      if (m == 0) {
        A[v][0] = rdA(v, 0);
#pragma unroll
        for (int d = 4; d < 8; ++d) B[v][d] = rdB(v, d);
      } else { A[v][m] = rdA(v, m); if (m <= 4) B[v][4 - m] = rdB(v, 4 - m); }
      SB();
    };
    rd(0, 0); rd(0, 1); rd(0, 2); rd(0, 3);
#pragma unroll
    for (int v = 0; v < 2; ++v) {
#pragma unroll
      for (int m = 0; m < 8; ++m) {
#pragma unroll
        for (int c = 4; c < 8; ++c) if (m <= c) acc[c - 4] = mfma16(A[v][m], B[v][c - m], (v == 0 && m == 0) ? f32x4{} : acc[c - 4]);
        SB();
        if (m < 4) rd(v, m + 4); else if (v == 0) rd(1, m - 4);
        if (v == 0) {
          if (m == 0) w1_u<10>(w1, L_RING + 16384);
          if (m == 1) w1_g<11>(w1, L_RING + 32768);
          if (m == 2) w1_u<11>(w1, L_RING + 32768);
          if (m == 3) w1_g<12>(w1, L_RING + 49152);
          if (m == 4) w1_u<12>(w1, L_RING + 49152);
          if (m == 6) w1_g<13>(w1, L_RING + 65536);
          if (m == 7) w1_u<13>(w1, L_RING + 65536);
        }
        SB();
      }
    }
    if (w >= 4) terms();
    STAMP(11);
    VMW(0); bar();
#pragma unroll
    for (int c = 0; c < 4; ++c) *(f32x4*)(lds + rwr + c * 1024) = acc[c];
  }
  const u32 lo16 = l * 16;
  if (w < 4) {
#pragma unroll
    for (int k = 0; k < 7; ++k) dma<SC1>(dw, lb + L_H0 + (7 * w + k) * 1024, lo16, WS_H + (u32)(2 * gi * TB) + wrap(7 * w + k + 2 * w1.rot, 28) * 1024);
  }
  w1_read<9>(lds, ra1, W); w1_read<10>(lds, ra1 + 16384, W); w1_read<11>(lds, ra1 + 32768, W); w1_read<12>(lds, ra2, W); w1_read<13>(lds, ra2 + 16384, W);
  bar();
  STAMP(12);

  const u32 bta = (u32)((8 * (G >> 1) + q4) * 64 + (G & 1) * 32 + p4 * 8);
  auto hbase = [](int g) AI { return g == 0 ? L_H0 : g == 1 ? L_H1 : g == 2 ? L_H2 : L_H3; };
  u32 csh, csl;
  auto colfin = [&]() AI {
    colsum(18, 28);
    const float t = hsum(cs0.x + cs1.x);
    const u32 hi = pk(t, t);
    csh = hh ? 0u : hi; csl = hh ? 0u : pk(t - bflo(hi), 0.f);
    PIN(csh); PIN(csl);
  };
  f32x4 tm; float gu[8], gt[8]; u32 od[4];
  auto center = [&](f32x16& acc, float m) AI {
    const u32 d = __float_as_uint(m);
    acc = mfma32(u32x4{csh, csl, 0, 0}, u32x4{d, d & 0xffffu, 0, 0}, acc);
  };
  auto geglu_step = [&](int g, f32x16& acc, int i) AI {
#pragma unroll
    for (int e = 0; e < 8; ++e) {
      const int s = i - (2 + (18 * e) / 7);
      if (s == 0) { gt[e] = acc[e] * acc[e]; gu[e] = op(acc[e] * acc[e + 8]); PIN(gt[e]); PIN(gu[e]); }
      if (s == 1) { gt[e] = tm.w * gt[e] + tm.z; PIN(gt[e]); }
      if (s == 2) { gt[e] = acc[e] * gt[e]; PIN(gt[e]); }
      if (s == 3) { gt[e] = __builtin_amdgcn_exp2f(gt[e]); PIN(gt[e]); }
      if (s == 4) { gt[e] = gt[e] * tm.y + tm.y; PIN(gt[e]); }
      if (s == 5) { gt[e] = __builtin_amdgcn_rcpf(gt[e]); PIN(gt[e]); }
      if (s == 6) { gu[e] = gu[e] * gt[e]; PIN(gu[e]); }
      if (s == 7 && (e & 1)) { od[e >> 1] = pk(gu[e - 1], gu[e]); PIN(od[e >> 1]); }
    }
    if (i == 28) {
      __builtin_amdgcn_raw_buffer_store_b128(u32x4{od[0], od[1], od[2], od[3]}, rw, gso, gofs(g), SC1);
    }
  };
  constexpr int PF = 4;
  u32x4 B[PF];
  u32 ba = opq(hbase(0) + bta);
  auto chain = [&](int g, f32x16& acc, auto side) AI {
    const u32 bn = opq(hbase(g < 3 ? g + 1 : g) + bta);
    sfor<28>([&](auto kc) AI {
      constexpr int k = decltype(kc)::value;
      acc = mfma32(W[k], B[k % PF], k == 0 ? f32x16{} : acc);
      SB();
      side(k);
      if (k + PF < 28) B[k % PF] = cat(trd(lds, ba + (k + PF) * 1024), trd(lds, ba + (k + PF) * 1024 + 256));
      else if (g < 3) B[k % PF] = cat(trd(lds, bn + (k + PF - 28) * 1024), trd(lds, bn + (k + PF - 28) * 1024 + 256));
      SB();
    });
    ba = bn;
  };
  auto flag3 = [&](int g) AI { setflag(ws, WS_F3 + (u32)(((gi * 4 + g) * 14) + gj) * 128, tag); };
  const int ri = 2 * xc + sc / 14, rq2 = (sc / 7) & 1, n2 = sc % 7;
  const u32x4 rd = srd(gwd, D_ * F_ * 2);
  const u32 wdv = (u32)(((224 * w + r8) * D_ + 64 * n2) * 2 + (((l & 7) ^ (((r8 >> 1) & 3) << 1)) << 4));
  const u32 blkR = w < 7 ? L_R + w * 4096 : L_R2;
  u32 wda[4], wdr[4];
#pragma unroll
  for (int nt = 0; nt < 4; ++nt) {
    const u32 a = (16 * (G >> 1) + 4 * (G & 1) + q4) * 128 + ((nt ^ (2 * (G & 1) + (q4 >> 1))) * 32) + p4 * 8;
    wda[nt] = opq(a + L_P + w * 4096); wdr[nt] = opq(a + blkR);
  }
  u32x4 Wd[7][4];
  auto wblk = [](int k) AI { return (k == 1 || k == 4) ? 1 : (k == 3 || k == 6) ? 2 : 0; };
  auto wd_dma = [&](int k, int i) AI {
    if (i == 0) dma_at(lb + (wblk(k) == 0 ? blkR : (wblk(k) == 2 ? L_Q : L_P) + w * 4096));
    dma_i<0>(i, rd, wdv, 4 * k * (8 * D_ * 2) + i * (8 * D_ * 2 - 1024));
  };
  auto wd_rd = [&](int k, int nt) AI {
    const u32 a = wblk(k) == 0 ? wdr[nt] : wda[nt] + (wblk(k) == 2 ? L_Q - L_P : 0);
    Wd[k][nt] = cat(trd(lds, a), trd(lds, a + 1024));
  };
  auto wd_read = [&](int k) AI { wd_rd(k, 0); wd_rd(k, 1); wd_rd(k, 2); wd_rd(k, 3); };
  const int j0 = (14 * w) >> 3, jn = ((14 * w + 13) >> 3) - j0;
  const u32 f3o = WS_F3 + (u32)(ri * 56 + j0 + ((l & 3) < jn ? (l & 3) : jn)) * 128;

  f32x16 accA, accB;
  u32x4 tm0, tm1, tm2, tm3;
  const u32 s2o = WS_S2 + (u32)(2 * gi * 512 + tk * 16);
  const u32 hp = (u32)(w < 4 ? 4 * w : 4 + 3 * w) * 1024;
  u32 hs[4];
#pragma unroll
  for (int i = 0; i < 4; ++i) hs[i] = (wrap((hp >> 10) + i + 2 * w1.rot, 28) - i) * 1024;
  auto htb = [&](int g) AI { return WS_H + (u32)((32 * (g >> 1) + 2 * gi + (g & 1)) * TB); };
  auto h_dma = [&](int g, int i) AI {
    if (i == 0) dma_at(lb + hbase(g) + hp);
    if (i < 3 || w < 4) dma_i<SC1>(i, dw, lo16, htb(g) + hs[i]);
  };
  auto redo1 = [&](int g, f32x16& acc, u32 m) AI {
    if (RARE(__any(acc[0] != acc[0]))) {
      REDO_COUNT();
      const u32 sc0 = L_SP + w * 1024;
      acc = f32x16{};
      const rsrc_t rg = mk(gwg, D_ * F_ * 2), ru = mk(gwu, D_ * F_ * 2);
#pragma unroll 1
      for (int k = 0; k < 28; ++k) {
        const u32 wo = (u32)(((16 * k + ((l >> 1) & 15)) * F_ + 128 * gj + 16 * w) * 2 + (l & 1) * 16);
        const u32x4 wg = __builtin_amdgcn_raw_buffer_load_b128(rg, wo, 0, 0), wu = __builtin_amdgcn_raw_buffer_load_b128(ru, wo, 0, 0);
        *(u32x4*)(lds + sc0 + lo16) = l < 32 ? wg : wu;
        const u32 wa = sc0 + (G & 1) * 512 + (8 * (G >> 1) + q4) * 32 + p4 * 8;
        const u32x4 a = cat(trd(lds, wa), trd(lds, wa + 128));
        u32x4 v;
#pragma unroll 1
        for (int it = 0; it < 400; ++it) { v = ldg(dw, lo16, htb(g) + k * 1024); vmw<0>(v); if (!__any(max4(v) == EMPTY)) break; }
        *(u32x4*)(lds + sc0 + lo16) = v;
        acc = mfma32(a, cat(trd(lds, sc0 + bta), trd(lds, sc0 + bta + 256)), acc);
      }
      center(acc, __uint_as_float(m));
    }
  };
  auto tm_fix = [&](u32x4& t, u32 so) AI {
    if (RARE(__any(max4(t) == EMPTY))) {
#pragma unroll 1
      for (int it = 0; it < 4000; ++it) { FIX_COUNT(); t = ldg(dw, s2o, so); vmw<0>(t); if (!__any(max4(t) == EMPTY)) break; }
    }
  };
  SB();
  if (w >= 4) {
    epilogue();
    SB();
    colfin();
  } else {
    colfin();
    SB();
    const u32 ha = opq(L_H0 + 7 * w * 1024 + lo16);
#pragma unroll 1
    for (int it = 0; it < 4000; ++it) {
      VMW(0);
      u32 m = 0;
#pragma unroll
      for (int k = 0; k < 7; ++k) { const u32x4 v = *(const u32x4*)(lds + ha + k * 1024); m = max(m, max4(v)); }
      if (!__any(m == EMPTY)) break;
#pragma unroll
      for (int k = 0; k < 7; ++k) dma<SC1>(dw, lb + L_H0 + (7 * w + k) * 1024, lo16, WS_H + (u32)(2 * gi * TB) + wrap(7 * w + k + 2 * w1.rot, 28) * 1024);
    }
    VMW(0);
    STAMP(13);
  }
  __builtin_amdgcn_s_setprio(1);
  bar();
  STAMP(14);
#pragma unroll
  for (int k = 0; k < PF; ++k) B[k] = cat(trd(lds, ba + k * 1024), trd(lds, ba + k * 1024 + 256));
  SB();
  chain(0, accA, [&](int k) AI {
    if (k <= 6 && !(k & 1)) h_dma(1, k >> 1);
    if (k == TM0K) tm0 = ldg(dw, s2o, 0);
    if (k == 10 || k == 13) __builtin_amdgcn_raw_buffer_store_b128(u32x4{EMPTY, EMPTY, EMPTY, EMPTY}, rw, gso, gofs(k == 10 ? 2 : 3), SC1);
    if (k >= 16 && k <= 25 && k % 3 == 1) wd_dma(0, (k - 16) / 3);
    if (k == 23) { VMW(6); RBAR(); if (tid == 0 && b == 0) setflag(ws, WS_E, tag); }
    if (k == 27) { vmw<(TM0K > 13 ? 4 : 6)>(tm0); tm_fix(tm0, 0); }
  });
  center(accA, __uint_as_float(tm0.x));
  tm = __builtin_bit_cast(f32x4, tm0);
  SB();
  STAMP(16);
  chain(1, accB, [&](int k) AI {
    if (k == 1) RBAR();
    geglu_step(0, accA, k);
    if (k == 2) tm1 = ldg(dw, s2o, 512);
    if (k == 3 || k == 5) wd_dma(1, (k - 3) >> 1);
    if (k >= 7 && k <= 10) h_dma(2, k - 7);
    if (k == 12) { VMW(6); dma_at(lb + L_P + w * 4096); }
    if (k == 13 || k == 15) wd_dma(1, (k - 9) >> 1);
    if (k >= 12 && k <= 18 && !(k & 1)) wd_rd(0, (k - 12) >> 1);
    if (k == 17) { vmw<7>(tm1); tm_fix(tm1, 512); }
    if (k >= 21 && (k & 1)) wd_dma(2, (k - 21) >> 1);
    if (k == 23) { VMW(4); RBAR(); }
  });
  center(accB, __uint_as_float(tm1.x));
  geglu_step(0, accA, 28);
  tm = __builtin_bit_cast(f32x4, tm1);
  SB();
  STAMP(18);
  chain(2, accA, [&](int k) AI {
    if (k == 1) RBAR();
    if (k == 2) redo1(1, accB, tm1.x);
    geglu_step(1, accB, k);
    if (k <= 6 && !(k & 1)) h_dma(3, k >> 1);
    if (k >= 3 && k <= 9 && (k & 1)) { if (k == 3) VMW(7); wd_rd(1, (k - 3) >> 1); }
    if (k == 8) { tm2 = ldg(dw, s2o, 32 * 512); tm3 = ldg(dw, s2o, 33 * 512); }
    if (k >= 10 && k <= 16 && !(k & 1)) wd_dma(3, (k - 10) >> 1);
    if (k >= 18 && k <= 27 && k % 3 == 0) wd_dma(4, (k - 18) / 3);
    if (k == 23) { vmw<6>(tm2); PIN(tm3); RBAR(); if (tid == 0) flag3(0); }
    if (k == 25) tm_fix(tm2, 32 * 512);
    if (k == 26) tm_fix(tm3, 33 * 512);
  });
  center(accA, __uint_as_float(tm2.x));
  geglu_step(1, accB, 28);
  tm = __builtin_bit_cast(f32x4, tm2);
  SB();
  STAMP(20);
  const u32 gb = WS_G + (u32)((2 * ri * 2 + rq2) * 56 + 7 * w) * 1024;
  constexpr int NS = 9;
  u32x4 Hf[NS];
  auto hl = [&](int t) AI { Hf[t % NS] = ldg(dw, lo16, gb + gofs(t / 7) + (t % 7) * 1024); };
  chain(3, accB, [&](int k) AI {
    if (k == 2) redo1(2, accA, tm2.x);
    geglu_step(2, accA, k);
    if (k >= 5 && k < 13) { if (k == 5) VMW(5); wd_rd(2 + (k - 5) / 4, (k - 5) & 3); }
    if (k == 10) fl = flag_ld(dw, f3o);
    if (k >= 12 && k <= 18 && !(k & 1)) wd_dma(5, (k - 12) >> 1);
    if (k == 13) { VMW(2); RBAR(); if (tid == 0) flag3(1); }
    if (k >= 13 && k < 21 && (k & 1)) wd_rd(4, (k - 13) >> 1);
    if (k >= 19 && k <= 22) wd_dma(6, k - 19);
    if (k == 23) { VMWP(8, fl); if (RARE(!__all(fl == tag))) poll(ws, f3o, true, tag); }
    if (k >= 23) hl(k - 23);
  });
  center(accB, __uint_as_float(tm3.x));
  geglu_step(2, accA, 28);
  tm = __builtin_bit_cast(f32x4, tm3);
  redo1(3, accB, tm3.x);
  STAMP(21);
#pragma unroll
  for (int e = 0; e < 8; ++e) {
    const float a = accB[e], u = accB[e + 8];
    gu[e] = (a * u) * __builtin_amdgcn_rcpf(__builtin_amdgcn_exp2f(a * (tm.w * (a * a) + tm.z)) * tm.y + tm.y);
  }
#pragma unroll
  for (int e = 0; e < 4; ++e) od[e] = pk(gu[2 * e], gu[2 * e + 1]);
  geglu_step(3, accB, 28);
  __builtin_amdgcn_s_setprio(0);
  SB();
  STAMP(22);
  f32x4 acc[2][4];
  const rsrc_t ro = mk(gout, S_ * D_ * 2);
  const u32 pw = (u32)(i16 * 256), tf = 4 * (w & 3) + G;
  const u32 hro = WS_H + (u32)((32 * (G >> 1) + (G & 1) + 2 * ri) * TB + (64 * n2 + 8 * w + (i16 >> 1)) * 64 + rq2 * 32 + (i16 & 1) * 16);
  auto pbuf = [](int g) AI { return g == 3 ? L_T : g == 1 ? L_Q : L_P; };
  auto put = [&](int g) AI {
    const u32 rb = opq(pbuf(g) + w * 4096 + pw);
#pragma unroll
    for (int nt = 0; nt < 4; ++nt) *(f32x4*)(lds + rb + (((4 * nt + G) ^ (i16 & 7)) << 4)) = acc[g & 1][nt];
  };
  auto fin = [&](int g) AI {
    if ((w >> 2) == (g & 1)) {
      const u32 a0 = tf * 256 + ((i16 ^ (tf & 7)) << 4), a = opq(pbuf(g) + a0);
      f32x4 p[9];
      p[8] = *(const f32x4*)(lds + L_RES + g * 4096 + a0);
#pragma unroll
      for (int n = 0; n < 8; ++n) p[n] = *(const f32x4*)(lds + a + n * 4096);
      const f32x4 sum = (((p[0] + p[1]) + (p[2] + p[3])) + ((p[4] + p[5]) + (p[6] + p[7]))) + p[8];
      const int tok = 32 * (32 * (g >> 1) + (g & 1) + 2 * ri) + 16 * rq2 + tf;
      __builtin_amdgcn_raw_buffer_store_b64(u32x2{pk(sum.x, sum.y), pk(sum.z, sum.w)}, ro, (u32)((tok * D_ + 64 * n2 + 4 * i16) * 2), 0, 2);
    }
  };
  auto redo = [&](int g) AI {
    if (RARE(__any(acc[g & 1][0].x != acc[g & 1][0].x))) {
      REDO_COUNT();
      u32x4 v[7];
#pragma unroll 1
      for (int it = 0; it < 4000; ++it) {
        u32 m = 0;
#pragma unroll
        for (int k = 0; k < 7; ++k) v[k] = ldg(dw, lo16, gb + gofs(g) + k * 1024);
#pragma unroll
        for (int k = 0; k < 7; ++k) { vmw<0>(v[k]); m = max(m, max4(v[k])); }
        if (!__any(m == EMPTY)) break;
      }
#pragma unroll
      for (int k = 0; k < 7; ++k)
#pragma unroll
        for (int nt = 0; nt < 4; ++nt) acc[g & 1][nt] = mfma16(Wd[k][nt], v[k], k == 0 ? f32x4{} : acc[g & 1][nt]);
    }
  };
  fl = flag_ld(dw, f3o + 14 * 128);
  u32x4 hr = ldg(dw, hro, 0);
  hl(5); hl(6);
  VMW(11);
  wd_read(5); wd_read(6);
  SB();
  STAMP(23);
  sfor<28>([&](auto tc) AI {
    constexpr int t = decltype(tc)::value, g = t / 7, k = t % 7;
    constexpr auto pos = [](int h) { return h < 5 ? h : h + 4; };
    constexpr auto last = [](int u) { return u < 2 ? 6 : u == 2 ? 8 : u == 3 ? 10 : u < 21 ? u + 7 : 27; };
    if (t == 2) { VMWP(3, fl); if (RARE(!__all(fl == tag))) poll(ws, f3o + 14 * 128, true, tag); }
#pragma unroll
    for (int h = (t ? last(t - 1) : 6) + 1; h <= last(t); ++h) hl(h);
    vmw<pos(last(t)) - pos(t)>(Hf[t % NS]);
#pragma unroll
    for (int nt = 0; nt < 4; ++nt) acc[g & 1][nt] = mfma16(Wd[k][nt], Hf[t % NS], k == 0 ? f32x4{} : acc[g & 1][nt]);
    SB();
    if (t == 3) {
      vmw<6>(hr);
      if (RARE(__any(max4(hr) == EMPTY))) {
#pragma unroll 1
        for (int it = 0; it < 4000; ++it) { hr = ldg(dw, hro, 0); vmw<0>(hr); if (!__any(max4(hr) == EMPTY)) break; }
      }
      *(u32x4*)(lds + blkR + lo16) = hr;
#pragma unroll
      for (int q = 0; q < 4; ++q) {
        const u32x2 r = trd(lds, blkR + q * 256 + (4 * (G & 1) + q4) * 32 + p4 * 8);
        *(f32x4*)(lds + L_RES + q * 4096 + pw + (((2 * w + (G & 1)) ^ (i16 & 7)) << 4)) = f32x4{bflo(r.x), bfhi(r.x), bflo(r.y), bfhi(r.y)};
      }
    }
    if (t == 8) put(0);
    if (t == 12) { bar(); STAMP(24); fin(0); }
    if (t == 15) put(1);
    if (t == 19) { bar(); STAMP(25); fin(1); }
    if (t == 22) { redo(2); put(2); STAMP(26); }
    SB();
  });
  redo(3);
  put(3);
  bar();
  STAMP(27);
  fin(2); fin(3);
  STAMP(28);
  STAMP_OUT();
}

}

extern "C" void kernel_launch(void* const* d_in, const int* in_sizes, int n_in, void* d_out, int out_size, void* d_ws, size_t ws_size,
                              hipStream_t stream) {
  h3_ffn<<<256, 512, 0, stream>>>((const char*)d_in[0], (const char*)d_in[1], (const char*)d_in[2], (const char*)d_in[3], (const char*)d_in[4],
                                  (const char*)d_in[5], (const char*)d_in[7], (const char*)d_in[8], (const char*)d_in[9], (char*)d_out, (char*)d_ws);
}
```

```cpp
#include <hip/hip_runtime.h>
#include <stdint.h>

namespace {

#define LAS __attribute__((address_space(3)))
#define GAS __attribute__((address_space(1)))
#define DI __device__ __forceinline__
#define AI __attribute__((always_inline))
#define RARE(x) __builtin_expect(!!(x), 0)
typedef uint32_t u32;
typedef unsigned long long u64;
typedef u32 u32x2 __attribute__((ext_vector_type(2)));
typedef u32 u32x4 __attribute__((ext_vector_type(4)));
typedef short s16x4 __attribute__((ext_vector_type(4)));
typedef short s16x8 __attribute__((ext_vector_type(8)));
typedef float f32x4 __attribute__((ext_vector_type(4)));
typedef float f32x16 __attribute__((ext_vector_type(16)));
typedef __bf16 bf2 __attribute__((ext_vector_type(2)));
typedef __amdgpu_buffer_rsrc_t rsrc_t;
#define RLX_AGENT __ATOMIC_RELAXED, __HIP_MEMORY_SCOPE_AGENT

constexpr int S_ = 2048, D_ = 448, F_ = 1792;
constexpr u32 TB = 448 * 64;
constexpr u32 WS_E  = 0;
constexpr u32 WS_F1 = 128;
constexpr u32 WS_F3 = WS_F1 + 256 * 128;
constexpr u32 WS_F2 = WS_F3 + 896 * 128;
constexpr u32 WS_S2 = 256 * 1024 + 16384;
constexpr u32 WS_ST = 256 * 1024;
constexpr u32 WS_U  = WS_S2 + 32768;
constexpr u32 WS_X  = WS_U + 64 * TB;
constexpr u32 WS_H  = WS_X + 64 * TB;
constexpr u32 WS_G  = WS_H + 64 * TB;
constexpr u32 WS_END = WS_G + 64 * 112 * 1024;
constexpr u32 WS_DBG = 16u << 20;
constexpr int SC1 = 16;
#define HELP_DELAY 8
#define TM0K 8
#define ROT1 (7 * (xc & 1) + ((7 * (gx & 3)) >> 2))
#define PRETOUCH 0
constexpr u32 OOB = 0xFFFFFF00u;
constexpr u32 ONES = 0x3f803f80u;
constexpr u32 EMPTY = 0xffffffffu;

constexpr u32 L_K = 0, L_V = 9216, L_X = 17408, L_SK = 25600;
constexpr u32 L_RED = 0, L_FC = 32768, FCS = 4160, FCH = 4 * FCS;
constexpr u32 L_U = L_FC + 2 * FCH, UCH = 4608, L_RING = L_U + 2 * UCH;
constexpr u32 L_H0 = 32768, L_H1 = L_H0 + TB, L_H2 = 0, L_H3 = L_H1 + TB + 32768, L_SP = 155648;
constexpr u32 L_P = TB, L_Q = L_P + 32768, L_R = L_Q + 32768, L_R2 = L_H3 + TB;
constexpr u32 L_RES = 0, L_T = L_H3;
constexpr u32 LDS_BYTES = 163840;

DI float bflo(u32 x) { return __uint_as_float(x << 16); }
DI float bfhi(u32 x) { return __uint_as_float(x & 0xffff0000u); }
DI u32 pk(float lo, float hi) { bf2 p; p.x = (__bf16)lo; p.y = (__bf16)hi; return __builtin_bit_cast(u32, p); }
DI rsrc_t mk(const void* p, u32 bytes) { return __builtin_amdgcn_make_buffer_rsrc((void*)p, 0, bytes, 0x00020000); }
DI u32x2 trd(const char* lds, u32 a) { return __builtin_bit_cast(u32x2, __builtin_amdgcn_ds_read_tr16_b64_v4i16((LAS s16x4*)(lds + a))); }
DI u32x4 cat(u32x2 a, u32x2 b) { return u32x4{a.x, a.y, b.x, b.y}; }
DI u32x4 srd(const void* p, u32 bytes) { const u64 a = (u64)p; return u32x4{(u32)a, (u32)(a >> 32) & 0xffffu, bytes, 0x00020000u}; }
template <int AUX = 0> DI void dma(u32x4 r, u32 ldsoff, u32 voff, u32 soff) {
  if (AUX) asm volatile("s_mov_b32 m0, %0\n\ts_nop 0\n\tbuffer_load_dwordx4 %1, %2, %3 offen sc1 lds" :: "s"(ldsoff), "v"(voff), "s"(r), "s"(soff) : "memory");
  else asm volatile("s_mov_b32 m0, %0\n\ts_nop 0\n\tbuffer_load_dwordx4 %1, %2, %3 offen lds" :: "s"(ldsoff), "v"(voff), "s"(r), "s"(soff) : "memory");
}
DI void dma_at(u32 ldsoff) { asm volatile("s_mov_b32 m0, %0\n\ts_nop 0" :: "s"(ldsoff) : "memory"); }
template <int OFF, int AUX> DI void dma_go(u32x4 r, u32 voff, u32 soff) {
  if (AUX) asm volatile("buffer_load_dwordx4 %0, %1, %2 offen offset:%3 sc1 lds" :: "v"(voff), "s"(r), "s"(soff), "n"(OFF) : "memory");
  else asm volatile("buffer_load_dwordx4 %0, %1, %2 offen offset:%3 lds" :: "v"(voff), "s"(r), "s"(soff), "n"(OFF) : "memory");
}
template <int AUX> DI void dma_i(int i, u32x4 r, u32 voff, u32 soff) {
  if (i == 0) dma_go<0, AUX>(r, voff, soff); else if (i == 1) dma_go<1024, AUX>(r, voff, soff); else if (i == 2) dma_go<2048, AUX>(r, voff, soff); else dma_go<3072, AUX>(r, voff, soff);
}
DI float fnma(float a, float b, float c) { float r; asm("v_fma_f32 %0, -%1, %2, %3" : "=v"(r) : "v"(a), "v"(b), "v"(c)); return r; }
DI float op(float x) { asm("" : "+v"(x)); return x; }
DI u32 opq(u32 x) { asm volatile("" : "+v"(x)); return x; }
#define PIN(x) asm volatile("" : "+v"(x))
DI f32x4 mfma16(u32x4 a, u32x4 b, f32x4 c) { return __builtin_amdgcn_mfma_f32_16x16x32_bf16(__builtin_bit_cast(s16x8, a), __builtin_bit_cast(s16x8, b), c, 0, 0, 0); }
DI f32x16 mfma32(u32x4 a, u32x4 b, f32x16 c) { return __builtin_amdgcn_mfma_f32_32x32x16_bf16(__builtin_bit_cast(s16x8, a), __builtin_bit_cast(s16x8, b), c, 0, 0, 0); }
DI f32x4 mfma4(u32x2 a, u32x2 b, f32x4 c) { return __builtin_amdgcn_mfma_f32_4x4x4bf16_1k(__builtin_bit_cast(s16x4, a), __builtin_bit_cast(s16x4, b), c, 0, 0, 0); }
DI float dot2(u32 a, u32 b, float c) { return __builtin_amdgcn_fdot2_f32_bf16(__builtin_bit_cast(bf2, a), __builtin_bit_cast(bf2, b), c, false); }
#define VMW(n) asm volatile("s_waitcnt vmcnt(%0)" :: "n"(n) : "memory")
#define LG0() asm volatile("s_waitcnt lgkmcnt(0)" ::: "memory")
#define SB() __builtin_amdgcn_sched_barrier(0)
DI void bar() { asm volatile("s_waitcnt lgkmcnt(0)\n\ts_barrier" ::: "memory"); }
#define RBAR() asm volatile("s_barrier" ::: "memory")
DI float wsum(float v) {
  v += __builtin_amdgcn_update_dpp(0.f, v, 0x111, 0xf, 0xf, true);
  v += __builtin_amdgcn_update_dpp(0.f, v, 0x112, 0xf, 0xf, true);
  v += __builtin_amdgcn_update_dpp(0.f, v, 0x114, 0xf, 0xf, true);
  v += __builtin_amdgcn_update_dpp(0.f, v, 0x118, 0xf, 0xf, true);
  v += __builtin_amdgcn_update_dpp(0.f, v, 0x142, 0xa, 0xf, true);
  v += __builtin_amdgcn_update_dpp(0.f, v, 0x143, 0xc, 0xf, true);
  return __uint_as_float(__builtin_amdgcn_readlane(__float_as_uint(v), 63));
}
DI float hsum(float v) {
  const auto r = __builtin_amdgcn_permlane32_swap(__float_as_uint(v), __float_as_uint(v), false, false);
  return __uint_as_float(r[0]) + __uint_as_float(r[1]);
}
template <int SLEEP = 1> DI void poll(char* ws, u32 off, bool act, u64 tag) {
  bool ok = !act;
#pragma unroll 1
  for (int it = 0; it < 20000; ++it) {
    u64 x = tag;
    if (!ok) x = __hip_atomic_load((GAS u64*)(ws + off), RLX_AGENT);
    ok = (x == tag);
    if (__all(ok)) break;
    __builtin_amdgcn_s_sleep(SLEEP);
  }
  asm volatile("" ::: "memory");
}
DI u32 max4(u32x4 v) { return max(max(v.x, v.y), max(v.z, v.w)); }
DI u64 flag_ld(u32x4 r, u32 voff) { u64 v; asm volatile("buffer_load_dwordx2 %0, %1, %2, 0 offen sc1" : "=v"(v) : "v"(voff), "s"(r) : "memory"); return v; }
DI u32x4 ldg(u32x4 r, u32 voff, u32 soff) { u32x4 v; asm volatile("buffer_load_dwordx4 %0, %1, %2, %3 offen sc1" : "=v"(v) : "v"(voff), "s"(r), "s"(soff) : "memory"); return v; }
template <int N> DI void vmw(u32x4& x) { asm volatile("s_waitcnt vmcnt(%1)" : "+v"(x) : "n"(N) : "memory"); }
#define VMWP(n, x) asm volatile("s_waitcnt vmcnt(" #n ")" : "+v"(x) :: "memory")
DI void setflag(char* ws, u32 off, u64 tag) { __hip_atomic_store((GAS u64*)(ws + off), tag, RLX_AGENT); }

#define STAMP(i)
#define STAMPC(i)
#define REDO_COUNT()
#define FIX_COUNT()
#define STAMP_OUT()

DI float xsum(float a, float c) {
  const auto r = __builtin_amdgcn_permlane32_swap(__float_as_uint(a), __float_as_uint(c), false, false);
  return __uint_as_float(r[0]) + __uint_as_float(r[1]);
}
DI f32x4 ln2_terms(float t) {
  const auto r = __builtin_amdgcn_permlane32_swap(__float_as_uint(t), __float_as_uint(t), false, false);
  const float mean = __uint_as_float(r[0]) * (1.f / D_);
  const float V = fmaxf(__uint_as_float(r[1]) * (1.f / D_) - mean * mean, 0.f) + 1e-5f;
  const float rs = __builtin_amdgcn_rsqf(V);
  const float K1 = -2.f * 0.7978845608028654f * 1.4426950408889634f * rs;
  const float lo = -mean - bflo(pk(-mean, 0.f));
  return f32x4{__uint_as_float(pk(-mean, lo)), V, K1, (K1 * rs) * (0.044715f * rs)};
}
template <int N> struct IC { static constexpr int value = N; };
template <int N, int I = 0, class F> DI void sfor(F&& f) { if constexpr (I < N) { f(IC<I>{}); sfor<N, I + 1>(f); } }
struct W1 { u32x4 rg, ru; u32 vg, vu, ra, lb, rot; };
DI u32 wrap(u32 x, u32 n) { return x >= n ? x - n : x; }
template <int T> DI void w1_g(const W1& c, u32 base) { dma(c.rg, c.lb + base, c.vg, wrap(T + c.rot, 14) * (32 * F_ * 2)); }
template <int T> DI void w1_u(const W1& c, u32 base) { dma(c.ru, c.lb + base + 8192, c.vu, wrap(T + c.rot, 14) * (32 * F_ * 2)); }
template <int T> DI void w1_issue(const W1& c, u32 base) { w1_g<T>(c, base); w1_u<T>(c, base); }
template <int T> DI void w1_read(const char* lds, u32 a, u32x4 (&W)[28]) {
#pragma unroll
  for (int kk = 0; kk < 2; ++kk) W[2 * T + kk] = cat(trd(lds, a + kk * 4096), trd(lds, a + kk * 4096 + 1024));
}
DI float x8sum(float v) {
  v += __builtin_amdgcn_update_dpp(0.f, v, 0xb1, 0xf, 0xf, true);
  v += __builtin_amdgcn_update_dpp(0.f, v, 0x4e, 0xf, 0xf, true);
  v += __builtin_amdgcn_update_dpp(0.f, v, 0x141, 0xf, 0xf, true);
  return v;
}

__global__ __launch_bounds__(512, 2) void h3_ffn(const char* __restrict__ gq, const char* __restrict__ gk, const char* __restrict__ gv,
                                                 const char* __restrict__ gsk, const char* __restrict__ gf, const char* __restrict__ gD,
                                                 const char* __restrict__ gwg, const char* __restrict__ gwu, const char* __restrict__ gwd,
                                                 char* __restrict__ gout, char* ws) {
  __shared__ __attribute__((aligned(16))) char lds[LDS_BYTES];
  const int tid = threadIdx.x, l = tid & 63, w = __builtin_amdgcn_readfirstlane(tid >> 6);
  const int b = blockIdx.x, xc = b & 7, sl = b >> 3;
  const int tb64 = 4 * xc + (sl >> 3), role = sl & 7;
  const int sc = (sl >> 3) * 7 + role;
  asm volatile("" :: "s"(gq), "s"(gk), "s"(gv), "s"(gsk), "s"(gf), "s"(gD), "s"(gwg), "s"(gwu), "s"(gwd), "s"(gout), "s"(ws));
  STAMP(0);
  const rsrc_t rw = mk(ws, WS_END);
  const int G = l >> 4, i16 = l & 15, q4 = i16 >> 2, p4 = l & 3, r8 = l >> 3;
  auto mktag = [&](u64 e0) AI { return ((u64)__builtin_amdgcn_readfirstlane((u32)e0) | ((u64)__builtin_amdgcn_readfirstlane((u32)(e0 >> 32)) << 32)) * 6364136223846793005ull + 1442695040888963407ull; };
  if (RARE(role == 7)) {
    const rsrc_t rq = mk(gq, S_ * D_ * 2);
    const int row = 64 * tb64 + 8 * w + r8;
    u32x4 xs[7];
#pragma unroll
    for (int i = 0; i < 7; ++i) xs[i] = __builtin_amdgcn_raw_buffer_load_b128(rq, (u32)(row * 896 + (l & 7) * 16 + i * 128), 0, 0);
    const u64 e0 = __hip_atomic_load((GAS u64*)(ws + WS_E), RLX_AGENT);
    __builtin_amdgcn_raw_buffer_store_b128(u32x4{EMPTY, EMPTY, EMPTY, EMPTY}, rw, tid < 64 ? WS_S2 + (u32)((tb64 + (tid & 32)) * 512 + (tid & 31) * 16) : OOB, 0, SC1);
    SB();
    u32 tlb = 0;
    if (w == 7) {
      const u32 n = (WS_END + 1048576 - 1) / 1048576, m2 = (S_ * D_ * 2 + 1048576 - 1) / 1048576;
      const u32 i = min((u32)l, n + 4 * m2 - 1);
      const char* p = i < n ? ws + (size_t)i * 1048576 + 64 : (i < n + m2 ? (const char*)gout : i < n + 2 * m2 ? gwd : i < n + 3 * m2 ? gwg : gwu) + ((i - n) % m2) * 1048576;
      asm volatile("global_load_dword %0, %1, off" : "=v"(tlb) : "v"(p) : "memory");
    }
    SB();
    const u64 tag = mktag(e0);
    float s[4] = {};
#pragma unroll
    for (int i = 0; i < 7; ++i)
#pragma unroll
      for (int e = 0; e < 4; ++e) s[e] += bflo(xs[i][e]) + bfhi(xs[i][e]);
    const float mean = x8sum((s[0] + s[1]) + (s[2] + s[3])) * (1.f / D_);
    float v[4] = {};
#pragma unroll
    for (int i = 0; i < 7; ++i)
#pragma unroll
      for (int e = 0; e < 4; ++e) { const float d0 = bflo(xs[i][e]) - mean, d1 = bfhi(xs[i][e]) - mean; v[e] += d0 * d0; v[e] += d1 * d1; }
    const float rstd = __builtin_amdgcn_rsqf(x8sum((v[0] + v[1]) + (v[2] + v[3])) * (1.f / D_) + 1e-5f);
    if ((l & 7) == 0) *(u32x2*)(lds + ((r8 >> 1) & 3) * 128 + w * 16 + (r8 & 1) * 8) = u32x2{__float_as_uint(mean), __float_as_uint(rstd)};
    bar();
    if (w == 0) {
      __builtin_amdgcn_raw_buffer_store_b128(*(const u32x4*)(lds + (l & 31) * 16), rw, l < 32 ? WS_ST + (u32)((tb64 >> 2) * 2048 + (l >> 3) * 512 + (tb64 & 3) * 128 + (l & 7) * 16) : OOB, 0, SC1);
      VMW(0);
    }
    if (tid == 0) setflag(ws, WS_F1 + (tb64 * 8 + 7) * 128, tag);
    bar();
#pragma unroll
    for (int i = 0; i < 7; ++i) {
      const u32 o = (u32)(i * 8192 + tid * 16);
      __builtin_amdgcn_raw_buffer_store_b128(u32x4{EMPTY, EMPTY, EMPTY, EMPTY}, rw, o < TB ? o : o + 31 * TB, WS_H + (u32)tb64 * TB, SC1);
    }
    VMW(0); PIN(tlb); bar();
    if (tid == 0) setflag(ws, WS_F2 + tb64 * 128, tag);
    STAMP(1);
    {
      const rsrc_t rdw = mk(gwd, D_ * F_ * 2);
      const u32 t = __builtin_amdgcn_raw_buffer_load_b32(rdw, tid < 392 ? (u32)((tb64 * 392 + tid) * 128) : OOB, 0, 0);
      asm volatile("" :: "v"(t));
    }
    STAMP(2);
    const u32x4 dw = srd(ws, WS_END);
    const u32 lb = (u32)(size_t)(LAS char*)lds;
    __builtin_amdgcn_s_sleep(HELP_DELAY);
#pragma unroll 1
    for (int half = 0; half < 2; ++half) {
      const int tb = 32 * half + tb64;
#pragma unroll 1
      for (int it = 0; it < 4000; ++it) {
#pragma unroll
        for (int k = 0; k < 4; ++k) if (k < 3 || l < 32) dma<SC1>(dw, lb + 32768 + w * 3584 + k * 1024, l * 16, WS_H + (u32)(tb * TB + w * 3584 + k * 1024));
        VMW(0);
        u32 m = 0;
#pragma unroll
        for (int k = 0; k < 4; ++k) m = max(m, max4(*(const u32x4*)(lds + 32768 + w * 3584 + (k < 3 ? k * 1024 + l * 16 : 3072 + (l & 31) * 16))));
        if (!__any(m == EMPTY)) break;
        __builtin_amdgcn_s_sleep(3);
      }
      if (half == 0) { STAMP(4); } else { STAMP(6); }
      float s1 = 0.f, s2 = 0.f;
#pragma unroll
      for (int i = 0; i < 7; ++i) {
        const u32x2 t = trd(lds, 32768 + (u32)((56 * w + 4 * (G >> 1) + q4) * 64 + (G & 1) * 32 + p4 * 8) + i * 512);
        s1 = dot2(t.x, ONES, s1); s2 = dot2(t.x, t.x, s2); s1 = dot2(t.y, ONES, s1); s2 = dot2(t.y, t.y, s2);
      }
      *(float*)(lds + 20480 + half * 2048 + l * 32 + w * 4) = xsum(s1, s2);
      bar();
      if (w == 0) {
        const f32x4 a = *(const f32x4*)(lds + 20480 + half * 2048 + l * 32), c = *(const f32x4*)(lds + 20480 + half * 2048 + l * 32 + 16);
        __builtin_amdgcn_raw_buffer_store_b128(__builtin_bit_cast(u32x4, ln2_terms(((a.x + a.y) + (a.z + a.w)) + ((c.x + c.y) + (c.z + c.w)))), rw, l < 32 ? WS_S2 + (u32)(tb * 512 + l * 16) : OOB, 0, SC1);
      }
      if (half == 0) { STAMP(5); }
    }
    STAMP(3);
    STAMP_OUT();
    return;
  }
  const int cp = xc * 28 + sc, cch = w & 1, vp = w >> 1;
  const int ech = (l >> 2) & 1;
  const u32 eo = (u32)((8 * w + r8) * TB + (2 * cp + ech) * 64 + p4 * 16);
  u32x4 fhi; u32x2 flo; u32 Dh; u64 e0;
  const u32 lb = (u32)(size_t)(LAS char*)lds;
  const u32x4 dw = srd(ws, WS_END);
  {
    const u32x4 rk = srd(gk, S_ * D_ * 2), rv = srd(gv, S_ * D_ * 2);
    const int c8 = l & 7, s0 = 64 * tb64;
    const int swv = ((r8 >> 1) & 1) | ((w & 1) << 1), swk = swv ^ 2;
    const u32 g0 = (u32)((s0 + 8 * w + r8) * 896 + role * 128);
    if (w == 0) dma(rk, lb + L_K, g0 - 8 * 896 + ((c8 ^ (swv << 1)) << 4), 0);
    if (w == 1) dma(srd(gsk, D_ * 8), lb + L_SK, (u32)(512 * role + 16 * l), 0);
    dma(rk, lb + L_K + 1024 + w * 1024, g0 + ((c8 ^ (swk << 1)) << 4), 0);
    dma(rv, lb + L_V + w * 1024, g0 + ((c8 ^ (swv << 1)) << 4), 0);
    dma(srd(gq, S_ * D_ * 2), lb + L_X + w * 1024, g0 + ((c8 ^ (swv << 1)) << 4), 0);
    const u32x4 rf = srd(gf, S_ * D_ * 2);
    const int fq = vp * 64 + l;
    const u32 fo = (u32)((2 * cp + cch) * 4096 + 16 * fq);
    asm volatile("buffer_load_dwordx4 %0, %2, %4, 0 offen\n\tbuffer_load_dwordx2 %1, %3, %4, 0 offen" : "=&v"(fhi), "=&v"(flo) : "v"(fo), "v"(fq > 0 ? fo - 8 : OOB), "s"(rf) : "memory");
    asm volatile("buffer_load_ushort %0, %1, %2, 0 offen" : "=v"(Dh) : "v"((u32)((2 * cp + ((l >> 2) & 1)) * 2)), "s"(srd(gD, D_ * 2)) : "memory");
    e0 = flag_ld(dw, WS_E);
    SB();
  }
  const int gx = (xc >> 1) * 28 + sc, gi = 8 * (xc & 1) + (gx & 7), gj = gx >> 3;
  W1 w1;
  {
    const u32 v0 = (u32)(((4 * w + G) * F_ + 128 * gj) * 2);
    w1.rg = srd(gwg, D_ * F_ * 2); w1.ru = srd(gwu, D_ * F_ * 2); w1.lb = lb + w * 1024;
    w1.rot = ROT1;
    w1.vg = v0 + ((i16 ^ (4 * G)) << 4); w1.vu = v0 + ((i16 ^ (4 * G + 2)) << 4);
    w1.ra = (u32)((G & 1) * 8192 + (8 * (G >> 1) + q4) * 256 + ((w ^ (2 * q4 + (G & 1))) * 32) + p4 * 8);
  }
  w1_issue<0>(w1, L_FC); w1_issue<1>(w1, L_FC + 16384);
  VMW(9); bar();
  STAMP(1);
  {
    const int tbl = w >> 2, chg = w & 3;
    const u32 rb = (u32)((32 * tbl + 8 * G + q4) * 128 + p4 * 8);
    const u32 sv = (u32)((chg ^ ((q4 >> 1) | ((G & 1) << 1))) * 32), sk2 = sv ^ 64;
    const u32x2 km = trd(lds, L_K + rb + 512 + sv), k0 = trd(lds, L_K + rb + 1024 + sk2), k1 = trd(lds, L_K + rb + 1536 + sk2);
    const u32x2 v0 = trd(lds, L_V + rb + sv), v1 = trd(lds, L_V + rb + 512 + sv);
    const u32x2 skv = *(const u32x2*)(lds + L_SK + (16 * chg + i16) * 8);
    const float kk[11] = {bfhi(km.x), bflo(km.y), bfhi(km.y), bflo(k0.x), bfhi(k0.x), bflo(k0.y), bfhi(k0.y), bflo(k1.x), bfhi(k1.x), bflo(k1.y), bfhi(k1.y)};
    const float vv[8] = {bflo(v0.x), bfhi(v0.x), bflo(v0.y), bfhi(v0.y), bflo(v1.x), bfhi(v1.x), bflo(v1.y), bfhi(v1.y)};
    const float s0 = bflo(skv.x), s1 = bfhi(skv.x), s2 = bflo(skv.y), s3 = bfhi(skv.y);
    float u[8];
#pragma unroll
    for (int e = 0; e < 8; ++e) u[e] = (kk[e + 3] * s0 + kk[e + 2] * s1 + kk[e + 1] * s2 + kk[e] * s3) * vv[e];
    const u32 o = (u32)((2 * tb64 + tbl) * TB + (64 * role + 16 * chg + i16) * 64 + G * 16);
    __builtin_amdgcn_raw_buffer_store_b128(u32x4{pk(u[0], u[1]), pk(u[2], u[3]), pk(u[4], u[5]), pk(u[6], u[7])}, rw, WS_U + o, 0, SC1);
    SB();
    STAMP(2);
    VMW(9); bar();
    const u32x2 x0 = trd(lds, L_X + rb + sv), x1 = trd(lds, L_X + rb + 512 + sv);
    __builtin_amdgcn_raw_buffer_store_b128(cat(x0, x1), rw, WS_X + o, 0, SC1);
    VMW(6); PIN(fhi); PIN(flo); PIN(Dh); PIN(e0);
    SB();
  }
  STAMP(3);
  const u64 tag = mktag(e0);
  const float Dv = bflo(Dh);
  SB();
  w1_issue<2>(w1, L_RING); w1_issue<3>(w1, L_RING + 16384);
  STAMP(4);
  VMW(4); bar();
  if (tid == 0) setflag(ws, WS_F1 + (tb64 * 8 + role) * 128, tag);
  STAMP(5);
  const int hh = l >> 5, tk = l & 31;
  const u32 gso = WS_G + (u32)(((2 * gi * 2 + (tk >> 4)) * 56 + 4 * gj + (w >> 1)) * 1024 + (2 * (w & 1) + hh) * 256 + i16 * 16);
  auto gofs = [](int g) AI { return (u32)((32 * (g >> 1) + (g & 1)) * 2 * 56 * 1024); };
  u32x4 W[28];
  f32x4 cs0 = {}, cs1 = {};
  auto colsum = [&](int k0, int k1) AI {
#pragma unroll
    for (int k = k0; k < k1; ++k) { cs0 = mfma4(u32x2{ONES, ONES}, u32x2{W[k].x, W[k].y}, cs0); cs1 = mfma4(u32x2{ONES, ONES}, u32x2{W[k].z, W[k].w}, cs1); }
  };
  const u32 ra0 = opq(w1.ra), ra1 = opq(w1.ra + L_RING), ra2 = opq(w1.ra + L_RING + 49152);
  w1_read<0>(lds, ra0 + L_FC, W); w1_read<1>(lds, ra0 + L_FC + 16384, W);
  w1_issue<7>(w1, L_RED); w1_issue<8>(w1, L_RED + 16384);
  bar();
  {
    const u32 a0 = __builtin_amdgcn_alignbit(flo.y, flo.x, 16), a1 = __builtin_amdgcn_alignbit(fhi.x, flo.y, 16), a2 = __builtin_amdgcn_alignbit(fhi.y, fhi.x, 16),
              a3 = __builtin_amdgcn_alignbit(fhi.z, fhi.y, 16), a4 = __builtin_amdgcn_alignbit(fhi.w, fhi.z, 16);
    char* fc = lds + L_FC + cch * FCH + (vp * 64 + l) * 16 + 32;
    *(u32x4*)(fc) = fhi;
    *(u32x4*)(fc + FCS) = u32x4{a1, a2, a3, a4};
    *(u32x4*)(fc + 2 * FCS) = u32x4{flo.y, fhi.x, fhi.y, fhi.z};
    *(u32x4*)(fc + 3 * FCS) = u32x4{a0, a1, a2, a3};
    if (tid >= 128 && tid < 144) *(u32x4*)(lds + L_FC + (tid & 7) * FCS + ((tid >> 3) & 1) * 16) = u32x4{0, 0, 0, 0};
    if (tid >= 192 && tid < 252) { const int z = tid - 192; *(u32x4*)(lds + L_U + (z / 30) * UCH + (z % 30) * 16) = u32x4{0, 0, 0, 0}; }
  }
  SB();
  colsum(0, 4);
  w1_issue<4>(w1, L_RING + 32768); w1_issue<5>(w1, L_RING + 49152); w1_issue<6>(w1, L_RING + 65536);
  SB();

  STAMP(6);
  poll(ws, WS_F1 + (u32)(32 * w + 8 * (l & 3) + ((l & 4) ? 7 : (cp >> 5))) * 128, l < 8, tag);
  STAMP(7);
  u32x4 uu = ldg(dw, eo, WS_U), ex = ldg(dw, eo, WS_X);
  u32x4 est[4];
#pragma unroll
  for (int i = 0; i < 4; ++i) est[i] = ldg(dw, (u32)(r8 * 64 + p4 * 16), WS_ST + (u32)(w * 2048 + i * 512));
  bar();
  w1_read<7>(lds, ra0 + L_RED, W); w1_read<8>(lds, ra0 + L_RED + 16384, W);
  w1_read<2>(lds, ra1, W); w1_read<3>(lds, ra1 + 16384, W); w1_read<4>(lds, ra1 + 32768, W); w1_read<5>(lds, ra2, W); w1_read<6>(lds, ra2 + 16384, W);
  const u32 aA = opq(L_FC + cch * FCH + q4 * FCS + 64 - 16 * G + 8 * p4 - 8 + 128 * vp);
  const u32 aB = opq(L_U + cch * UCH + 32 * i16 + 16 * G + 448 - 128 * vp - 64);
  const u32 rwr = opq(L_RED + w * 4096 + G * 256 + ((i16 ^ ((G >> 1) << 3)) << 4));
  const u32 rr = opq(L_RED + ech * 4096 + (w & 3) * 1024 + (p4 & 1) * 512 + (((2 * r8 + (p4 >> 1)) ^ ((p4 & 1) << 3)) << 4));
  colsum(4, 18);
  PIN(cs0); PIN(cs1);
  SB();
  vmw<5>(uu);
  *(u32x4*)(lds + L_U + ech * UCH + 480 + (8 * w + r8) * 64 + p4 * 16) = uu;
  bar();
  STAMP(8);
  auto rdA = [&](int v, int m) AI { const u32 o = 64 * v + 512 * m; return cat(trd(lds, aA + o + 8), trd(lds, aA + o)); };
  auto rdB = [&](int v, int m) AI { return *(const u32x4*)(lds + aB + 64 * (1 - v) + 512 * m); };
  float ex8[8], eq8[8], eu8[8];
  auto terms = [&]() AI {
#pragma unroll
    for (int i = 0; i < 4; ++i) {
      ex8[2 * i] = bflo(ex[i]); ex8[2 * i + 1] = bfhi(ex[i]);
      eu8[2 * i] = bflo(uu[i]) * Dv; eu8[2 * i + 1] = op(bfhi(uu[i]) * Dv);
      eq8[2 * i] = (ex8[2 * i] - __uint_as_float(est[i].x)) * __uint_as_float(est[i].y);
      eq8[2 * i + 1] = op(op(ex8[2 * i + 1] - __uint_as_float(est[i].z)) * __uint_as_float(est[i].w));
    }
#pragma unroll
    for (int i = 0; i < 8; ++i) { PIN(ex8[i]); PIN(eq8[i]); PIN(eu8[i]); }
  };
  auto epilogue = [&]() AI {
    f32x4 p[8];
#pragma unroll
    for (int n = 0; n < 4; ++n) { p[2 * n] = *(const f32x4*)(lds + rr + n * 8192); p[2 * n + 1] = *(const f32x4*)(lds + rr + n * 8192 + 256); }
    float h[8];
#pragma unroll
    for (int i = 0; i < 8; ++i) {
      const float y = op(op(eu8[i] + p[i >> 2][i & 3]) + p[2 + (i >> 2)][i & 3]) + op(p[4 + (i >> 2)][i & 3] + p[6 + (i >> 2)][i & 3]);
      h[i] = op(ex8[i] + eq8[i] * op(y));
    }
    __builtin_amdgcn_raw_buffer_store_b128(u32x4{pk(h[0], h[1]), pk(h[2], h[3]), pk(h[4], h[5]), pk(h[6], h[7])}, rw, WS_H + eo, 0, SC1);
  };
  u64 fl;
  const u32 f2o = WS_F2 + (u32)(8 * (w & 3) + (l & 7)) * 128;
  {
    f32x4 acc[4];
    u32x4 A[2][4], B[2][4];
#pragma unroll
    for (int v = 0; v < 2; ++v) {
      A[v][0] = rdA(v, 0); SB();
#pragma unroll
      for (int m = 0; m < 4; ++m) { B[v][m] = rdB(v, m); SB(); }
#pragma unroll
      for (int m = 1; m < 4; ++m) { A[v][m] = rdA(v, m); SB(); }
      if (v == 0) {
        vmw<0>(ex); PIN(est[0]); PIN(est[1]); PIN(est[2]); PIN(est[3]);
        fl = flag_ld(dw, f2o);
        if (w < 4) terms();
        SB();
      }
    }
#pragma unroll
    for (int v = 0; v < 2; ++v)
#pragma unroll
      for (int m = 0; m < 4; ++m) {
#pragma unroll
        for (int c = m; c < 4; ++c) acc[c] = mfma16(A[v][m], B[v][c - m], (v == 0 && m == 0) ? f32x4{} : acc[c]);
        SB();
        if (v == 0 && m == 0) w1_g<9>(w1, L_RING);
        if (v == 0 && m == 2) w1_u<9>(w1, L_RING);
        if (v == 1 && m == 0) w1_g<10>(w1, L_RING + 16384);
        SB();
      }
#pragma unroll
    for (int c = 0; c < 4; ++c) *(f32x4*)(lds + rwr + c * 1024) = acc[c];
  }
  VMWP(3, fl);
  if (RARE(!__all(fl == tag))) poll<2>(ws, f2o, true, tag);
  bar();
  STAMP(9);
  if (w < 4) epilogue();
  SB();
  STAMP(10);
  {
    f32x4 acc[4];
    u32x4 A[2][8], B[2][8];
    auto rd = [&](int v, int m) AI {
      if (m == 0) {
        A[v][0] = rdA(v, 0);
#pragma unroll
        for (int d = 4; d < 8; ++d) B[v][d] = rdB(v, d);
      } else { A[v][m] = rdA(v, m); if (m <= 4) B[v][4 - m] = rdB(v, 4 - m); }
      SB();
    };
    rd(0, 0); rd(0, 1); rd(0, 2); rd(0, 3);
#pragma unroll
    for (int v = 0; v < 2; ++v) {
#pragma unroll
      for (int m = 0; m < 8; ++m) {
#pragma unroll
        for (int c = 4; c < 8; ++c) if (m <= c) acc[c - 4] = mfma16(A[v][m], B[v][c - m], (v == 0 && m == 0) ? f32x4{} : acc[c - 4]);
        SB();
        if (m < 4) rd(v, m + 4); else if (v == 0) rd(1, m - 4);
        if (v == 0) {
          if (m == 0) w1_u<10>(w1, L_RING + 16384);
          if (m == 1) w1_g<11>(w1, L_RING + 32768);
          if (m == 2) w1_u<11>(w1, L_RING + 32768);
          if (m == 3) w1_g<12>(w1, L_RING + 49152);
          if (m == 4) w1_u<12>(w1, L_RING + 49152);
          if (m == 6) w1_g<13>(w1, L_RING + 65536);
          if (m == 7) w1_u<13>(w1, L_RING + 65536);
        }
        SB();
      }
    }
    if (w >= 4) terms();
    STAMP(11);
    VMW(0); bar();
#pragma unroll
    for (int c = 0; c < 4; ++c) *(f32x4*)(lds + rwr + c * 1024) = acc[c];
  }
  const u32 lo16 = l * 16;
  if (w < 4) {
#pragma unroll
    for (int k = 0; k < 7; ++k) dma<SC1>(dw, lb + L_H0 + (7 * w + k) * 1024, lo16, WS_H + (u32)(2 * gi * TB) + wrap(7 * w + k + 2 * w1.rot, 28) * 1024);
  }
  w1_read<9>(lds, ra1, W); w1_read<10>(lds, ra1 + 16384, W); w1_read<11>(lds, ra1 + 32768, W); w1_read<12>(lds, ra2, W); w1_read<13>(lds, ra2 + 16384, W);
  bar();
  STAMP(12);

  const u32 bta = (u32)((8 * (G >> 1) + q4) * 64 + (G & 1) * 32 + p4 * 8);
  auto hbase = [](int g) AI { return g == 0 ? L_H0 : g == 1 ? L_H1 : g == 2 ? L_H2 : L_H3; };
  u32 csh, csl;
  auto colfin = [&]() AI {
    colsum(18, 28);
    const float t = hsum(cs0.x + cs1.x);
    const u32 hi = pk(t, t);
    csh = hh ? 0u : hi; csl = hh ? 0u : pk(t - bflo(hi), 0.f);
    PIN(csh); PIN(csl);
  };
  f32x4 tm; float gu[8], gt[8]; u32 od[4];
  auto center = [&](f32x16& acc, float m) AI {
    const u32 d = __float_as_uint(m);
    acc = mfma32(u32x4{csh, csl, 0, 0}, u32x4{d, d & 0xffffu, 0, 0}, acc);
  };
  auto geglu_step = [&](int g, f32x16& acc, int i) AI {
#pragma unroll
    for (int e = 0; e < 8; ++e) {
      const int s = i - (2 + (18 * e) / 7);
      if (s == 0) { gt[e] = acc[e] * acc[e]; gu[e] = op(acc[e] * acc[e + 8]); PIN(gt[e]); PIN(gu[e]); }
      if (s == 1) { gt[e] = tm.w * gt[e] + tm.z; PIN(gt[e]); }
      if (s == 2) { gt[e] = acc[e] * gt[e]; PIN(gt[e]); }
      if (s == 3) { gt[e] = __builtin_amdgcn_exp2f(gt[e]); PIN(gt[e]); }
      if (s == 4) { gt[e] = gt[e] * tm.y + tm.y; PIN(gt[e]); }
      if (s == 5) { gt[e] = __builtin_amdgcn_rcpf(gt[e]); PIN(gt[e]); }
      if (s == 6) { gu[e] = gu[e] * gt[e]; PIN(gu[e]); }
      if (s == 7 && (e & 1)) { od[e >> 1] = pk(gu[e - 1], gu[e]); PIN(od[e >> 1]); }
    }
    if (i == 28) {
      __builtin_amdgcn_raw_buffer_store_b128(u32x4{od[0], od[1], od[2], od[3]}, rw, gso, gofs(g), SC1);
    }
  };
  constexpr int PF = 4;
  u32x4 B[PF];
  u32 ba = opq(hbase(0) + bta);
  auto chain = [&](int g, f32x16& acc, auto side) AI {
    const u32 bn = opq(hbase(g < 3 ? g + 1 : g) + bta);
    sfor<28>([&](auto kc) AI {
      constexpr int k = decltype(kc)::value;
      acc = mfma32(W[k], B[k % PF], k == 0 ? f32x16{} : acc);
      SB();
      side(k);
      if (k + PF < 28) B[k % PF] = cat(trd(lds, ba + (k + PF) * 1024), trd(lds, ba + (k + PF) * 1024 + 256));
      else if (g < 3) B[k % PF] = cat(trd(lds, bn + (k + PF - 28) * 1024), trd(lds, bn + (k + PF - 28) * 1024 + 256));
      SB();
    });
    ba = bn;
  };
  auto flag3 = [&](int g) AI { setflag(ws, WS_F3 + (u32)(((gi * 4 + g) * 14) + gj) * 128, tag); };
  const int ri = 2 * xc + sc / 14, rq2 = (sc / 7) & 1, n2 = sc % 7;
  const u32x4 rd = srd(gwd, D_ * F_ * 2);
  const u32 wdv = (u32)(((224 * w + r8) * D_ + 64 * n2) * 2 + (((l & 7) ^ (((r8 >> 1) & 3) << 1)) << 4));
  const u32 blkR = w < 7 ? L_R + w * 4096 : L_R2;
  u32 wda[4], wdr[4];
#pragma unroll
  for (int nt = 0; nt < 4; ++nt) {
    const u32 a = (16 * (G >> 1) + 4 * (G & 1) + q4) * 128 + ((nt ^ (2 * (G & 1) + (q4 >> 1))) * 32) + p4 * 8;
    wda[nt] = opq(a + L_P + w * 4096); wdr[nt] = opq(a + blkR);
  }
  u32x4 Wd[7][4];
  auto wblk = [](int k) AI { return (k == 1 || k == 4) ? 1 : (k == 3 || k == 6) ? 2 : 0; };
  auto wd_dma = [&](int k, int i) AI {
    if (i == 0) dma_at(lb + (wblk(k) == 0 ? blkR : (wblk(k) == 2 ? L_Q : L_P) + w * 4096));
    dma_i<0>(i, rd, wdv, 4 * k * (8 * D_ * 2) + i * (8 * D_ * 2 - 1024));
  };
  auto wd_rd = [&](int k, int nt) AI {
    const u32 a = wblk(k) == 0 ? wdr[nt] : wda[nt] + (wblk(k) == 2 ? L_Q - L_P : 0);
    Wd[k][nt] = cat(trd(lds, a), trd(lds, a + 1024));
  };
  auto wd_read = [&](int k) AI { wd_rd(k, 0); wd_rd(k, 1); wd_rd(k, 2); wd_rd(k, 3); };
  const int j0 = (14 * w) >> 3, jn = ((14 * w + 13) >> 3) - j0;
  const u32 f3o = WS_F3 + (u32)(ri * 56 + j0 + ((l & 3) < jn ? (l & 3) : jn)) * 128;

  f32x16 accA, accB;
  u32x4 tm0, tm1, tm2, tm3;
  const u32 s2o = WS_S2 + (u32)(2 * gi * 512 + tk * 16);
  const u32 hp = (u32)(w < 4 ? 4 * w : 4 + 3 * w) * 1024;
  u32 hs[4];
#pragma unroll
  for (int i = 0; i < 4; ++i) hs[i] = (wrap((hp >> 10) + i + 2 * w1.rot, 28) - i) * 1024;
  auto htb = [&](int g) AI { return WS_H + (u32)((32 * (g >> 1) + 2 * gi + (g & 1)) * TB); };
  auto h_dma = [&](int g, int i) AI {
    if (i == 0) dma_at(lb + hbase(g) + hp);
    if (i < 3 || w < 4) dma_i<SC1>(i, dw, lo16, htb(g) + hs[i]);
  };
  auto redo1 = [&](int g, f32x16& acc, u32 m) AI {
    if (RARE(__any(acc[0] != acc[0]))) {
      REDO_COUNT();
      const u32 sc0 = L_SP + w * 1024;
      acc = f32x16{};
      const rsrc_t rg = mk(gwg, D_ * F_ * 2), ru = mk(gwu, D_ * F_ * 2);
#pragma unroll 1
      for (int k = 0; k < 28; ++k) {
        const u32 wo = (u32)(((16 * k + ((l >> 1) & 15)) * F_ + 128 * gj + 16 * w) * 2 + (l & 1) * 16);
        const u32x4 wg = __builtin_amdgcn_raw_buffer_load_b128(rg, wo, 0, 0), wu = __builtin_amdgcn_raw_buffer_load_b128(ru, wo, 0, 0);
        *(u32x4*)(lds + sc0 + lo16) = l < 32 ? wg : wu;
        const u32 wa = sc0 + (G & 1) * 512 + (8 * (G >> 1) + q4) * 32 + p4 * 8;
        const u32x4 a = cat(trd(lds, wa), trd(lds, wa + 128));
        u32x4 v;
#pragma unroll 1
        for (int it = 0; it < 400; ++it) { v = ldg(dw, lo16, htb(g) + k * 1024); vmw<0>(v); if (!__any(max4(v) == EMPTY)) break; }
        *(u32x4*)(lds + sc0 + lo16) = v;
        acc = mfma32(a, cat(trd(lds, sc0 + bta), trd(lds, sc0 + bta + 256)), acc);
      }
      center(acc, __uint_as_float(m));
    }
  };
  auto tm_fix = [&](u32x4& t, u32 so) AI {
    if (RARE(__any(max4(t) == EMPTY))) {
#pragma unroll 1
      for (int it = 0; it < 4000; ++it) { FIX_COUNT(); t = ldg(dw, s2o, so); vmw<0>(t); if (!__any(max4(t) == EMPTY)) break; }
    }
  };
  SB();
  if (w >= 4) {
    epilogue();
    SB();
    colfin();
  } else {
    colfin();
    SB();
    const u32 ha = opq(L_H0 + 7 * w * 1024 + lo16);
#pragma unroll 1
    for (int it = 0; it < 4000; ++it) {
      VMW(0);
      u32 m = 0;
#pragma unroll
      for (int k = 0; k < 7; ++k) { const u32x4 v = *(const u32x4*)(lds + ha + k * 1024); m = max(m, max4(v)); }
      if (!__any(m == EMPTY)) break;
#pragma unroll
      for (int k = 0; k < 7; ++k) dma<SC1>(dw, lb + L_H0 + (7 * w + k) * 1024, lo16, WS_H + (u32)(2 * gi * TB) + wrap(7 * w + k + 2 * w1.rot, 28) * 1024);
    }
    VMW(0);
    STAMP(13);
  }
  __builtin_amdgcn_s_setprio(1);
  bar();
  STAMP(14);
#pragma unroll
  for (int k = 0; k < PF; ++k) B[k] = cat(trd(lds, ba + k * 1024), trd(lds, ba + k * 1024 + 256));
  SB();
  chain(0, accA, [&](int k) AI {
    if (k <= 6 && !(k & 1)) h_dma(1, k >> 1);
    if (k == TM0K) tm0 = ldg(dw, s2o, 0);
    if (k == 10 || k == 13) __builtin_amdgcn_raw_buffer_store_b128(u32x4{EMPTY, EMPTY, EMPTY, EMPTY}, rw, gso, gofs(k == 10 ? 2 : 3), SC1);
    if (k >= 16 && k <= 25 && k % 3 == 1) wd_dma(0, (k - 16) / 3);
    if (k == 23) { VMW(6); RBAR(); if (tid == 0 && b == 0) setflag(ws, WS_E, tag); }
    if (k == 27) { vmw<(TM0K > 13 ? 4 : 6)>(tm0); tm_fix(tm0, 0); }
  });
  center(accA, __uint_as_float(tm0.x));
  tm = __builtin_bit_cast(f32x4, tm0);
  SB();
  STAMP(16);
  chain(1, accB, [&](int k) AI {
    if (k == 1) RBAR();
    geglu_step(0, accA, k);
    if (k == 2) tm1 = ldg(dw, s2o, 512);
    if (k == 3 || k == 5) wd_dma(1, (k - 3) >> 1);
    if (k >= 7 && k <= 10) h_dma(2, k - 7);
    if (k == 12) { VMW(6); dma_at(lb + L_P + w * 4096); }
    if (k == 13 || k == 15) wd_dma(1, (k - 9) >> 1);
    if (k >= 12 && k <= 18 && !(k & 1)) wd_rd(0, (k - 12) >> 1);
    if (k == 17) { vmw<7>(tm1); tm_fix(tm1, 512); }
    if (k >= 21 && (k & 1)) wd_dma(2, (k - 21) >> 1);
    if (k == 23) { VMW(4); RBAR(); }
  });
  center(accB, __uint_as_float(tm1.x));
  geglu_step(0, accA, 28);
  tm = __builtin_bit_cast(f32x4, tm1);
  SB();
  STAMP(18);
  chain(2, accA, [&](int k) AI {
    if (k == 1) RBAR();
    if (k == 2) redo1(1, accB, tm1.x);
    geglu_step(1, accB, k);
    if (k <= 6 && !(k & 1)) h_dma(3, k >> 1);
    if (k >= 3 && k <= 9 && (k & 1)) { if (k == 3) VMW(7); wd_rd(1, (k - 3) >> 1); }
    if (k == 8) { tm2 = ldg(dw, s2o, 32 * 512); tm3 = ldg(dw, s2o, 33 * 512); }
    if (k >= 10 && k <= 16 && !(k & 1)) wd_dma(3, (k - 10) >> 1);
    if (k >= 18 && k <= 27 && k % 3 == 0) wd_dma(4, (k - 18) / 3);
    if (k == 13) { VMW(7); RBAR(); if (tid == 0) flag3(0); }
    if (k == 23) { vmw<6>(tm2); PIN(tm3); RBAR(); }
    if (k == 25) tm_fix(tm2, 32 * 512);
    if (k == 26) tm_fix(tm3, 33 * 512);
  });
  center(accA, __uint_as_float(tm2.x));
  geglu_step(1, accB, 28);
  tm = __builtin_bit_cast(f32x4, tm2);
  SB();
  STAMP(20);
  const u32 gb = WS_G + (u32)((2 * ri * 2 + rq2) * 56 + 7 * w) * 1024;
  constexpr int NS = 9;
  u32x4 Hf[NS];
  auto hl = [&](int t) AI { Hf[t % NS] = ldg(dw, lo16, gb + gofs(t / 7) + (t % 7) * 1024); };
  chain(3, accB, [&](int k) AI {
    if (k == 2) redo1(2, accA, tm2.x);
    geglu_step(2, accA, k);
    if (k >= 5 && k < 13) { if (k == 5) VMW(5); wd_rd(2 + (k - 5) / 4, (k - 5) & 3); }
    if (k == 10) fl = flag_ld(dw, f3o);
    if (k >= 12 && k <= 18 && !(k & 1)) wd_dma(5, (k - 12) >> 1);
    if (k == 13) { VMW(2); RBAR(); if (tid == 0) flag3(1); }
    if (k >= 13 && k < 21 && (k & 1)) wd_rd(4, (k - 13) >> 1);
    if (k >= 19 && k <= 22) wd_dma(6, k - 19);
    if (k == 23) { VMWP(8, fl); if (RARE(!__all(fl == tag))) poll(ws, f3o, true, tag); }
    if (k >= 23) hl(k - 23);
  });
  center(accB, __uint_as_float(tm3.x));
  geglu_step(2, accA, 28);
  tm = __builtin_bit_cast(f32x4, tm3);
  redo1(3, accB, tm3.x);
  STAMP(21);
#pragma unroll
  for (int e = 0; e < 8; ++e) {
    const float a = accB[e], u = accB[e + 8];
    gu[e] = (a * u) * __builtin_amdgcn_rcpf(__builtin_amdgcn_exp2f(a * (tm.w * (a * a) + tm.z)) * tm.y + tm.y);
  }
#pragma unroll
  for (int e = 0; e < 4; ++e) od[e] = pk(gu[2 * e], gu[2 * e + 1]);
  geglu_step(3, accB, 28);
  __builtin_amdgcn_s_setprio(0);
  SB();
  STAMP(22);
  f32x4 acc[2][4];
  const rsrc_t ro = mk(gout, S_ * D_ * 2);
  const u32 pw = (u32)(i16 * 256), tf = 4 * (w & 3) + G;
  const u32 hro = WS_H + (u32)((32 * (G >> 1) + (G & 1) + 2 * ri) * TB + (64 * n2 + 8 * w + (i16 >> 1)) * 64 + rq2 * 32 + (i16 & 1) * 16);
  auto pbuf = [](int g) AI { return g == 3 ? L_T : g == 1 ? L_Q : L_P; };
  auto put = [&](int g) AI {
    const u32 rb = opq(pbuf(g) + w * 4096 + pw);
#pragma unroll
    for (int nt = 0; nt < 4; ++nt) *(f32x4*)(lds + rb + (((4 * nt + G) ^ (i16 & 7)) << 4)) = acc[g & 1][nt];
  };
  auto fin = [&](int g) AI {
    if ((w >> 2) == (g & 1)) {
      const u32 a0 = tf * 256 + ((i16 ^ (tf & 7)) << 4), a = opq(pbuf(g) + a0);
      f32x4 p[9];
      p[8] = *(const f32x4*)(lds + L_RES + g * 4096 + a0);
#pragma unroll
      for (int n = 0; n < 8; ++n) p[n] = *(const f32x4*)(lds + a + n * 4096);
      const f32x4 sum = (((p[0] + p[1]) + (p[2] + p[3])) + ((p[4] + p[5]) + (p[6] + p[7]))) + p[8];
      const int tok = 32 * (32 * (g >> 1) + (g & 1) + 2 * ri) + 16 * rq2 + tf;
      __builtin_amdgcn_raw_buffer_store_b64(u32x2{pk(sum.x, sum.y), pk(sum.z, sum.w)}, ro, (u32)((tok * D_ + 64 * n2 + 4 * i16) * 2), 0, 2);
    }
  };
  auto redo = [&](int g) AI {
    if (RARE(__any(acc[g & 1][0].x != acc[g & 1][0].x))) {
      REDO_COUNT();
      u32x4 v[7];
#pragma unroll 1
      for (int it = 0; it < 4000; ++it) {
        u32 m = 0;
#pragma unroll
        for (int k = 0; k < 7; ++k) v[k] = ldg(dw, lo16, gb + gofs(g) + k * 1024);
#pragma unroll
        for (int k = 0; k < 7; ++k) { vmw<0>(v[k]); m = max(m, max4(v[k])); }
        if (!__any(m == EMPTY)) break;
      }
#pragma unroll
      for (int k = 0; k < 7; ++k)
#pragma unroll
        for (int nt = 0; nt < 4; ++nt) acc[g & 1][nt] = mfma16(Wd[k][nt], v[k], k == 0 ? f32x4{} : acc[g & 1][nt]);
    }
  };
  fl = flag_ld(dw, f3o + 14 * 128);
  u32x4 hr = ldg(dw, hro, 0);
  hl(5); hl(6);
  VMW(11);
  wd_read(5); wd_read(6);
  SB();
  STAMP(23);
  sfor<28>([&](auto tc) AI {
    constexpr int t = decltype(tc)::value, g = t / 7, k = t % 7;
    constexpr auto pos = [](int h) { return h < 5 ? h : h + 4; };
    constexpr auto last = [](int u) { return u < 2 ? 6 : u == 2 ? 8 : u == 3 ? 10 : u < 21 ? u + 7 : 27; };
    if (t == 2) { VMWP(3, fl); if (RARE(!__all(fl == tag))) poll(ws, f3o + 14 * 128, true, tag); }
#pragma unroll
    for (int h = (t ? last(t - 1) : 6) + 1; h <= last(t); ++h) hl(h);
    vmw<pos(last(t)) - pos(t)>(Hf[t % NS]);
#pragma unroll
    for (int nt = 0; nt < 4; ++nt) acc[g & 1][nt] = mfma16(Wd[k][nt], Hf[t % NS], k == 0 ? f32x4{} : acc[g & 1][nt]);
    SB();
    if (t == 3) {
      vmw<6>(hr);
      if (RARE(__any(max4(hr) == EMPTY))) {
#pragma unroll 1
        for (int it = 0; it < 4000; ++it) { hr = ldg(dw, hro, 0); vmw<0>(hr); if (!__any(max4(hr) == EMPTY)) break; }
      }
      *(u32x4*)(lds + blkR + lo16) = hr;
#pragma unroll
      for (int q = 0; q < 4; ++q) {
        const u32x2 r = trd(lds, blkR + q * 256 + (4 * (G & 1) + q4) * 32 + p4 * 8);
        *(f32x4*)(lds + L_RES + q * 4096 + pw + (((2 * w + (G & 1)) ^ (i16 & 7)) << 4)) = f32x4{bflo(r.x), bfhi(r.x), bflo(r.y), bfhi(r.y)};
      }
    }
    if (t == 8) put(0);
    if (t == 12) { bar(); STAMP(24); fin(0); }
    if (t == 15) put(1);
    if (t == 19) { bar(); STAMP(25); fin(1); }
    if (t == 22) { redo(2); put(2); STAMP(26); }
    SB();
  });
  redo(3);
  put(3);
  bar();
  STAMP(27);
  fin(2); fin(3);
  STAMP(28);
  STAMP_OUT();
}

}

extern "C" void kernel_launch(void* const* d_in, const int* in_sizes, int n_in, void* d_out, int out_size, void* d_ws, size_t ws_size,
                              hipStream_t stream) {
  h3_ffn<<<256, 512, 0, stream>>>((const char*)d_in[0], (const char*)d_in[1], (const char*)d_in[2], (const char*)d_in[3], (const char*)d_in[4],
                                  (const char*)d_in[5], (const char*)d_in[7], (const char*)d_in[8], (const char*)d_in[9], (char*)d_out, (char*)d_ws);
}
```

```cpp
#include <hip/hip_runtime.h>
#include <stdint.h>

namespace {

#define LAS __attribute__((address_space(3)))
#define GAS __attribute__((address_space(1)))
#define DI __device__ __forceinline__
#define AI __attribute__((always_inline))
#define RARE(x) __builtin_expect(!!(x), 0)
typedef uint32_t u32;
typedef unsigned long long u64;
typedef u32 u32x2 __attribute__((ext_vector_type(2)));
typedef u32 u32x4 __attribute__((ext_vector_type(4)));
typedef short s16x4 __attribute__((ext_vector_type(4)));
typedef short s16x8 __attribute__((ext_vector_type(8)));
typedef float f32x4 __attribute__((ext_vector_type(4)));
typedef float f32x16 __attribute__((ext_vector_type(16)));
typedef __bf16 bf2 __attribute__((ext_vector_type(2)));
typedef __amdgpu_buffer_rsrc_t rsrc_t;
#define RLX_AGENT __ATOMIC_RELAXED, __HIP_MEMORY_SCOPE_AGENT

constexpr int S_ = 2048, D_ = 448, F_ = 1792;
constexpr u32 TB = 448 * 64;
constexpr u32 WS_E  = 0;
constexpr u32 WS_F1 = 128;
constexpr u32 WS_F3 = WS_F1 + 256 * 128;
constexpr u32 WS_F2 = WS_F3 + 896 * 128;
constexpr u32 WS_S2 = 256 * 1024 + 16384;
constexpr u32 WS_ST = 256 * 1024;
constexpr u32 WS_U  = WS_S2 + 32768;
constexpr u32 WS_X  = WS_U + 64 * TB;
constexpr u32 WS_H  = WS_X + 64 * TB;
constexpr u32 WS_G  = WS_H + 64 * TB;
constexpr u32 WS_END = WS_G + 64 * 112 * 1024;
constexpr u32 WS_DBG = 16u << 20;
constexpr int SC1 = 16;
#define HELP_DELAY 8
#define TM0K 8
#define ROT1 (7 * (xc & 1) + ((7 * (gx & 3)) >> 2))
#define CODEPF 24576
#define PRETOUCH 0
constexpr u32 OOB = 0xFFFFFF00u;
constexpr u32 ONES = 0x3f803f80u;
constexpr u32 EMPTY = 0xffffffffu;

constexpr u32 L_K = 0, L_V = 9216, L_X = 17408, L_SK = 25600;
constexpr u32 L_RED = 0, L_FC = 32768, FCS = 4160, FCH = 4 * FCS;
constexpr u32 L_U = L_FC + 2 * FCH, UCH = 4608, L_RING = L_U + 2 * UCH;
constexpr u32 L_H0 = 32768, L_H1 = L_H0 + TB, L_H2 = 0, L_H3 = L_H1 + TB + 32768, L_SP = 155648;
constexpr u32 L_P = TB, L_Q = L_P + 32768, L_R = L_Q + 32768, L_R2 = L_H3 + TB;
constexpr u32 L_RES = 0, L_T = L_H3;
constexpr u32 LDS_BYTES = 163840;

DI float bflo(u32 x) { return __uint_as_float(x << 16); }
DI float bfhi(u32 x) { return __uint_as_float(x & 0xffff0000u); }
DI u32 pk(float lo, float hi) { bf2 p; p.x = (__bf16)lo; p.y = (__bf16)hi; return __builtin_bit_cast(u32, p); }
DI rsrc_t mk(const void* p, u32 bytes) { return __builtin_amdgcn_make_buffer_rsrc((void*)p, 0, bytes, 0x00020000); }
DI u32x2 trd(const char* lds, u32 a) { return __builtin_bit_cast(u32x2, __builtin_amdgcn_ds_read_tr16_b64_v4i16((LAS s16x4*)(lds + a))); }
DI u32x4 cat(u32x2 a, u32x2 b) { return u32x4{a.x, a.y, b.x, b.y}; }
DI u32x4 srd(const void* p, u32 bytes) { const u64 a = (u64)p; return u32x4{(u32)a, (u32)(a >> 32) & 0xffffu, bytes, 0x00020000u}; }
template <int AUX = 0> DI void dma(u32x4 r, u32 ldsoff, u32 voff, u32 soff) {
  if (AUX) asm volatile("s_mov_b32 m0, %0\n\ts_nop 0\n\tbuffer_load_dwordx4 %1, %2, %3 offen sc1 lds" :: "s"(ldsoff), "v"(voff), "s"(r), "s"(soff) : "memory");
  else asm volatile("s_mov_b32 m0, %0\n\ts_nop 0\n\tbuffer_load_dwordx4 %1, %2, %3 offen lds" :: "s"(ldsoff), "v"(voff), "s"(r), "s"(soff) : "memory");
}
DI void dma_at(u32 ldsoff) { asm volatile("s_mov_b32 m0, %0\n\ts_nop 0" :: "s"(ldsoff) : "memory"); }
template <int OFF, int AUX> DI void dma_go(u32x4 r, u32 voff, u32 soff) {
  if (AUX) asm volatile("buffer_load_dwordx4 %0, %1, %2 offen offset:%3 sc1 lds" :: "v"(voff), "s"(r), "s"(soff), "n"(OFF) : "memory");
  else asm volatile("buffer_load_dwordx4 %0, %1, %2 offen offset:%3 lds" :: "v"(voff), "s"(r), "s"(soff), "n"(OFF) : "memory");
}
template <int AUX> DI void dma_i(int i, u32x4 r, u32 voff, u32 soff) {
  if (i == 0) dma_go<0, AUX>(r, voff, soff); else if (i == 1) dma_go<1024, AUX>(r, voff, soff); else if (i == 2) dma_go<2048, AUX>(r, voff, soff); else dma_go<3072, AUX>(r, voff, soff);
}
DI float fnma(float a, float b, float c) { float r; asm("v_fma_f32 %0, -%1, %2, %3" : "=v"(r) : "v"(a), "v"(b), "v"(c)); return r; }
DI float op(float x) { asm("" : "+v"(x)); return x; }
DI u32 opq(u32 x) { asm volatile("" : "+v"(x)); return x; }
#define PIN(x) asm volatile("" : "+v"(x))
DI f32x4 mfma16(u32x4 a, u32x4 b, f32x4 c) { return __builtin_amdgcn_mfma_f32_16x16x32_bf16(__builtin_bit_cast(s16x8, a), __builtin_bit_cast(s16x8, b), c, 0, 0, 0); }
DI f32x16 mfma32(u32x4 a, u32x4 b, f32x16 c) { return __builtin_amdgcn_mfma_f32_32x32x16_bf16(__builtin_bit_cast(s16x8, a), __builtin_bit_cast(s16x8, b), c, 0, 0, 0); }
DI f32x4 mfma4(u32x2 a, u32x2 b, f32x4 c) { return __builtin_amdgcn_mfma_f32_4x4x4bf16_1k(__builtin_bit_cast(s16x4, a), __builtin_bit_cast(s16x4, b), c, 0, 0, 0); }
DI float dot2(u32 a, u32 b, float c) { return __builtin_amdgcn_fdot2_f32_bf16(__builtin_bit_cast(bf2, a), __builtin_bit_cast(bf2, b), c, false); }
#define VMW(n) asm volatile("s_waitcnt vmcnt(%0)" :: "n"(n) : "memory")
#define LG0() asm volatile("s_waitcnt lgkmcnt(0)" ::: "memory")
#define SB() __builtin_amdgcn_sched_barrier(0)
DI void bar() { asm volatile("s_waitcnt lgkmcnt(0)\n\ts_barrier" ::: "memory"); }
#define RBAR() asm volatile("s_barrier" ::: "memory")
DI float wsum(float v) {
  v += __builtin_amdgcn_update_dpp(0.f, v, 0x111, 0xf, 0xf, true);
  v += __builtin_amdgcn_update_dpp(0.f, v, 0x112, 0xf, 0xf, true);
  v += __builtin_amdgcn_update_dpp(0.f, v, 0x114, 0xf, 0xf, true);
  v += __builtin_amdgcn_update_dpp(0.f, v, 0x118, 0xf, 0xf, true);
  v += __builtin_amdgcn_update_dpp(0.f, v, 0x142, 0xa, 0xf, true);
  v += __builtin_amdgcn_update_dpp(0.f, v, 0x143, 0xc, 0xf, true);
  return __uint_as_float(__builtin_amdgcn_readlane(__float_as_uint(v), 63));
}
DI float hsum(float v) {
  const auto r = __builtin_amdgcn_permlane32_swap(__float_as_uint(v), __float_as_uint(v), false, false);
  return __uint_as_float(r[0]) + __uint_as_float(r[1]);
}
template <int SLEEP = 1> DI void poll(char* ws, u32 off, bool act, u64 tag) {
  bool ok = !act;
#pragma unroll 1
  for (int it = 0; it < 20000; ++it) {
    u64 x = tag;
    if (!ok) x = __hip_atomic_load((GAS u64*)(ws + off), RLX_AGENT);
    ok = (x == tag);
    if (__all(ok)) break;
    __builtin_amdgcn_s_sleep(SLEEP);
  }
  asm volatile("" ::: "memory");
}
DI u32 max4(u32x4 v) { return max(max(v.x, v.y), max(v.z, v.w)); }
DI u64 flag_ld(u32x4 r, u32 voff) { u64 v; asm volatile("buffer_load_dwordx2 %0, %1, %2, 0 offen sc1" : "=v"(v) : "v"(voff), "s"(r) : "memory"); return v; }
DI u32x4 ldg(u32x4 r, u32 voff, u32 soff) { u32x4 v; asm volatile("buffer_load_dwordx4 %0, %1, %2, %3 offen sc1" : "=v"(v) : "v"(voff), "s"(r), "s"(soff) : "memory"); return v; }
template <int N> DI void vmw(u32x4& x) { asm volatile("s_waitcnt vmcnt(%1)" : "+v"(x) : "n"(N) : "memory"); }
#define VMWP(n, x) asm volatile("s_waitcnt vmcnt(" #n ")" : "+v"(x) :: "memory")
DI void setflag(char* ws, u32 off, u64 tag) { __hip_atomic_store((GAS u64*)(ws + off), tag, RLX_AGENT); }

#define STAMP(i)
#define STAMPC(i)
#define REDO_COUNT()
#define FIX_COUNT()
#define STAMP_OUT()

DI float xsum(float a, float c) {
  const auto r = __builtin_amdgcn_permlane32_swap(__float_as_uint(a), __float_as_uint(c), false, false);
  return __uint_as_float(r[0]) + __uint_as_float(r[1]);
}
DI f32x4 ln2_terms(float t) {
  const auto r = __builtin_amdgcn_permlane32_swap(__float_as_uint(t), __float_as_uint(t), false, false);
  const float mean = __uint_as_float(r[0]) * (1.f / D_);
  const float V = fmaxf(__uint_as_float(r[1]) * (1.f / D_) - mean * mean, 0.f) + 1e-5f;
  const float rs = __builtin_amdgcn_rsqf(V);
  const float K1 = -2.f * 0.7978845608028654f * 1.4426950408889634f * rs;
  const float lo = -mean - bflo(pk(-mean, 0.f));
  return f32x4{__uint_as_float(pk(-mean, lo)), V, K1, (K1 * rs) * (0.044715f * rs)};
}
template <int N> struct IC { static constexpr int value = N; };
template <int N, int I = 0, class F> DI void sfor(F&& f) { if constexpr (I < N) { f(IC<I>{}); sfor<N, I + 1>(f); } }
struct W1 { u32x4 rg, ru; u32 vg, vu, ra, lb, rot; };
DI u32 wrap(u32 x, u32 n) { return x >= n ? x - n : x; }
template <int T> DI void w1_g(const W1& c, u32 base) { dma(c.rg, c.lb + base, c.vg, wrap(T + c.rot, 14) * (32 * F_ * 2)); }
template <int T> DI void w1_u(const W1& c, u32 base) { dma(c.ru, c.lb + base + 8192, c.vu, wrap(T + c.rot, 14) * (32 * F_ * 2)); }
template <int T> DI void w1_issue(const W1& c, u32 base) { w1_g<T>(c, base); w1_u<T>(c, base); }
template <int T> DI void w1_read(const char* lds, u32 a, u32x4 (&W)[28]) {
#pragma unroll
  for (int kk = 0; kk < 2; ++kk) W[2 * T + kk] = cat(trd(lds, a + kk * 4096), trd(lds, a + kk * 4096 + 1024));
}
DI float x8sum(float v) {
  v += __builtin_amdgcn_update_dpp(0.f, v, 0xb1, 0xf, 0xf, true);
  v += __builtin_amdgcn_update_dpp(0.f, v, 0x4e, 0xf, 0xf, true);
  v += __builtin_amdgcn_update_dpp(0.f, v, 0x141, 0xf, 0xf, true);
  return v;
}

__global__ __launch_bounds__(512, 2) void h3_ffn(const char* __restrict__ gq, const char* __restrict__ gk, const char* __restrict__ gv,
                                                 const char* __restrict__ gsk, const char* __restrict__ gf, const char* __restrict__ gD,
                                                 const char* __restrict__ gwg, const char* __restrict__ gwu, const char* __restrict__ gwd,
                                                 char* __restrict__ gout, char* ws) {
  __shared__ __attribute__((aligned(16))) char lds[LDS_BYTES];
  const int tid = threadIdx.x, l = tid & 63, w = __builtin_amdgcn_readfirstlane(tid >> 6);
  const int b = blockIdx.x, xc = b & 7, sl = b >> 3;
  const int tb64 = 4 * xc + (sl >> 3), role = sl & 7;
  const int sc = (sl >> 3) * 7 + role;
  asm volatile("" :: "s"(gq), "s"(gk), "s"(gv), "s"(gsk), "s"(gf), "s"(gD), "s"(gwg), "s"(gwu), "s"(gwd), "s"(gout), "s"(ws));
  STAMP(0);
  u64 pc0; asm volatile("s_getpc_b64 %0" : "=s"(pc0));
  const rsrc_t rw = mk(ws, WS_END);
  const int G = l >> 4, i16 = l & 15, q4 = i16 >> 2, p4 = l & 3, r8 = l >> 3;
  auto mktag = [&](u64 e0) AI { return ((u64)__builtin_amdgcn_readfirstlane((u32)e0) | ((u64)__builtin_amdgcn_readfirstlane((u32)(e0 >> 32)) << 32)) * 6364136223846793005ull + 1442695040888963407ull; };
  if (RARE(role == 7)) {
    u32 ipf = 0;
    if (w >= 3 && w < 7) asm volatile("global_load_dword %0, %1, off" : "=v"(ipf) : "v"((pc0 & ~255ull) + (u64)min((u32)((w - 3) * 64 + l) * 128u, (u32)CODEPF)) : "memory");
    const rsrc_t rq = mk(gq, S_ * D_ * 2);
    const int row = 64 * tb64 + 8 * w + r8;
    u32x4 xs[7];
#pragma unroll
    for (int i = 0; i < 7; ++i) xs[i] = __builtin_amdgcn_raw_buffer_load_b128(rq, (u32)(row * 896 + (l & 7) * 16 + i * 128), 0, 0);
    const u64 e0 = __hip_atomic_load((GAS u64*)(ws + WS_E), RLX_AGENT);
    __builtin_amdgcn_raw_buffer_store_b128(u32x4{EMPTY, EMPTY, EMPTY, EMPTY}, rw, tid < 64 ? WS_S2 + (u32)((tb64 + (tid & 32)) * 512 + (tid & 31) * 16) : OOB, 0, SC1);
    SB();
    u32 tlb = 0;
    if (w == 7) {
      const u32 n = (WS_END + 1048576 - 1) / 1048576, m2 = (S_ * D_ * 2 + 1048576 - 1) / 1048576;
      const u32 i = min((u32)l, n + 4 * m2 - 1);
      const char* p = i < n ? ws + (size_t)i * 1048576 + 64 : (i < n + m2 ? (const char*)gout : i < n + 2 * m2 ? gwd : i < n + 3 * m2 ? gwg : gwu) + ((i - n) % m2) * 1048576;
      asm volatile("global_load_dword %0, %1, off" : "=v"(tlb) : "v"(p) : "memory");
    }
    SB();
    const u64 tag = mktag(e0);
    float s[4] = {};
#pragma unroll
    for (int i = 0; i < 7; ++i)
#pragma unroll
      for (int e = 0; e < 4; ++e) s[e] += bflo(xs[i][e]) + bfhi(xs[i][e]);
    const float mean = x8sum((s[0] + s[1]) + (s[2] + s[3])) * (1.f / D_);
    float v[4] = {};
#pragma unroll
    for (int i = 0; i < 7; ++i)
#pragma unroll
      for (int e = 0; e < 4; ++e) { const float d0 = bflo(xs[i][e]) - mean, d1 = bfhi(xs[i][e]) - mean; v[e] += d0 * d0; v[e] += d1 * d1; }
    const float rstd = __builtin_amdgcn_rsqf(x8sum((v[0] + v[1]) + (v[2] + v[3])) * (1.f / D_) + 1e-5f);
    if ((l & 7) == 0) *(u32x2*)(lds + ((r8 >> 1) & 3) * 128 + w * 16 + (r8 & 1) * 8) = u32x2{__float_as_uint(mean), __float_as_uint(rstd)};
    bar();
    if (w == 0) {
      __builtin_amdgcn_raw_buffer_store_b128(*(const u32x4*)(lds + (l & 31) * 16), rw, l < 32 ? WS_ST + (u32)((tb64 >> 2) * 2048 + (l >> 3) * 512 + (tb64 & 3) * 128 + (l & 7) * 16) : OOB, 0, SC1);
      VMW(0);
    }
    if (tid == 0) setflag(ws, WS_F1 + (tb64 * 8 + 7) * 128, tag);
    bar();
#pragma unroll
    for (int i = 0; i < 7; ++i) {
      const u32 o = (u32)(i * 8192 + tid * 16);
      __builtin_amdgcn_raw_buffer_store_b128(u32x4{EMPTY, EMPTY, EMPTY, EMPTY}, rw, o < TB ? o : o + 31 * TB, WS_H + (u32)tb64 * TB, SC1);
    }
    VMW(0); PIN(tlb); PIN(ipf); bar();
    if (tid == 0) setflag(ws, WS_F2 + tb64 * 128, tag);
    STAMP(1);
    {
      const rsrc_t rdw = mk(gwd, D_ * F_ * 2);
      const u32 t = __builtin_amdgcn_raw_buffer_load_b32(rdw, tid < 392 ? (u32)((tb64 * 392 + tid) * 128) : OOB, 0, 0);
      asm volatile("" :: "v"(t));
    }
    STAMP(2);
    const u32x4 dw = srd(ws, WS_END);
    const u32 lb = (u32)(size_t)(LAS char*)lds;
    __builtin_amdgcn_s_sleep(HELP_DELAY);
#pragma unroll 1
    for (int half = 0; half < 2; ++half) {
      const int tb = 32 * half + tb64;
#pragma unroll 1
      for (int it = 0; it < 4000; ++it) {
#pragma unroll
        for (int k = 0; k < 4; ++k) if (k < 3 || l < 32) dma<SC1>(dw, lb + 32768 + w * 3584 + k * 1024, l * 16, WS_H + (u32)(tb * TB + w * 3584 + k * 1024));
        VMW(0);
        u32 m = 0;
#pragma unroll
        for (int k = 0; k < 4; ++k) m = max(m, max4(*(const u32x4*)(lds + 32768 + w * 3584 + (k < 3 ? k * 1024 + l * 16 : 3072 + (l & 31) * 16))));
        if (!__any(m == EMPTY)) break;
        __builtin_amdgcn_s_sleep(3);
      }
      if (half == 0) { STAMP(4); } else { STAMP(6); }
      float s1 = 0.f, s2 = 0.f;
#pragma unroll
      for (int i = 0; i < 7; ++i) {
        const u32x2 t = trd(lds, 32768 + (u32)((56 * w + 4 * (G >> 1) + q4) * 64 + (G & 1) * 32 + p4 * 8) + i * 512);
        s1 = dot2(t.x, ONES, s1); s2 = dot2(t.x, t.x, s2); s1 = dot2(t.y, ONES, s1); s2 = dot2(t.y, t.y, s2);
      }
      *(float*)(lds + 20480 + half * 2048 + l * 32 + w * 4) = xsum(s1, s2);
      bar();
      if (w == 0) {
        const f32x4 a = *(const f32x4*)(lds + 20480 + half * 2048 + l * 32), c = *(const f32x4*)(lds + 20480 + half * 2048 + l * 32 + 16);
        __builtin_amdgcn_raw_buffer_store_b128(__builtin_bit_cast(u32x4, ln2_terms(((a.x + a.y) + (a.z + a.w)) + ((c.x + c.y) + (c.z + c.w)))), rw, l < 32 ? WS_S2 + (u32)(tb * 512 + l * 16) : OOB, 0, SC1);
      }
      if (half == 0) { STAMP(5); }
    }
    STAMP(3);
    STAMP_OUT();
    return;
  }
  const int cp = xc * 28 + sc, cch = w & 1, vp = w >> 1;
  const int ech = (l >> 2) & 1;
  const u32 eo = (u32)((8 * w + r8) * TB + (2 * cp + ech) * 64 + p4 * 16);
  u32x4 fhi; u32x2 flo; u32 Dh; u64 e0;
  const u32 lb = (u32)(size_t)(LAS char*)lds;
  const u32x4 dw = srd(ws, WS_END);
  {
    const u32x4 rk = srd(gk, S_ * D_ * 2), rv = srd(gv, S_ * D_ * 2);
    const int c8 = l & 7, s0 = 64 * tb64;
    const int swv = ((r8 >> 1) & 1) | ((w & 1) << 1), swk = swv ^ 2;
    const u32 g0 = (u32)((s0 + 8 * w + r8) * 896 + role * 128);
    if (w == 0) dma(rk, lb + L_K, g0 - 8 * 896 + ((c8 ^ (swv << 1)) << 4), 0);
    if (w == 1) dma(srd(gsk, D_ * 8), lb + L_SK, (u32)(512 * role + 16 * l), 0);
    dma(rk, lb + L_K + 1024 + w * 1024, g0 + ((c8 ^ (swk << 1)) << 4), 0);
    dma(rv, lb + L_V + w * 1024, g0 + ((c8 ^ (swv << 1)) << 4), 0);
    dma(srd(gq, S_ * D_ * 2), lb + L_X + w * 1024, g0 + ((c8 ^ (swv << 1)) << 4), 0);
    const u32x4 rf = srd(gf, S_ * D_ * 2);
    const int fq = vp * 64 + l;
    const u32 fo = (u32)((2 * cp + cch) * 4096 + 16 * fq);
    asm volatile("buffer_load_dwordx4 %0, %2, %4, 0 offen\n\tbuffer_load_dwordx2 %1, %3, %4, 0 offen" : "=&v"(fhi), "=&v"(flo) : "v"(fo), "v"(fq > 0 ? fo - 8 : OOB), "s"(rf) : "memory");
    asm volatile("buffer_load_ushort %0, %1, %2, 0 offen" : "=v"(Dh) : "v"((u32)((2 * cp + ((l >> 2) & 1)) * 2)), "s"(srd(gD, D_ * 2)) : "memory");
    e0 = flag_ld(dw, WS_E);
    SB();
  }
  const int gx = (xc >> 1) * 28 + sc, gi = 8 * (xc & 1) + (gx & 7), gj = gx >> 3;
  W1 w1;
  {
    const u32 v0 = (u32)(((4 * w + G) * F_ + 128 * gj) * 2);
    w1.rg = srd(gwg, D_ * F_ * 2); w1.ru = srd(gwu, D_ * F_ * 2); w1.lb = lb + w * 1024;
    w1.rot = ROT1;
    w1.vg = v0 + ((i16 ^ (4 * G)) << 4); w1.vu = v0 + ((i16 ^ (4 * G + 2)) << 4);
    w1.ra = (u32)((G & 1) * 8192 + (8 * (G >> 1) + q4) * 256 + ((w ^ (2 * q4 + (G & 1))) * 32) + p4 * 8);
  }
  w1_issue<0>(w1, L_FC); w1_issue<1>(w1, L_FC + 16384);
  VMW(9); bar();
  STAMP(1);
  {
    const int tbl = w >> 2, chg = w & 3;
    const u32 rb = (u32)((32 * tbl + 8 * G + q4) * 128 + p4 * 8);
    const u32 sv = (u32)((chg ^ ((q4 >> 1) | ((G & 1) << 1))) * 32), sk2 = sv ^ 64;
    const u32x2 km = trd(lds, L_K + rb + 512 + sv), k0 = trd(lds, L_K + rb + 1024 + sk2), k1 = trd(lds, L_K + rb + 1536 + sk2);
    const u32x2 v0 = trd(lds, L_V + rb + sv), v1 = trd(lds, L_V + rb + 512 + sv);
    const u32x2 skv = *(const u32x2*)(lds + L_SK + (16 * chg + i16) * 8);
    const float kk[11] = {bfhi(km.x), bflo(km.y), bfhi(km.y), bflo(k0.x), bfhi(k0.x), bflo(k0.y), bfhi(k0.y), bflo(k1.x), bfhi(k1.x), bflo(k1.y), bfhi(k1.y)};
    const float vv[8] = {bflo(v0.x), bfhi(v0.x), bflo(v0.y), bfhi(v0.y), bflo(v1.x), bfhi(v1.x), bflo(v1.y), bfhi(v1.y)};
    const float s0 = bflo(skv.x), s1 = bfhi(skv.x), s2 = bflo(skv.y), s3 = bfhi(skv.y);
    float u[8];
#pragma unroll
    for (int e = 0; e < 8; ++e) u[e] = (kk[e + 3] * s0 + kk[e + 2] * s1 + kk[e + 1] * s2 + kk[e] * s3) * vv[e];
    const u32 o = (u32)((2 * tb64 + tbl) * TB + (64 * role + 16 * chg + i16) * 64 + G * 16);
    __builtin_amdgcn_raw_buffer_store_b128(u32x4{pk(u[0], u[1]), pk(u[2], u[3]), pk(u[4], u[5]), pk(u[6], u[7])}, rw, WS_U + o, 0, SC1);
    SB();
    STAMP(2);
    VMW(9); bar();
    const u32x2 x0 = trd(lds, L_X + rb + sv), x1 = trd(lds, L_X + rb + 512 + sv);
    __builtin_amdgcn_raw_buffer_store_b128(cat(x0, x1), rw, WS_X + o, 0, SC1);
    VMW(6); PIN(fhi); PIN(flo); PIN(Dh); PIN(e0);
    SB();
  }
  STAMP(3);
  const u64 tag = mktag(e0);
  const float Dv = bflo(Dh);
  SB();
  w1_issue<2>(w1, L_RING); w1_issue<3>(w1, L_RING + 16384);
  STAMP(4);
  VMW(4); bar();
  if (tid == 0) setflag(ws, WS_F1 + (tb64 * 8 + role) * 128, tag);
  STAMP(5);
  const int hh = l >> 5, tk = l & 31;
  const u32 gso = WS_G + (u32)(((2 * gi * 2 + (tk >> 4)) * 56 + 4 * gj + (w >> 1)) * 1024 + (2 * (w & 1) + hh) * 256 + i16 * 16);
  auto gofs = [](int g) AI { return (u32)((32 * (g >> 1) + (g & 1)) * 2 * 56 * 1024); };
  u32x4 W[28];
  f32x4 cs0 = {}, cs1 = {};
  auto colsum = [&](int k0, int k1) AI {
#pragma unroll
    for (int k = k0; k < k1; ++k) { cs0 = mfma4(u32x2{ONES, ONES}, u32x2{W[k].x, W[k].y}, cs0); cs1 = mfma4(u32x2{ONES, ONES}, u32x2{W[k].z, W[k].w}, cs1); }
  };
  const u32 ra0 = opq(w1.ra), ra1 = opq(w1.ra + L_RING), ra2 = opq(w1.ra + L_RING + 49152);
  w1_read<0>(lds, ra0 + L_FC, W); w1_read<1>(lds, ra0 + L_FC + 16384, W);
  w1_issue<7>(w1, L_RED); w1_issue<8>(w1, L_RED + 16384);
  bar();
  {
    const u32 a0 = __builtin_amdgcn_alignbit(flo.y, flo.x, 16), a1 = __builtin_amdgcn_alignbit(fhi.x, flo.y, 16), a2 = __builtin_amdgcn_alignbit(fhi.y, fhi.x, 16),
              a3 = __builtin_amdgcn_alignbit(fhi.z, fhi.y, 16), a4 = __builtin_amdgcn_alignbit(fhi.w, fhi.z, 16);
    char* fc = lds + L_FC + cch * FCH + (vp * 64 + l) * 16 + 32;
    *(u32x4*)(fc) = fhi;
    *(u32x4*)(fc + FCS) = u32x4{a1, a2, a3, a4};
    *(u32x4*)(fc + 2 * FCS) = u32x4{flo.y, fhi.x, fhi.y, fhi.z};
    *(u32x4*)(fc + 3 * FCS) = u32x4{a0, a1, a2, a3};
    if (tid >= 128 && tid < 144) *(u32x4*)(lds + L_FC + (tid & 7) * FCS + ((tid >> 3) & 1) * 16) = u32x4{0, 0, 0, 0};
    if (tid >= 192 && tid < 252) { const int z = tid - 192; *(u32x4*)(lds + L_U + (z / 30) * UCH + (z % 30) * 16) = u32x4{0, 0, 0, 0}; }
  }
  SB();
  colsum(0, 4);
  w1_issue<4>(w1, L_RING + 32768); w1_issue<5>(w1, L_RING + 49152); w1_issue<6>(w1, L_RING + 65536);
  SB();

  STAMP(6);
  poll(ws, WS_F1 + (u32)(32 * w + 8 * (l & 3) + ((l & 4) ? 7 : (cp >> 5))) * 128, l < 8, tag);
  STAMP(7);
  u32x4 uu = ldg(dw, eo, WS_U), ex = ldg(dw, eo, WS_X);
  u32x4 est[4];
#pragma unroll
  for (int i = 0; i < 4; ++i) est[i] = ldg(dw, (u32)(r8 * 64 + p4 * 16), WS_ST + (u32)(w * 2048 + i * 512));
  bar();
  w1_read<7>(lds, ra0 + L_RED, W); w1_read<8>(lds, ra0 + L_RED + 16384, W);
  w1_read<2>(lds, ra1, W); w1_read<3>(lds, ra1 + 16384, W); w1_read<4>(lds, ra1 + 32768, W); w1_read<5>(lds, ra2, W); w1_read<6>(lds, ra2 + 16384, W);
  const u32 aA = opq(L_FC + cch * FCH + q4 * FCS + 64 - 16 * G + 8 * p4 - 8 + 128 * vp);
  const u32 aB = opq(L_U + cch * UCH + 32 * i16 + 16 * G + 448 - 128 * vp - 64);
  const u32 rwr = opq(L_RED + w * 4096 + G * 256 + ((i16 ^ ((G >> 1) << 3)) << 4));
  const u32 rr = opq(L_RED + ech * 4096 + (w & 3) * 1024 + (p4 & 1) * 512 + (((2 * r8 + (p4 >> 1)) ^ ((p4 & 1) << 3)) << 4));
  colsum(4, 18);
  PIN(cs0); PIN(cs1);
  SB();
  vmw<5>(uu);
  *(u32x4*)(lds + L_U + ech * UCH + 480 + (8 * w + r8) * 64 + p4 * 16) = uu;
  bar();
  STAMP(8);
  auto rdA = [&](int v, int m) AI { const u32 o = 64 * v + 512 * m; return cat(trd(lds, aA + o + 8), trd(lds, aA + o)); };
  auto rdB = [&](int v, int m) AI { return *(const u32x4*)(lds + aB + 64 * (1 - v) + 512 * m); };
  float ex8[8], eq8[8], eu8[8];
  auto terms = [&]() AI {
#pragma unroll
    for (int i = 0; i < 4; ++i) {
      ex8[2 * i] = bflo(ex[i]); ex8[2 * i + 1] = bfhi(ex[i]);
      eu8[2 * i] = bflo(uu[i]) * Dv; eu8[2 * i + 1] = op(bfhi(uu[i]) * Dv);
      eq8[2 * i] = (ex8[2 * i] - __uint_as_float(est[i].x)) * __uint_as_float(est[i].y);
      eq8[2 * i + 1] = op(op(ex8[2 * i + 1] - __uint_as_float(est[i].z)) * __uint_as_float(est[i].w));
    }
#pragma unroll
    for (int i = 0; i < 8; ++i) { PIN(ex8[i]); PIN(eq8[i]); PIN(eu8[i]); }
  };
  auto epilogue = [&]() AI {
    f32x4 p[8];
#pragma unroll
    for (int n = 0; n < 4; ++n) { p[2 * n] = *(const f32x4*)(lds + rr + n * 8192); p[2 * n + 1] = *(const f32x4*)(lds + rr + n * 8192 + 256); }
    float h[8];
#pragma unroll
    for (int i = 0; i < 8; ++i) {
      const float y = op(op(eu8[i] + p[i >> 2][i & 3]) + p[2 + (i >> 2)][i & 3]) + op(p[4 + (i >> 2)][i & 3] + p[6 + (i >> 2)][i & 3]);
      h[i] = op(ex8[i] + eq8[i] * op(y));
    }
    __builtin_amdgcn_raw_buffer_store_b128(u32x4{pk(h[0], h[1]), pk(h[2], h[3]), pk(h[4], h[5]), pk(h[6], h[7])}, rw, WS_H + eo, 0, SC1);
  };
  u64 fl;
  const u32 f2o = WS_F2 + (u32)(8 * (w & 3) + (l & 7)) * 128;
  {
    f32x4 acc[4];
    u32x4 A[2][4], B[2][4];
#pragma unroll
    for (int v = 0; v < 2; ++v) {
      A[v][0] = rdA(v, 0); SB();
#pragma unroll
      for (int m = 0; m < 4; ++m) { B[v][m] = rdB(v, m); SB(); }
#pragma unroll
      for (int m = 1; m < 4; ++m) { A[v][m] = rdA(v, m); SB(); }
      if (v == 0) {
        vmw<0>(ex); PIN(est[0]); PIN(est[1]); PIN(est[2]); PIN(est[3]);
        fl = flag_ld(dw, f2o);
        if (w < 4) terms();
        SB();
      }
    }
#pragma unroll
    for (int v = 0; v < 2; ++v)
#pragma unroll
      for (int m = 0; m < 4; ++m) {
#pragma unroll
        for (int c = m; c < 4; ++c) acc[c] = mfma16(A[v][m], B[v][c - m], (v == 0 && m == 0) ? f32x4{} : acc[c]);
        SB();
        if (v == 0 && m == 0) w1_g<9>(w1, L_RING);
        if (v == 0 && m == 2) w1_u<9>(w1, L_RING);
        if (v == 1 && m == 0) w1_g<10>(w1, L_RING + 16384);
        SB();
      }
#pragma unroll
    for (int c = 0; c < 4; ++c) *(f32x4*)(lds + rwr + c * 1024) = acc[c];
  }
  VMWP(3, fl);
  if (RARE(!__all(fl == tag))) poll<2>(ws, f2o, true, tag);
  bar();
  STAMP(9);
  if (w < 4) epilogue();
  SB();
  STAMP(10);
  {
    f32x4 acc[4];
    u32x4 A[2][8], B[2][8];
    auto rd = [&](int v, int m) AI {
      if (m == 0) {
        A[v][0] = rdA(v, 0);
#pragma unroll
        for (int d = 4; d < 8; ++d) B[v][d] = rdB(v, d);
      } else { A[v][m] = rdA(v, m); if (m <= 4) B[v][4 - m] = rdB(v, 4 - m); }
      SB();
    };
    rd(0, 0); rd(0, 1); rd(0, 2); rd(0, 3);
#pragma unroll
    for (int v = 0; v < 2; ++v) {
#pragma unroll
      for (int m = 0; m < 8; ++m) {
#pragma unroll
        for (int c = 4; c < 8; ++c) if (m <= c) acc[c - 4] = mfma16(A[v][m], B[v][c - m], (v == 0 && m == 0) ? f32x4{} : acc[c - 4]);
        SB();
        if (m < 4) rd(v, m + 4); else if (v == 0) rd(1, m - 4);
        if (v == 0) {
          if (m == 0) w1_u<10>(w1, L_RING + 16384);
          if (m == 1) w1_g<11>(w1, L_RING + 32768);
          if (m == 2) w1_u<11>(w1, L_RING + 32768);
          if (m == 3) w1_g<12>(w1, L_RING + 49152);
          if (m == 4) w1_u<12>(w1, L_RING + 49152);
          if (m == 6) w1_g<13>(w1, L_RING + 65536);
          if (m == 7) w1_u<13>(w1, L_RING + 65536);
        }
        SB();
      }
    }
    if (w >= 4) terms();
    STAMP(11);
    VMW(0); bar();
#pragma unroll
    for (int c = 0; c < 4; ++c) *(f32x4*)(lds + rwr + c * 1024) = acc[c];
  }
  const u32 lo16 = l * 16;
  if (w < 4) {
#pragma unroll
    for (int k = 0; k < 7; ++k) dma<SC1>(dw, lb + L_H0 + (7 * w + k) * 1024, lo16, WS_H + (u32)(2 * gi * TB) + wrap(7 * w + k + 2 * w1.rot, 28) * 1024);
  }
  w1_read<9>(lds, ra1, W); w1_read<10>(lds, ra1 + 16384, W); w1_read<11>(lds, ra1 + 32768, W); w1_read<12>(lds, ra2, W); w1_read<13>(lds, ra2 + 16384, W);
  bar();
  STAMP(12);

  const u32 bta = (u32)((8 * (G >> 1) + q4) * 64 + (G & 1) * 32 + p4 * 8);
  auto hbase = [](int g) AI { return g == 0 ? L_H0 : g == 1 ? L_H1 : g == 2 ? L_H2 : L_H3; };
  u32 csh, csl;
  auto colfin = [&]() AI {
    colsum(18, 28);
    const float t = hsum(cs0.x + cs1.x);
    const u32 hi = pk(t, t);
    csh = hh ? 0u : hi; csl = hh ? 0u : pk(t - bflo(hi), 0.f);
    PIN(csh); PIN(csl);
  };
  f32x4 tm; float gu[8], gt[8]; u32 od[4];
  auto center = [&](f32x16& acc, float m) AI {
    const u32 d = __float_as_uint(m);
    acc = mfma32(u32x4{csh, csl, 0, 0}, u32x4{d, d & 0xffffu, 0, 0}, acc);
  };
  auto geglu_step = [&](int g, f32x16& acc, int i) AI {
#pragma unroll
    for (int e = 0; e < 8; ++e) {
      const int s = i - (2 + (18 * e) / 7);
      if (s == 0) { gt[e] = acc[e] * acc[e]; gu[e] = op(acc[e] * acc[e + 8]); PIN(gt[e]); PIN(gu[e]); }
      if (s == 1) { gt[e] = tm.w * gt[e] + tm.z; PIN(gt[e]); }
      if (s == 2) { gt[e] = acc[e] * gt[e]; PIN(gt[e]); }
      if (s == 3) { gt[e] = __builtin_amdgcn_exp2f(gt[e]); PIN(gt[e]); }
      if (s == 4) { gt[e] = gt[e] * tm.y + tm.y; PIN(gt[e]); }
      if (s == 5) { gt[e] = __builtin_amdgcn_rcpf(gt[e]); PIN(gt[e]); }
      if (s == 6) { gu[e] = gu[e] * gt[e]; PIN(gu[e]); }
      if (s == 7 && (e & 1)) { od[e >> 1] = pk(gu[e - 1], gu[e]); PIN(od[e >> 1]); }
    }
    if (i == 28) {
      __builtin_amdgcn_raw_buffer_store_b128(u32x4{od[0], od[1], od[2], od[3]}, rw, gso, gofs(g), SC1);
    }
  };
  constexpr int PF = 4;
  u32x4 B[PF];
  u32 ba = opq(hbase(0) + bta);
  auto chain = [&](int g, f32x16& acc, auto side) AI {
    const u32 bn = opq(hbase(g < 3 ? g + 1 : g) + bta);
    sfor<28>([&](auto kc) AI {
      constexpr int k = decltype(kc)::value;
      acc = mfma32(W[k], B[k % PF], k == 0 ? f32x16{} : acc);
      SB();
      side(k);
      if (k + PF < 28) B[k % PF] = cat(trd(lds, ba + (k + PF) * 1024), trd(lds, ba + (k + PF) * 1024 + 256));
      else if (g < 3) B[k % PF] = cat(trd(lds, bn + (k + PF - 28) * 1024), trd(lds, bn + (k + PF - 28) * 1024 + 256));
      SB();
    });
    ba = bn;
  };
  auto flag3 = [&](int g) AI { setflag(ws, WS_F3 + (u32)(((gi * 4 + g) * 14) + gj) * 128, tag); };
  const int ri = 2 * xc + sc / 14, rq2 = (sc / 7) & 1, n2 = sc % 7;
  const u32x4 rd = srd(gwd, D_ * F_ * 2);
  const u32 wdv = (u32)(((224 * w + r8) * D_ + 64 * n2) * 2 + (((l & 7) ^ (((r8 >> 1) & 3) << 1)) << 4));
  const u32 blkR = w < 7 ? L_R + w * 4096 : L_R2;
  u32 wda[4], wdr[4];
#pragma unroll
  for (int nt = 0; nt < 4; ++nt) {
    const u32 a = (16 * (G >> 1) + 4 * (G & 1) + q4) * 128 + ((nt ^ (2 * (G & 1) + (q4 >> 1))) * 32) + p4 * 8;
    wda[nt] = opq(a + L_P + w * 4096); wdr[nt] = opq(a + blkR);
  }
  u32x4 Wd[7][4];
  auto wblk = [](int k) AI { return (k == 1 || k == 4) ? 1 : (k == 3 || k == 6) ? 2 : 0; };
  auto wd_dma = [&](int k, int i) AI {
    if (i == 0) dma_at(lb + (wblk(k) == 0 ? blkR : (wblk(k) == 2 ? L_Q : L_P) + w * 4096));
    dma_i<0>(i, rd, wdv, 4 * k * (8 * D_ * 2) + i * (8 * D_ * 2 - 1024));
  };
  auto wd_rd = [&](int k, int nt) AI {
    const u32 a = wblk(k) == 0 ? wdr[nt] : wda[nt] + (wblk(k) == 2 ? L_Q - L_P : 0);
    Wd[k][nt] = cat(trd(lds, a), trd(lds, a + 1024));
  };
  auto wd_read = [&](int k) AI { wd_rd(k, 0); wd_rd(k, 1); wd_rd(k, 2); wd_rd(k, 3); };
  const int j0 = (14 * w) >> 3, jn = ((14 * w + 13) >> 3) - j0;
  const u32 f3o = WS_F3 + (u32)(ri * 56 + j0 + ((l & 3) < jn ? (l & 3) : jn)) * 128;

  f32x16 accA, accB;
  u32x4 tm0, tm1, tm2, tm3;
  const u32 s2o = WS_S2 + (u32)(2 * gi * 512 + tk * 16);
  const u32 hp = (u32)(w < 4 ? 4 * w : 4 + 3 * w) * 1024;
  u32 hs[4];
#pragma unroll
  for (int i = 0; i < 4; ++i) hs[i] = (wrap((hp >> 10) + i + 2 * w1.rot, 28) - i) * 1024;
  auto htb = [&](int g) AI { return WS_H + (u32)((32 * (g >> 1) + 2 * gi + (g & 1)) * TB); };
  auto h_dma = [&](int g, int i) AI {
    if (i == 0) dma_at(lb + hbase(g) + hp);
    if (i < 3 || w < 4) dma_i<SC1>(i, dw, lo16, htb(g) + hs[i]);
  };
  auto redo1 = [&](int g, f32x16& acc, u32 m) AI {
    if (RARE(__any(acc[0] != acc[0]))) {
      REDO_COUNT();
      const u32 sc0 = L_SP + w * 1024;
      acc = f32x16{};
      const rsrc_t rg = mk(gwg, D_ * F_ * 2), ru = mk(gwu, D_ * F_ * 2);
#pragma unroll 1
      for (int k = 0; k < 28; ++k) {
        const u32 wo = (u32)(((16 * k + ((l >> 1) & 15)) * F_ + 128 * gj + 16 * w) * 2 + (l & 1) * 16);
        const u32x4 wg = __builtin_amdgcn_raw_buffer_load_b128(rg, wo, 0, 0), wu = __builtin_amdgcn_raw_buffer_load_b128(ru, wo, 0, 0);
        *(u32x4*)(lds + sc0 + lo16) = l < 32 ? wg : wu;
        const u32 wa = sc0 + (G & 1) * 512 + (8 * (G >> 1) + q4) * 32 + p4 * 8;
        const u32x4 a = cat(trd(lds, wa), trd(lds, wa + 128));
        u32x4 v;
#pragma unroll 1
        for (int it = 0; it < 400; ++it) { v = ldg(dw, lo16, htb(g) + k * 1024); vmw<0>(v); if (!__any(max4(v) == EMPTY)) break; }
        *(u32x4*)(lds + sc0 + lo16) = v;
        acc = mfma32(a, cat(trd(lds, sc0 + bta), trd(lds, sc0 + bta + 256)), acc);
      }
      center(acc, __uint_as_float(m));
    }
  };
  auto tm_fix = [&](u32x4& t, u32 so) AI {
    if (RARE(__any(max4(t) == EMPTY))) {
#pragma unroll 1
      for (int it = 0; it < 4000; ++it) { FIX_COUNT(); t = ldg(dw, s2o, so); vmw<0>(t); if (!__any(max4(t) == EMPTY)) break; }
    }
  };
  SB();
  if (w >= 4) {
    epilogue();
    SB();
    colfin();
  } else {
    colfin();
    SB();
    const u32 ha = opq(L_H0 + 7 * w * 1024 + lo16);
#pragma unroll 1
    for (int it = 0; it < 4000; ++it) {
      VMW(0);
      u32 m = 0;
#pragma unroll
      for (int k = 0; k < 7; ++k) { const u32x4 v = *(const u32x4*)(lds + ha + k * 1024); m = max(m, max4(v)); }
      if (!__any(m == EMPTY)) break;
#pragma unroll
      for (int k = 0; k < 7; ++k) dma<SC1>(dw, lb + L_H0 + (7 * w + k) * 1024, lo16, WS_H + (u32)(2 * gi * TB) + wrap(7 * w + k + 2 * w1.rot, 28) * 1024);
    }
    VMW(0);
    STAMP(13);
  }
  __builtin_amdgcn_s_setprio(1);
  bar();
  STAMP(14);
#pragma unroll
  for (int k = 0; k < PF; ++k) B[k] = cat(trd(lds, ba + k * 1024), trd(lds, ba + k * 1024 + 256));
  SB();
  chain(0, accA, [&](int k) AI {
    if (k <= 6 && !(k & 1)) h_dma(1, k >> 1);
    if (k == TM0K) tm0 = ldg(dw, s2o, 0);
    if (k == 10 || k == 13) __builtin_amdgcn_raw_buffer_store_b128(u32x4{EMPTY, EMPTY, EMPTY, EMPTY}, rw, gso, gofs(k == 10 ? 2 : 3), SC1);
    if (k >= 16 && k <= 25 && k % 3 == 1) wd_dma(0, (k - 16) / 3);
    if (k == 23) { VMW(6); RBAR(); if (tid == 0 && b == 0) setflag(ws, WS_E, tag); }
    if (k == 27) { vmw<(TM0K > 13 ? 4 : 6)>(tm0); tm_fix(tm0, 0); }
  });
  center(accA, __uint_as_float(tm0.x));
  tm = __builtin_bit_cast(f32x4, tm0);
  SB();
  STAMP(16);
  chain(1, accB, [&](int k) AI {
    if (k == 1) RBAR();
    geglu_step(0, accA, k);
    if (k == 2) tm1 = ldg(dw, s2o, 512);
    if (k == 3 || k == 5) wd_dma(1, (k - 3) >> 1);
    if (k >= 7 && k <= 10) h_dma(2, k - 7);
    if (k == 12) { VMW(6); dma_at(lb + L_P + w * 4096); }
    if (k == 13 || k == 15) wd_dma(1, (k - 9) >> 1);
    if (k >= 12 && k <= 18 && !(k & 1)) wd_rd(0, (k - 12) >> 1);
    if (k == 17) { vmw<7>(tm1); tm_fix(tm1, 512); }
    if (k >= 21 && (k & 1)) wd_dma(2, (k - 21) >> 1);
    if (k == 23) { VMW(4); RBAR(); }
  });
  center(accB, __uint_as_float(tm1.x));
  geglu_step(0, accA, 28);
  tm = __builtin_bit_cast(f32x4, tm1);
  SB();
  STAMP(18);
  chain(2, accA, [&](int k) AI {
    if (k == 1) RBAR();
    if (k == 2) redo1(1, accB, tm1.x);
    geglu_step(1, accB, k);
    if (k <= 6 && !(k & 1)) h_dma(3, k >> 1);
    if (k >= 3 && k <= 9 && (k & 1)) { if (k == 3) VMW(7); wd_rd(1, (k - 3) >> 1); }
    if (k == 8) { tm2 = ldg(dw, s2o, 32 * 512); tm3 = ldg(dw, s2o, 33 * 512); }
    if (k >= 10 && k <= 16 && !(k & 1)) wd_dma(3, (k - 10) >> 1);
    if (k >= 18 && k <= 27 && k % 3 == 0) wd_dma(4, (k - 18) / 3);
    if (k == 13) { VMW(7); RBAR(); if (tid == 0) flag3(0); }
    if (k == 23) { vmw<6>(tm2); PIN(tm3); RBAR(); }
    if (k == 25) tm_fix(tm2, 32 * 512);
    if (k == 26) tm_fix(tm3, 33 * 512);
  });
  center(accA, __uint_as_float(tm2.x));
  geglu_step(1, accB, 28);
  tm = __builtin_bit_cast(f32x4, tm2);
  SB();
  STAMP(20);
  const u32 gb = WS_G + (u32)((2 * ri * 2 + rq2) * 56 + 7 * w) * 1024;
  constexpr int NS = 9;
  u32x4 Hf[NS];
  auto hl = [&](int t) AI { Hf[t % NS] = ldg(dw, lo16, gb + gofs(t / 7) + (t % 7) * 1024); };
  chain(3, accB, [&](int k) AI {
    if (k == 2) redo1(2, accA, tm2.x);
    geglu_step(2, accA, k);
    if (k >= 5 && k < 13) { if (k == 5) VMW(5); wd_rd(2 + (k - 5) / 4, (k - 5) & 3); }
    if (k == 10) fl = flag_ld(dw, f3o);
    if (k >= 12 && k <= 18 && !(k & 1)) wd_dma(5, (k - 12) >> 1);
    if (k == 13) { VMW(2); RBAR(); if (tid == 0) flag3(1); }
    if (k >= 13 && k < 21 && (k & 1)) wd_rd(4, (k - 13) >> 1);
    if (k >= 19 && k <= 22) wd_dma(6, k - 19);
    if (k == 23) { VMWP(8, fl); if (RARE(!__all(fl == tag))) poll(ws, f3o, true, tag); }
    if (k >= 23) hl(k - 23);
  });
  center(accB, __uint_as_float(tm3.x));
  geglu_step(2, accA, 28);
  tm = __builtin_bit_cast(f32x4, tm3);
  redo1(3, accB, tm3.x);
  STAMP(21);
#pragma unroll
  for (int e = 0; e < 8; ++e) {
    const float a = accB[e], u = accB[e + 8];
    gu[e] = (a * u) * __builtin_amdgcn_rcpf(__builtin_amdgcn_exp2f(a * (tm.w * (a * a) + tm.z)) * tm.y + tm.y);
  }
#pragma unroll
  for (int e = 0; e < 4; ++e) od[e] = pk(gu[2 * e], gu[2 * e + 1]);
  geglu_step(3, accB, 28);
  __builtin_amdgcn_s_setprio(0);
  SB();
  STAMP(22);
  f32x4 acc[2][4];
  const rsrc_t ro = mk(gout, S_ * D_ * 2);
  const u32 pw = (u32)(i16 * 256), tf = 4 * (w & 3) + G;
  const u32 hro = WS_H + (u32)((32 * (G >> 1) + (G & 1) + 2 * ri) * TB + (64 * n2 + 8 * w + (i16 >> 1)) * 64 + rq2 * 32 + (i16 & 1) * 16);
  auto pbuf = [](int g) AI { return g == 3 ? L_T : g == 1 ? L_Q : L_P; };
  auto put = [&](int g) AI {
    const u32 rb = opq(pbuf(g) + w * 4096 + pw);
#pragma unroll
    for (int nt = 0; nt < 4; ++nt) *(f32x4*)(lds + rb + (((4 * nt + G) ^ (i16 & 7)) << 4)) = acc[g & 1][nt];
  };
  auto fin = [&](int g) AI {
    if ((w >> 2) == (g & 1)) {
      const u32 a0 = tf * 256 + ((i16 ^ (tf & 7)) << 4), a = opq(pbuf(g) + a0);
      f32x4 p[9];
      p[8] = *(const f32x4*)(lds + L_RES + g * 4096 + a0);
#pragma unroll
      for (int n = 0; n < 8; ++n) p[n] = *(const f32x4*)(lds + a + n * 4096);
      const f32x4 sum = (((p[0] + p[1]) + (p[2] + p[3])) + ((p[4] + p[5]) + (p[6] + p[7]))) + p[8];
      const int tok = 32 * (32 * (g >> 1) + (g & 1) + 2 * ri) + 16 * rq2 + tf;
      __builtin_amdgcn_raw_buffer_store_b64(u32x2{pk(sum.x, sum.y), pk(sum.z, sum.w)}, ro, (u32)((tok * D_ + 64 * n2 + 4 * i16) * 2), 0, 2);
    }
  };
  auto redo = [&](int g) AI {
    if (RARE(__any(acc[g & 1][0].x != acc[g & 1][0].x))) {
      REDO_COUNT();
      u32x4 v[7];
#pragma unroll 1
      for (int it = 0; it < 4000; ++it) {
        u32 m = 0;
#pragma unroll
        for (int k = 0; k < 7; ++k) v[k] = ldg(dw, lo16, gb + gofs(g) + k * 1024);
#pragma unroll
        for (int k = 0; k < 7; ++k) { vmw<0>(v[k]); m = max(m, max4(v[k])); }
        if (!__any(m == EMPTY)) break;
      }
#pragma unroll
      for (int k = 0; k < 7; ++k)
#pragma unroll
        for (int nt = 0; nt < 4; ++nt) acc[g & 1][nt] = mfma16(Wd[k][nt], v[k], k == 0 ? f32x4{} : acc[g & 1][nt]);
    }
  };
  fl = flag_ld(dw, f3o + 14 * 128);
  u32x4 hr = ldg(dw, hro, 0);
  hl(5); hl(6);
  VMW(11);
  wd_read(5); wd_read(6);
  SB();
  STAMP(23);
  sfor<28>([&](auto tc) AI {
    constexpr int t = decltype(tc)::value, g = t / 7, k = t % 7;
    constexpr auto pos = [](int h) { return h < 5 ? h : h + 4; };
    constexpr auto last = [](int u) { return u < 2 ? 6 : u == 2 ? 8 : u == 3 ? 10 : u < 21 ? u + 7 : 27; };
    if (t == 2) { VMWP(3, fl); if (RARE(!__all(fl == tag))) poll(ws, f3o + 14 * 128, true, tag); }
#pragma unroll
    for (int h = (t ? last(t - 1) : 6) + 1; h <= last(t); ++h) hl(h);
    vmw<pos(last(t)) - pos(t)>(Hf[t % NS]);
#pragma unroll
    for (int nt = 0; nt < 4; ++nt) acc[g & 1][nt] = mfma16(Wd[k][nt], Hf[t % NS], k == 0 ? f32x4{} : acc[g & 1][nt]);
    SB();
    if (t == 3) {
      vmw<6>(hr);
      if (RARE(__any(max4(hr) == EMPTY))) {
#pragma unroll 1
        for (int it = 0; it < 4000; ++it) { hr = ldg(dw, hro, 0); vmw<0>(hr); if (!__any(max4(hr) == EMPTY)) break; }
      }
      *(u32x4*)(lds + blkR + lo16) = hr;
#pragma unroll
      for (int q = 0; q < 4; ++q) {
        const u32x2 r = trd(lds, blkR + q * 256 + (4 * (G & 1) + q4) * 32 + p4 * 8);
        *(f32x4*)(lds + L_RES + q * 4096 + pw + (((2 * w + (G & 1)) ^ (i16 & 7)) << 4)) = f32x4{bflo(r.x), bfhi(r.x), bflo(r.y), bfhi(r.y)};
      }
    }
    if (t == 8) put(0);
    if (t == 12) { bar(); STAMP(24); fin(0); }
    if (t == 15) put(1);
    if (t == 19) { bar(); STAMP(25); fin(1); }
    if (t == 22) { redo(2); put(2); STAMP(26); }
    SB();
  });
  redo(3);
  put(3);
  bar();
  STAMP(27);
  fin(2); fin(3);
  STAMP(28);
  STAMP_OUT();
}

}

extern "C" void kernel_launch(void* const* d_in, const int* in_sizes, int n_in, void* d_out, int out_size, void* d_ws, size_t ws_size,
                              hipStream_t stream) {
  h3_ffn<<<256, 512, 0, stream>>>((const char*)d_in[0], (const char*)d_in[1], (const char*)d_in[2], (const char*)d_in[3], (const char*)d_in[4],
                                  (const char*)d_in[5], (const char*)d_in[7], (const char*)d_in[8], (const char*)d_in[9], (char*)d_out, (char*)d_ws);
}
```

```cpp
#include <hip/hip_runtime.h>
#include <stdint.h>

namespace {

#define LAS __attribute__((address_space(3)))
#define GAS __attribute__((address_space(1)))
#define DI __device__ __forceinline__
#define AI __attribute__((always_inline))
#define RARE(x) __builtin_expect(!!(x), 0)
typedef uint32_t u32;
typedef unsigned long long u64;
typedef u32 u32x2 __attribute__((ext_vector_type(2)));
typedef u32 u32x4 __attribute__((ext_vector_type(4)));
typedef short s16x4 __attribute__((ext_vector_type(4)));
typedef short s16x8 __attribute__((ext_vector_type(8)));
typedef float f32x4 __attribute__((ext_vector_type(4)));
typedef float f32x16 __attribute__((ext_vector_type(16)));
typedef __bf16 bf2 __attribute__((ext_vector_type(2)));
typedef __amdgpu_buffer_rsrc_t rsrc_t;
#define RLX_AGENT __ATOMIC_RELAXED, __HIP_MEMORY_SCOPE_AGENT

constexpr int S_ = 2048, D_ = 448, F_ = 1792;
constexpr u32 TB = 448 * 64;
constexpr u32 WS_E  = 0;
constexpr u32 WS_F1 = 128;
constexpr u32 WS_F3 = WS_F1 + 256 * 128;
constexpr u32 WS_F2 = WS_F3 + 896 * 128;
constexpr u32 WS_S2 = 256 * 1024 + 16384;
constexpr u32 WS_ST = 256 * 1024;
constexpr u32 WS_U  = WS_S2 + 32768;
constexpr u32 WS_X  = WS_U + 64 * TB;
constexpr u32 WS_H  = WS_X + 64 * TB;
constexpr u32 WS_G  = WS_H + 64 * TB;
constexpr u32 WS_END = WS_G + 64 * 112 * 1024;
constexpr int SC1 = 16;
constexpr u32 OOB = 0xFFFFFF00u;
constexpr u32 ONES = 0x3f803f80u;
constexpr u32 EMPTY = 0xffffffffu;

constexpr u32 L_K = 0, L_V = 9216, L_X = 17408, L_SK = 25600;
constexpr u32 L_RED = 0, L_FC = 32768, FCS = 4160, FCH = 4 * FCS;
constexpr u32 L_U = L_FC + 2 * FCH, UCH = 4608, L_RING = L_U + 2 * UCH;
constexpr u32 L_H0 = 32768, L_H1 = L_H0 + TB, L_H2 = 0, L_H3 = L_H1 + TB + 32768, L_SP = 155648;
constexpr u32 L_P = TB, L_Q = L_P + 32768, L_R = L_Q + 32768, L_R2 = L_H3 + TB;
constexpr u32 L_T = L_H3;
constexpr u32 LDS_BYTES = 163840;

DI float bflo(u32 x) { return __uint_as_float(x << 16); }
DI float bfhi(u32 x) { return __uint_as_float(x & 0xffff0000u); }
DI u32 pk(float lo, float hi) { bf2 p; p.x = (__bf16)lo; p.y = (__bf16)hi; return __builtin_bit_cast(u32, p); }
DI rsrc_t mk(const void* p, u32 bytes) { return __builtin_amdgcn_make_buffer_rsrc((void*)p, 0, bytes, 0x00020000); }
DI u32x2 trd(const char* lds, u32 a) { return __builtin_bit_cast(u32x2, __builtin_amdgcn_ds_read_tr16_b64_v4i16((LAS s16x4*)(lds + a))); }
DI u32x4 cat(u32x2 a, u32x2 b) { return u32x4{a.x, a.y, b.x, b.y}; }
DI u32x4 srd(const void* p, u32 bytes) { const u64 a = (u64)p; return u32x4{(u32)a, (u32)(a >> 32) & 0xffffu, bytes, 0x00020000u}; }
template <int AUX = 0> DI void dma(u32x4 r, u32 ldsoff, u32 voff, u32 soff) {
  if (AUX) asm volatile("s_mov_b32 m0, %0\n\ts_nop 0\n\tbuffer_load_dwordx4 %1, %2, %3 offen sc1 lds" :: "s"(ldsoff), "v"(voff), "s"(r), "s"(soff) : "memory");
  else asm volatile("s_mov_b32 m0, %0\n\ts_nop 0\n\tbuffer_load_dwordx4 %1, %2, %3 offen lds" :: "s"(ldsoff), "v"(voff), "s"(r), "s"(soff) : "memory");
}
DI void dma_at(u32 ldsoff) { asm volatile("s_mov_b32 m0, %0\n\ts_nop 0" :: "s"(ldsoff) : "memory"); }
template <int OFF, int AUX> DI void dma_go(u32x4 r, u32 voff, u32 soff) {
  if (AUX) asm volatile("buffer_load_dwordx4 %0, %1, %2 offen offset:%3 sc1 lds" :: "v"(voff), "s"(r), "s"(soff), "n"(OFF) : "memory");
  else asm volatile("buffer_load_dwordx4 %0, %1, %2 offen offset:%3 lds" :: "v"(voff), "s"(r), "s"(soff), "n"(OFF) : "memory");
}
template <int AUX> DI void dma_i(int i, u32x4 r, u32 voff, u32 soff) {
  if (i == 0) dma_go<0, AUX>(r, voff, soff); else if (i == 1) dma_go<1024, AUX>(r, voff, soff); else if (i == 2) dma_go<2048, AUX>(r, voff, soff); else dma_go<3072, AUX>(r, voff, soff);
}
DI float op(float x) { asm("" : "+v"(x)); return x; }
DI u32 opq(u32 x) { asm volatile("" : "+v"(x)); return x; }
#define PIN(x) asm volatile("" : "+v"(x))
DI f32x4 mfma16(u32x4 a, u32x4 b, f32x4 c) { return __builtin_amdgcn_mfma_f32_16x16x32_bf16(__builtin_bit_cast(s16x8, a), __builtin_bit_cast(s16x8, b), c, 0, 0, 0); }
DI f32x16 mfma32(u32x4 a, u32x4 b, f32x16 c) { return __builtin_amdgcn_mfma_f32_32x32x16_bf16(__builtin_bit_cast(s16x8, a), __builtin_bit_cast(s16x8, b), c, 0, 0, 0); }
DI f32x4 mfma4(u32x2 a, u32x2 b, f32x4 c) { return __builtin_amdgcn_mfma_f32_4x4x4bf16_1k(__builtin_bit_cast(s16x4, a), __builtin_bit_cast(s16x4, b), c, 0, 0, 0); }
DI float dot2(u32 a, u32 b, float c) { return __builtin_amdgcn_fdot2_f32_bf16(__builtin_bit_cast(bf2, a), __builtin_bit_cast(bf2, b), c, false); }
#define VMW(n) asm volatile("s_waitcnt vmcnt(%0)" :: "n"(n) : "memory")
#define LG0() asm volatile("s_waitcnt lgkmcnt(0)" ::: "memory")
#define SB() __builtin_amdgcn_sched_barrier(0)
DI void bar() { asm volatile("s_waitcnt lgkmcnt(0)\n\ts_barrier" ::: "memory"); }
#define RBAR() asm volatile("s_barrier" ::: "memory")
DI float hsum(float v) {
  const auto r = __builtin_amdgcn_permlane32_swap(__float_as_uint(v), __float_as_uint(v), false, false);
  return __uint_as_float(r[0]) + __uint_as_float(r[1]);
}
template <int SLEEP = 1> DI void poll(char* ws, u32 off, bool act, u64 tag) {
  bool ok = !act;
#pragma unroll 1
  for (int it = 0; it < 20000; ++it) {
    u64 x = tag;
    if (!ok) x = __hip_atomic_load((GAS u64*)(ws + off), RLX_AGENT);
    ok = (x == tag);
    if (__all(ok)) break;
    __builtin_amdgcn_s_sleep(SLEEP);
  }
  asm volatile("" ::: "memory");
}
DI u32 max4(u32x4 v) { return max(max(v.x, v.y), max(v.z, v.w)); }
DI u64 flag_ld(u32x4 r, u32 voff) { u64 v; asm volatile("buffer_load_dwordx2 %0, %1, %2, 0 offen sc1" : "=v"(v) : "v"(voff), "s"(r) : "memory"); return v; }
DI u32x4 ldg(u32x4 r, u32 voff, u32 soff) { u32x4 v; asm volatile("buffer_load_dwordx4 %0, %1, %2, %3 offen sc1" : "=v"(v) : "v"(voff), "s"(r), "s"(soff) : "memory"); return v; }
template <int N> DI void vmw(u32x4& x) { asm volatile("s_waitcnt vmcnt(%1)" : "+v"(x) : "n"(N) : "memory"); }
#define VMWP(n, x) asm volatile("s_waitcnt vmcnt(" #n ")" : "+v"(x) :: "memory")
DI void setflag(char* ws, u32 off, u64 tag) { __hip_atomic_store((GAS u64*)(ws + off), tag, RLX_AGENT); }


DI float xsum(float a, float c) {
  const auto r = __builtin_amdgcn_permlane32_swap(__float_as_uint(a), __float_as_uint(c), false, false);
  return __uint_as_float(r[0]) + __uint_as_float(r[1]);
}
DI f32x4 ln2_terms(float t) {
  const auto r = __builtin_amdgcn_permlane32_swap(__float_as_uint(t), __float_as_uint(t), false, false);
  const float mean = __uint_as_float(r[0]) * (1.f / D_);
  const float V = fmaxf(__uint_as_float(r[1]) * (1.f / D_) - mean * mean, 0.f) + 1e-5f;
  const float rs = __builtin_amdgcn_rsqf(V);
  const float K1 = -2.f * 0.7978845608028654f * 1.4426950408889634f * rs;
  const float lo = -mean - bflo(pk(-mean, 0.f));
  return f32x4{__uint_as_float(pk(-mean, lo)), V, K1, (K1 * rs) * (0.044715f * rs)};
}
template <int N> struct IC { static constexpr int value = N; };
template <int N, int I = 0, class F> DI void sfor(F&& f) { if constexpr (I < N) { f(IC<I>{}); sfor<N, I + 1>(f); } }
struct W1 { u32x4 rg, ru; u32 vg, vu, ra, lb, rot; };
DI u32 wrap(u32 x, u32 n) { return x >= n ? x - n : x; }
template <int T> DI void w1_g(const W1& c, u32 base) { dma(c.rg, c.lb + base, c.vg, wrap(T + c.rot, 14) * (32 * F_ * 2)); }
template <int T> DI void w1_u(const W1& c, u32 base) { dma(c.ru, c.lb + base + 8192, c.vu, wrap(T + c.rot, 14) * (32 * F_ * 2)); }
template <int T> DI void w1_issue(const W1& c, u32 base) { w1_g<T>(c, base); w1_u<T>(c, base); }
template <int T> DI void w1_read(const char* lds, u32 a, u32x4 (&W)[28]) {
#pragma unroll
  for (int kk = 0; kk < 2; ++kk) W[2 * T + kk] = cat(trd(lds, a + kk * 4096), trd(lds, a + kk * 4096 + 1024));
}
DI float x8sum(float v) {
  v += __builtin_amdgcn_update_dpp(0.f, v, 0xb1, 0xf, 0xf, true);
  v += __builtin_amdgcn_update_dpp(0.f, v, 0x4e, 0xf, 0xf, true);
  v += __builtin_amdgcn_update_dpp(0.f, v, 0x141, 0xf, 0xf, true);
  return v;
}

__global__ __launch_bounds__(512, 2) void h3_ffn(const char* __restrict__ gq, const char* __restrict__ gk, const char* __restrict__ gv,
                                                 const char* __restrict__ gsk, const char* __restrict__ gf, const char* __restrict__ gD,
                                                 const char* __restrict__ gwg, const char* __restrict__ gwu, const char* __restrict__ gwd,
                                                 char* __restrict__ gout, char* ws) {
  __shared__ __attribute__((aligned(16))) char lds[LDS_BYTES];
  const int tid = threadIdx.x, l = tid & 63, w = __builtin_amdgcn_readfirstlane(tid >> 6);
  const int b = blockIdx.x, xc = b & 7, sl = b >> 3;
  const int tb64 = 4 * xc + (sl >> 3), role = sl & 7;
  const int sc = (sl >> 3) * 7 + role;
  asm volatile("" :: "s"(gq), "s"(gk), "s"(gv), "s"(gsk), "s"(gf), "s"(gD), "s"(gwg), "s"(gwu), "s"(gwd), "s"(gout), "s"(ws));
  u64 pc0; asm volatile("s_getpc_b64 %0" : "=s"(pc0));
  u32 ipf = 0;
  if (role == 7 && w >= 4) asm volatile("global_load_dword %0, %1, off" : "=v"(ipf) : "v"((pc0 & ~255ull) + (u64)min((u32)(tid - 256) * 128u, (u32)28672)) : "memory");
  SB();
  const rsrc_t rw = mk(ws, WS_END);
  const int G = l >> 4, i16 = l & 15, q4 = i16 >> 2, p4 = l & 3, r8 = l >> 3;
  auto mktag = [&](u64 e0) AI { return ((u64)__builtin_amdgcn_readfirstlane((u32)e0) | ((u64)__builtin_amdgcn_readfirstlane((u32)(e0 >> 32)) << 32)) * 6364136223846793005ull + 1442695040888963407ull; };
  if (RARE(role == 7)) {
    const rsrc_t rq = mk(gq, S_ * D_ * 2);
    const int row = 64 * tb64 + 8 * w + r8;
    u32x4 xs[7];
#pragma unroll
    for (int i = 0; i < 7; ++i) xs[i] = __builtin_amdgcn_raw_buffer_load_b128(rq, (u32)(row * 896 + (l & 7) * 16 + i * 128), 0, 0);
    const u64 e0 = __hip_atomic_load((GAS u64*)(ws + WS_E), RLX_AGENT);
    __builtin_amdgcn_raw_buffer_store_b128(u32x4{EMPTY, EMPTY, EMPTY, EMPTY}, rw, tid < 64 ? WS_S2 + (u32)((tb64 + (tid & 32)) * 512 + (tid & 31) * 16) : OOB, 0, SC1);
    SB();
    u32 tlb = 0;
    if (w == 7) {
      const u32 n = (WS_END + 1048576 - 1) / 1048576, m2 = (S_ * D_ * 2 + 1048576 - 1) / 1048576;
      const u32 i = min((u32)l, n + 4 * m2 - 1);
      const char* p = i < n ? ws + (size_t)i * 1048576 + 64 : (i < n + m2 ? (const char*)gout : i < n + 2 * m2 ? gwd : i < n + 3 * m2 ? gwg : gwu) + ((i - n) % m2) * 1048576;
      asm volatile("global_load_dword %0, %1, off" : "=v"(tlb) : "v"(p) : "memory");
    }
    SB();
    const u64 tag = mktag(e0);
    float s[4] = {};
#pragma unroll
    for (int i = 0; i < 7; ++i)
#pragma unroll
      for (int e = 0; e < 4; ++e) s[e] += bflo(xs[i][e]) + bfhi(xs[i][e]);
    const float mean = x8sum((s[0] + s[1]) + (s[2] + s[3])) * (1.f / D_);
    float v[4] = {};
#pragma unroll
    for (int i = 0; i < 7; ++i)
#pragma unroll
      for (int e = 0; e < 4; ++e) { const float d0 = bflo(xs[i][e]) - mean, d1 = bfhi(xs[i][e]) - mean; v[e] += d0 * d0; v[e] += d1 * d1; }
    const float rstd = __builtin_amdgcn_rsqf(x8sum((v[0] + v[1]) + (v[2] + v[3])) * (1.f / D_) + 1e-5f);
    if ((l & 7) == 0) *(u32x2*)(lds + ((r8 >> 1) & 3) * 128 + w * 16 + (r8 & 1) * 8) = u32x2{__float_as_uint(mean), __float_as_uint(rstd)};
    bar();
    if (w == 0) {
      __builtin_amdgcn_raw_buffer_store_b128(*(const u32x4*)(lds + (l & 31) * 16), rw, l < 32 ? WS_ST + (u32)((tb64 >> 2) * 2048 + (l >> 3) * 512 + (tb64 & 3) * 128 + (l & 7) * 16) : OOB, 0, SC1);
      VMW(0);
    }
    if (tid == 0) setflag(ws, WS_F1 + (tb64 * 8 + 7) * 128, tag);
    bar();
#pragma unroll
    for (int i = 0; i < 7; ++i) {
      const u32 o = (u32)(i * 8192 + tid * 16);
      __builtin_amdgcn_raw_buffer_store_b128(u32x4{EMPTY, EMPTY, EMPTY, EMPTY}, rw, o < TB ? o : o + 31 * TB, WS_H + (u32)tb64 * TB, SC1);
    }
    VMW(0); PIN(tlb); PIN(ipf); bar();
    if (tid == 0) setflag(ws, WS_F2 + tb64 * 128, tag);
    {
      const rsrc_t rdw = mk(gwd, D_ * F_ * 2);
      const u32 t = __builtin_amdgcn_raw_buffer_load_b32(rdw, tid < 392 ? (u32)((tb64 * 392 + tid) * 128) : OOB, 0, 0);
      asm volatile("" :: "v"(t));
    }
    const u32x4 dw = srd(ws, WS_END);
    const u32 lb = (u32)(size_t)(LAS char*)lds;
    __builtin_amdgcn_s_sleep(8);
#pragma unroll 1
    for (int half = 0; half < 2; ++half) {
      const int tb = 32 * half + tb64;
#pragma unroll 1
      for (int it = 0; it < 4000; ++it) {
#pragma unroll
        for (int k = 0; k < 4; ++k) if (k < 3 || l < 32) dma<SC1>(dw, lb + 32768 + w * 3584 + k * 1024, l * 16, WS_H + (u32)(tb * TB + w * 3584 + k * 1024));
        VMW(0);
        u32 m = 0;
#pragma unroll
        for (int k = 0; k < 4; ++k) m = max(m, max4(*(const u32x4*)(lds + 32768 + w * 3584 + (k < 3 ? k * 1024 + l * 16 : 3072 + (l & 31) * 16))));
        if (!__any(m == EMPTY)) break;
        __builtin_amdgcn_s_sleep(3);
      }
      float s1 = 0.f, s2 = 0.f;
#pragma unroll
      for (int i = 0; i < 7; ++i) {
        const u32x2 t = trd(lds, 32768 + (u32)((56 * w + 4 * (G >> 1) + q4) * 64 + (G & 1) * 32 + p4 * 8) + i * 512);
        s1 = dot2(t.x, ONES, s1); s2 = dot2(t.x, t.x, s2); s1 = dot2(t.y, ONES, s1); s2 = dot2(t.y, t.y, s2);
      }
      *(float*)(lds + 20480 + half * 2048 + l * 32 + w * 4) = xsum(s1, s2);
      bar();
      if (w == 0) {
        const f32x4 a = *(const f32x4*)(lds + 20480 + half * 2048 + l * 32), c = *(const f32x4*)(lds + 20480 + half * 2048 + l * 32 + 16);
        __builtin_amdgcn_raw_buffer_store_b128(__builtin_bit_cast(u32x4, ln2_terms(((a.x + a.y) + (a.z + a.w)) + ((c.x + c.y) + (c.z + c.w)))), rw, l < 32 ? WS_S2 + (u32)(tb * 512 + l * 16) : OOB, 0, SC1);
      }
    }
    return;
  }
  const int cp = xc * 28 + sc, cch = w & 1, vp = w >> 1;
  const int ech = (l >> 2) & 1;
  const u32 eo = (u32)((8 * w + r8) * TB + (2 * cp + ech) * 64 + p4 * 16);
  u32x4 fhi; u32x2 flo; u32 Dh; u64 e0;
  const u32 lb = (u32)(size_t)(LAS char*)lds;
  const u32x4 dw = srd(ws, WS_END);
  {
    const u32x4 rk = srd(gk, S_ * D_ * 2), rv = srd(gv, S_ * D_ * 2);
    const int c8 = l & 7, s0 = 64 * tb64;
    const int swv = ((r8 >> 1) & 1) | ((w & 1) << 1), swk = swv ^ 2;
    const u32 g0 = (u32)((s0 + 8 * w + r8) * 896 + role * 128);
    dma(rk, lb + L_K + 1024 + w * 1024, g0 + ((c8 ^ (swk << 1)) << 4), 0);
    dma(rv, lb + L_V + w * 1024, g0 + ((c8 ^ (swv << 1)) << 4), 0);
    SB();
    if (w == 0) dma(rk, lb + L_K, g0 - 8 * 896 + ((c8 ^ (swv << 1)) << 4), 0);
    if (w == 1) dma(srd(gsk, D_ * 8), lb + L_SK, (u32)(512 * role + 16 * l), 0);
    dma(srd(gq, S_ * D_ * 2), lb + L_X + w * 1024, g0 + ((c8 ^ (swv << 1)) << 4), 0);
    const u32x4 rf = srd(gf, S_ * D_ * 2);
    const int fq = vp * 64 + l;
    const u32 fo = (u32)((2 * cp + cch) * 4096 + 16 * fq);
    asm volatile("buffer_load_dwordx4 %0, %2, %4, 0 offen\n\tbuffer_load_dwordx2 %1, %3, %4, 0 offen" : "=&v"(fhi), "=&v"(flo) : "v"(fo), "v"(fq > 0 ? fo - 8 : OOB), "s"(rf) : "memory");
    asm volatile("buffer_load_ushort %0, %1, %2, 0 offen" : "=v"(Dh) : "v"((u32)((2 * cp + ((l >> 2) & 1)) * 2)), "s"(srd(gD, D_ * 2)) : "memory");
    e0 = flag_ld(dw, WS_E);
    SB();
  }
  const int gx = (xc >> 1) * 28 + sc, gi = 8 * (xc & 1) + (gx & 7), gj = gx >> 3;
  W1 w1;
  {
    const u32 v0 = (u32)(((4 * w + G) * F_ + 128 * gj) * 2);
    w1.rg = srd(gwg, D_ * F_ * 2); w1.ru = srd(gwu, D_ * F_ * 2); w1.lb = lb + w * 1024;
    w1.rot = (7 * (xc & 1) + ((7 * (gx & 3)) >> 2));
    w1.vg = v0 + ((i16 ^ (4 * G)) << 4); w1.vu = v0 + ((i16 ^ (4 * G + 2)) << 4);
    w1.ra = (u32)((G & 1) * 8192 + (8 * (G >> 1) + q4) * 256 + ((w ^ (2 * q4 + (G & 1))) * 32) + p4 * 8);
  }
  w1_issue<0>(w1, L_FC); w1_issue<1>(w1, L_FC + 16384);
  VMW(9); bar();
  {
    const int tbl = w >> 2, chg = w & 3;
    const u32 rb = (u32)((32 * tbl + 8 * G + q4) * 128 + p4 * 8);
    const u32 sv = (u32)((chg ^ ((q4 >> 1) | ((G & 1) << 1))) * 32), sk2 = sv ^ 64;
    const u32x2 km = trd(lds, L_K + rb + 512 + sv), k0 = trd(lds, L_K + rb + 1024 + sk2), k1 = trd(lds, L_K + rb + 1536 + sk2);
    const u32x2 v0 = trd(lds, L_V + rb + sv), v1 = trd(lds, L_V + rb + 512 + sv);
    const u32x2 skv = *(const u32x2*)(lds + L_SK + (16 * chg + i16) * 8);
    const float kk[11] = {bfhi(km.x), bflo(km.y), bfhi(km.y), bflo(k0.x), bfhi(k0.x), bflo(k0.y), bfhi(k0.y), bflo(k1.x), bfhi(k1.x), bflo(k1.y), bfhi(k1.y)};
    const float vv[8] = {bflo(v0.x), bfhi(v0.x), bflo(v0.y), bfhi(v0.y), bflo(v1.x), bfhi(v1.x), bflo(v1.y), bfhi(v1.y)};
    const float s0 = bflo(skv.x), s1 = bfhi(skv.x), s2 = bflo(skv.y), s3 = bfhi(skv.y);
    float u[8];
#pragma unroll
    for (int e = 0; e < 8; ++e) u[e] = (kk[e + 3] * s0 + kk[e + 2] * s1 + kk[e + 1] * s2 + kk[e] * s3) * vv[e];
    const u32 o = (u32)((2 * tb64 + tbl) * TB + (64 * role + 16 * chg + i16) * 64 + G * 16);
    __builtin_amdgcn_raw_buffer_store_b128(u32x4{pk(u[0], u[1]), pk(u[2], u[3]), pk(u[4], u[5]), pk(u[6], u[7])}, rw, WS_U + o, 0, SC1);
    SB();
    VMW(9); bar();
    const u32x2 x0 = trd(lds, L_X + rb + sv), x1 = trd(lds, L_X + rb + 512 + sv);
    __builtin_amdgcn_raw_buffer_store_b128(cat(x0, x1), rw, WS_X + o, 0, SC1);
    VMW(6); PIN(fhi); PIN(flo); PIN(Dh); PIN(e0);
    SB();
  }
  const u64 tag = mktag(e0);
  const float Dv = bflo(Dh);
  SB();
  w1_issue<2>(w1, L_RING); w1_issue<3>(w1, L_RING + 16384);
  VMW(4); bar();
  if (tid == 0) setflag(ws, WS_F1 + (tb64 * 8 + role) * 128, tag);
  const int hh = l >> 5, tk = l & 31;
  const u32 gso = WS_G + (u32)(((2 * gi * 2 + (tk >> 4)) * 56 + 4 * gj + (w >> 1)) * 1024 + (2 * (w & 1) + hh) * 256 + i16 * 16);
  auto gofs = [](int g) AI { return (u32)((32 * (g >> 1) + (g & 1)) * 2 * 56 * 1024); };
  u32x4 W[28];
  f32x4 cs0 = {}, cs1 = {};
  auto colsum = [&](int k0, int k1) AI {
#pragma unroll
    for (int k = k0; k < k1; ++k) { cs0 = mfma4(u32x2{ONES, ONES}, u32x2{W[k].x, W[k].y}, cs0); cs1 = mfma4(u32x2{ONES, ONES}, u32x2{W[k].z, W[k].w}, cs1); }
  };
  const u32 ra0 = opq(w1.ra), ra1 = opq(w1.ra + L_RING), ra2 = opq(w1.ra + L_RING + 49152);
  w1_read<0>(lds, ra0 + L_FC, W); w1_read<1>(lds, ra0 + L_FC + 16384, W);
  w1_issue<7>(w1, L_RED); w1_issue<8>(w1, L_RED + 16384);
  bar();
  {
    const u32 a0 = __builtin_amdgcn_alignbit(flo.y, flo.x, 16), a1 = __builtin_amdgcn_alignbit(fhi.x, flo.y, 16), a2 = __builtin_amdgcn_alignbit(fhi.y, fhi.x, 16),
              a3 = __builtin_amdgcn_alignbit(fhi.z, fhi.y, 16), a4 = __builtin_amdgcn_alignbit(fhi.w, fhi.z, 16);
    char* fc = lds + L_FC + cch * FCH + (vp * 64 + l) * 16 + 32;
    *(u32x4*)(fc) = fhi;
    *(u32x4*)(fc + FCS) = u32x4{a1, a2, a3, a4};
    *(u32x4*)(fc + 2 * FCS) = u32x4{flo.y, fhi.x, fhi.y, fhi.z};
    *(u32x4*)(fc + 3 * FCS) = u32x4{a0, a1, a2, a3};
    if (tid >= 128 && tid < 144) *(u32x4*)(lds + L_FC + (tid & 7) * FCS + ((tid >> 3) & 1) * 16) = u32x4{0, 0, 0, 0};
    if (tid >= 192 && tid < 252) { const int z = tid - 192; *(u32x4*)(lds + L_U + (z / 30) * UCH + (z % 30) * 16) = u32x4{0, 0, 0, 0}; }
  }
  SB();
  colsum(0, 4);
  w1_issue<4>(w1, L_RING + 32768); w1_issue<5>(w1, L_RING + 49152); w1_issue<6>(w1, L_RING + 65536);
  SB();

  poll(ws, WS_F1 + (u32)(32 * w + 8 * (l & 3) + ((l & 4) ? 7 : (cp >> 5))) * 128, l < 8, tag);
  u32x4 uu = ldg(dw, eo, WS_U), ex = ldg(dw, eo, WS_X);
  u32x4 est[4];
#pragma unroll
  for (int i = 0; i < 4; ++i) est[i] = ldg(dw, (u32)(r8 * 64 + p4 * 16), WS_ST + (u32)(w * 2048 + i * 512));
  bar();
  w1_read<7>(lds, ra0 + L_RED, W); w1_read<8>(lds, ra0 + L_RED + 16384, W);
  w1_read<2>(lds, ra1, W); w1_read<3>(lds, ra1 + 16384, W); w1_read<4>(lds, ra1 + 32768, W); w1_read<5>(lds, ra2, W); w1_read<6>(lds, ra2 + 16384, W);
  const u32 aA = opq(L_FC + cch * FCH + q4 * FCS + 64 - 16 * G + 8 * p4 - 8 + 128 * vp);
  const u32 aB = opq(L_U + cch * UCH + 32 * i16 + 16 * G + 448 - 128 * vp - 64);
  const u32 rwr = opq(L_RED + w * 4096 + G * 256 + ((i16 ^ ((G >> 1) << 3)) << 4));
  const u32 rr = opq(L_RED + ech * 4096 + (w & 3) * 1024 + (p4 & 1) * 512 + (((2 * r8 + (p4 >> 1)) ^ ((p4 & 1) << 3)) << 4));
  colsum(4, 18);
  PIN(cs0); PIN(cs1);
  SB();
  vmw<5>(uu);
  *(u32x4*)(lds + L_U + ech * UCH + 480 + (8 * w + r8) * 64 + p4 * 16) = uu;
  bar();
  auto rdA = [&](int v, int m) AI { const u32 o = 64 * v + 512 * m; return cat(trd(lds, aA + o + 8), trd(lds, aA + o)); };
  auto rdB = [&](int v, int m) AI { return *(const u32x4*)(lds + aB + 64 * (1 - v) + 512 * m); };
  float ex8[8], eq8[8], eu8[8];
  auto terms = [&]() AI {
#pragma unroll
    for (int i = 0; i < 4; ++i) {
      ex8[2 * i] = bflo(ex[i]); ex8[2 * i + 1] = bfhi(ex[i]);
      eu8[2 * i] = bflo(uu[i]) * Dv; eu8[2 * i + 1] = op(bfhi(uu[i]) * Dv);
      eq8[2 * i] = (ex8[2 * i] - __uint_as_float(est[i].x)) * __uint_as_float(est[i].y);
      eq8[2 * i + 1] = op(op(ex8[2 * i + 1] - __uint_as_float(est[i].z)) * __uint_as_float(est[i].w));
    }
#pragma unroll
    for (int i = 0; i < 8; ++i) { PIN(ex8[i]); PIN(eq8[i]); PIN(eu8[i]); }
  };
  auto epilogue = [&]() AI {
    f32x4 p[8];
#pragma unroll
    for (int n = 0; n < 4; ++n) { p[2 * n] = *(const f32x4*)(lds + rr + n * 8192); p[2 * n + 1] = *(const f32x4*)(lds + rr + n * 8192 + 256); }
    float h[8];
#pragma unroll
    for (int i = 0; i < 8; ++i) {
      const float y = op(op(eu8[i] + p[i >> 2][i & 3]) + p[2 + (i >> 2)][i & 3]) + op(p[4 + (i >> 2)][i & 3] + p[6 + (i >> 2)][i & 3]);
      h[i] = op(ex8[i] + eq8[i] * op(y));
    }
    __builtin_amdgcn_raw_buffer_store_b128(u32x4{pk(h[0], h[1]), pk(h[2], h[3]), pk(h[4], h[5]), pk(h[6], h[7])}, rw, WS_H + eo, 0, SC1);
  };
  u64 fl;
  const u32 f2o = WS_F2 + (u32)(8 * (w & 3) + (l & 7)) * 128;
  {
    f32x4 acc[4];
    u32x4 A[2][4], B[2][4];
#pragma unroll
    for (int v = 0; v < 2; ++v) {
      A[v][0] = rdA(v, 0); SB();
#pragma unroll
      for (int m = 0; m < 4; ++m) { B[v][m] = rdB(v, m); SB(); }
#pragma unroll
      for (int m = 1; m < 4; ++m) { A[v][m] = rdA(v, m); SB(); }
      if (v == 0) {
        vmw<0>(ex); PIN(est[0]); PIN(est[1]); PIN(est[2]); PIN(est[3]);
        fl = flag_ld(dw, f2o);
        if (w < 4) terms();
        SB();
      }
    }
#pragma unroll
    for (int v = 0; v < 2; ++v)
#pragma unroll
      for (int m = 0; m < 4; ++m) {
#pragma unroll
        for (int c = m; c < 4; ++c) acc[c] = mfma16(A[v][m], B[v][c - m], (v == 0 && m == 0) ? f32x4{} : acc[c]);
        SB();
        if (v == 0 && m == 0) w1_g<9>(w1, L_RING);
        if (v == 0 && m == 2) w1_u<9>(w1, L_RING);
        if (v == 1 && m == 0) w1_g<10>(w1, L_RING + 16384);
        SB();
      }
#pragma unroll
    for (int c = 0; c < 4; ++c) *(f32x4*)(lds + rwr + c * 1024) = acc[c];
  }
  VMWP(3, fl);
  if (RARE(!__all(fl == tag))) poll<2>(ws, f2o, true, tag);
  bar();
  if (w < 4) epilogue();
  SB();
  {
    f32x4 acc[4];
    u32x4 A[2][8], B[2][8];
    auto rd = [&](int v, int m) AI {
      if (m == 0) {
        A[v][0] = rdA(v, 0);
#pragma unroll
        for (int d = 4; d < 8; ++d) B[v][d] = rdB(v, d);
      } else { A[v][m] = rdA(v, m); if (m <= 4) B[v][4 - m] = rdB(v, 4 - m); }
      SB();
    };
    rd(0, 0); rd(0, 1); rd(0, 2); rd(0, 3);
#pragma unroll
    for (int v = 0; v < 2; ++v) {
#pragma unroll
      for (int m = 0; m < 8; ++m) {
#pragma unroll
        for (int c = 4; c < 8; ++c) if (m <= c) acc[c - 4] = mfma16(A[v][m], B[v][c - m], (v == 0 && m == 0) ? f32x4{} : acc[c - 4]);
        SB();
        if (m < 4) rd(v, m + 4); else if (v == 0) rd(1, m - 4);
        if (v == 0) {
          if (m == 0) w1_u<10>(w1, L_RING + 16384);
          if (m == 1) w1_g<11>(w1, L_RING + 32768);
          if (m == 2) w1_u<11>(w1, L_RING + 32768);
          if (m == 3) w1_g<12>(w1, L_RING + 49152);
          if (m == 4) w1_u<12>(w1, L_RING + 49152);
          if (m == 6) w1_g<13>(w1, L_RING + 65536);
          if (m == 7) w1_u<13>(w1, L_RING + 65536);
        }
        SB();
      }
    }
    if (w >= 4) terms();
    VMW(0); bar();
#pragma unroll
    for (int c = 0; c < 4; ++c) *(f32x4*)(lds + rwr + c * 1024) = acc[c];
  }
  const u32 lo16 = l * 16;
  if (w < 4) {
#pragma unroll
    for (int k = 0; k < 7; ++k) dma<SC1>(dw, lb + L_H0 + (7 * w + k) * 1024, lo16, WS_H + (u32)(2 * gi * TB) + wrap(7 * w + k + 2 * w1.rot, 28) * 1024);
  }
  w1_read<9>(lds, ra1, W); w1_read<10>(lds, ra1 + 16384, W); w1_read<11>(lds, ra1 + 32768, W); w1_read<12>(lds, ra2, W); w1_read<13>(lds, ra2 + 16384, W);
  bar();

  const u32 bta = (u32)((8 * (G >> 1) + q4) * 64 + (G & 1) * 32 + p4 * 8);
  auto hbase = [](int g) AI { return g == 0 ? L_H0 : g == 1 ? L_H1 : g == 2 ? L_H2 : L_H3; };
  u32 csh, csl;
  auto colfin = [&]() AI {
    colsum(18, 28);
    const float t = hsum(cs0.x + cs1.x);
    const u32 hi = pk(t, t);
    csh = hh ? 0u : hi; csl = hh ? 0u : pk(t - bflo(hi), 0.f);
    PIN(csh); PIN(csl);
  };
  f32x4 tm; float gu[8], gt[8]; u32 od[4];
  auto center = [&](f32x16& acc, float m) AI {
    const u32 d = __float_as_uint(m);
    acc = mfma32(u32x4{csh, csl, 0, 0}, u32x4{d, d & 0xffffu, 0, 0}, acc);
  };
  auto geglu_step = [&](int g, f32x16& acc, int i) AI {
#pragma unroll
    for (int e = 0; e < 8; ++e) {
      const int s = i - (2 + (18 * e) / 7);
      if (s == 0) { gt[e] = acc[e] * acc[e]; gu[e] = op(acc[e] * acc[e + 8]); PIN(gt[e]); PIN(gu[e]); }
      if (s == 1) { gt[e] = tm.w * gt[e] + tm.z; PIN(gt[e]); }
      if (s == 2) { gt[e] = acc[e] * gt[e]; PIN(gt[e]); }
      if (s == 3) { gt[e] = __builtin_amdgcn_exp2f(gt[e]); PIN(gt[e]); }
      if (s == 4) { gt[e] = gt[e] * tm.y + tm.y; PIN(gt[e]); }
      if (s == 5) { gt[e] = __builtin_amdgcn_rcpf(gt[e]); PIN(gt[e]); }
      if (s == 6) { gu[e] = gu[e] * gt[e]; PIN(gu[e]); }
      if (s == 7 && (e & 1)) { od[e >> 1] = pk(gu[e - 1], gu[e]); PIN(od[e >> 1]); }
    }
    if (i == 28) {
      __builtin_amdgcn_raw_buffer_store_b128(u32x4{od[0], od[1], od[2], od[3]}, rw, gso, gofs(g), SC1);
    }
  };
  constexpr int PF = 4;
  u32x4 B[PF];
  u32 ba = opq(hbase(0) + bta);
  auto chain = [&](int g, f32x16& acc, auto side) AI {
    const u32 bn = opq(hbase(g < 3 ? g + 1 : g) + bta);
    sfor<28>([&](auto kc) AI {
      constexpr int k = decltype(kc)::value;
      acc = mfma32(W[k], B[k % PF], k == 0 ? f32x16{} : acc);
      SB();
      side(k);
      if (k + PF < 28) B[k % PF] = cat(trd(lds, ba + (k + PF) * 1024), trd(lds, ba + (k + PF) * 1024 + 256));
      else if (g < 3) B[k % PF] = cat(trd(lds, bn + (k + PF - 28) * 1024), trd(lds, bn + (k + PF - 28) * 1024 + 256));
      SB();
    });
    ba = bn;
  };
  auto flag3 = [&](int g) AI { setflag(ws, WS_F3 + (u32)(((gi * 4 + g) * 14) + gj) * 128, tag); };
  const int ri = 2 * xc + sc / 14, rq2 = (sc / 7) & 1, n2 = sc % 7;
  const u32x4 rd = srd(gwd, D_ * F_ * 2);
  const u32 wdv = (u32)(((224 * w + r8) * D_ + 64 * n2) * 2 + (((l & 7) ^ (((r8 >> 1) & 3) << 1)) << 4));
  const u32 blkR = w < 7 ? L_R + w * 4096 : L_R2;
  u32 wda[4], wdr[4];
#pragma unroll
  for (int nt = 0; nt < 4; ++nt) {
    const u32 a = (16 * (G >> 1) + 4 * (G & 1) + q4) * 128 + ((nt ^ (2 * (G & 1) + (q4 >> 1))) * 32) + p4 * 8;
    wda[nt] = opq(a + L_P + w * 4096); wdr[nt] = opq(a + blkR);
  }
  u32x4 Wd[7][4];
  auto wblk = [](int k) AI { return (k == 1 || k == 4) ? 1 : (k == 3 || k == 6) ? 2 : 0; };
  auto wd_dma = [&](int k, int i) AI {
    if (i == 0) dma_at(lb + (wblk(k) == 0 ? blkR : (wblk(k) == 2 ? L_Q : L_P) + w * 4096));
    dma_i<0>(i, rd, wdv, 4 * k * (8 * D_ * 2) + i * (8 * D_ * 2 - 1024));
  };
  auto wd_rd = [&](int k, int nt) AI {
    const u32 a = wblk(k) == 0 ? wdr[nt] : wda[nt] + (wblk(k) == 2 ? L_Q - L_P : 0);
    Wd[k][nt] = cat(trd(lds, a), trd(lds, a + 1024));
  };
  auto wd_read = [&](int k) AI { wd_rd(k, 0); wd_rd(k, 1); wd_rd(k, 2); wd_rd(k, 3); };
  const int j0 = (14 * w) >> 3, jn = ((14 * w + 13) >> 3) - j0;
  const u32 f3o = WS_F3 + (u32)(ri * 56 + j0 + ((l & 3) < jn ? (l & 3) : jn)) * 128;

  f32x16 accA, accB;
  u32x4 tm0, tm1, tm2, tm3, hr;
  u32x2 res[2];
  const u32 hro = opq(WS_H + (u32)((32 * hh + (w >> 2) + 2 * ri) * TB + (64 * n2 + 16 * (w & 3) + ((l >> 1) & 15)) * 64 + rq2 * 32 + (l & 1) * 16));
  const u32 s2o = WS_S2 + (u32)(2 * gi * 512 + tk * 16);
  const u32 hp = (u32)(w < 4 ? 4 * w : 4 + 3 * w) * 1024;
  u32 hs[4];
#pragma unroll
  for (int i = 0; i < 4; ++i) hs[i] = (wrap((hp >> 10) + i + 2 * w1.rot, 28) - i) * 1024;
  auto htb = [&](int g) AI { return WS_H + (u32)((32 * (g >> 1) + 2 * gi + (g & 1)) * TB); };
  auto h_dma = [&](int g, int i) AI {
    if (i == 0) dma_at(lb + hbase(g) + hp);
    if (i < 3 || w < 4) dma_i<SC1>(i, dw, lo16, htb(g) + hs[i]);
  };
  auto redo1 = [&](int g, f32x16& acc, u32 m) AI {
    if (RARE(__any(acc[0] != acc[0]))) {
      const u32 sc0 = L_SP + w * 1024;
      acc = f32x16{};
      const rsrc_t rg = mk(gwg, D_ * F_ * 2), ru = mk(gwu, D_ * F_ * 2);
#pragma unroll 1
      for (int k = 0; k < 28; ++k) {
        const u32 wo = (u32)(((16 * k + ((l >> 1) & 15)) * F_ + 128 * gj + 16 * w) * 2 + (l & 1) * 16);
        const u32x4 wg = __builtin_amdgcn_raw_buffer_load_b128(rg, wo, 0, 0), wu = __builtin_amdgcn_raw_buffer_load_b128(ru, wo, 0, 0);
        *(u32x4*)(lds + sc0 + lo16) = l < 32 ? wg : wu;
        const u32 wa = sc0 + (G & 1) * 512 + (8 * (G >> 1) + q4) * 32 + p4 * 8;
        const u32x4 a = cat(trd(lds, wa), trd(lds, wa + 128));
        u32x4 v;
#pragma unroll 1
        for (int it = 0; it < 400; ++it) { v = ldg(dw, lo16, htb(g) + k * 1024); vmw<0>(v); if (!__any(max4(v) == EMPTY)) break; }
        *(u32x4*)(lds + sc0 + lo16) = v;
        acc = mfma32(a, cat(trd(lds, sc0 + bta), trd(lds, sc0 + bta + 256)), acc);
      }
      center(acc, __uint_as_float(m));
    }
  };
  auto tm_fix = [&](u32x4& t, u32 so) AI {
    if (RARE(__any(max4(t) == EMPTY))) {
#pragma unroll 1
      for (int it = 0; it < 4000; ++it) { t = ldg(dw, s2o, so); vmw<0>(t); if (!__any(max4(t) == EMPTY)) break; }
    }
  };
  SB();
  if (w >= 4) {
    epilogue();
    SB();
    colfin();
  } else {
    colfin();
    SB();
    const u32 ha = opq(L_H0 + 7 * w * 1024 + lo16);
#pragma unroll 1
    for (int it = 0; it < 4000; ++it) {
      VMW(0);
      u32 m = 0;
#pragma unroll
      for (int k = 0; k < 7; ++k) { const u32x4 v = *(const u32x4*)(lds + ha + k * 1024); m = max(m, max4(v)); }
      if (!__any(m == EMPTY)) break;
#pragma unroll
      for (int k = 0; k < 7; ++k) dma<SC1>(dw, lb + L_H0 + (7 * w + k) * 1024, lo16, WS_H + (u32)(2 * gi * TB) + wrap(7 * w + k + 2 * w1.rot, 28) * 1024);
    }
    VMW(0);
  }
  __builtin_amdgcn_s_setprio(1);
  bar();
#pragma unroll
  for (int k = 0; k < PF; ++k) B[k] = cat(trd(lds, ba + k * 1024), trd(lds, ba + k * 1024 + 256));
  SB();
  chain(0, accA, [&](int k) AI {
    if (k <= 6 && !(k & 1)) h_dma(1, k >> 1);
    if (k == 8) tm0 = ldg(dw, s2o, 0);
    if (k == 10 || k == 12 || k == 14) __builtin_amdgcn_raw_buffer_store_b128(u32x4{EMPTY, EMPTY, EMPTY, EMPTY}, rw, gso, gofs((k - 8) >> 1), SC1);
    if (k >= 16 && k <= 25 && k % 3 == 1) wd_dma(0, (k - 16) / 3);
    if (k == 23) { VMW(7); RBAR(); if (tid == 0 && b == 0) setflag(ws, WS_E, tag); }
    if (k == 27) { vmw<7>(tm0); tm_fix(tm0, 0); }
  });
  center(accA, __uint_as_float(tm0.x));
  tm = __builtin_bit_cast(f32x4, tm0);
  SB();
  chain(1, accB, [&](int k) AI {
    if (k == 1) RBAR();
    geglu_step(0, accA, k);
    if (k == 2) tm1 = ldg(dw, s2o, 512);
    if (k == 3 || k == 5) wd_dma(1, (k - 3) >> 1);
    if (k >= 7 && k <= 10) h_dma(2, k - 7);
    if (k == 12) { VMW(6); dma_at(lb + L_P + w * 4096); }
    if (k == 13 || k == 15) wd_dma(1, (k - 9) >> 1);
    if (k >= 12 && k <= 18 && !(k & 1)) wd_rd(0, (k - 12) >> 1);
    if (k == 17) { vmw<7>(tm1); tm_fix(tm1, 512); }
    if (k >= 21 && (k & 1)) wd_dma(2, (k - 21) >> 1);
    if (k == 23) { VMW(4); RBAR(); }
  });
  center(accB, __uint_as_float(tm1.x));
  geglu_step(0, accA, 28);
  tm = __builtin_bit_cast(f32x4, tm1);
  SB();
  chain(2, accA, [&](int k) AI {
    if (k == 1) RBAR();
    if (k == 2) redo1(1, accB, tm1.x);
    geglu_step(1, accB, k);
    if (k <= 6 && !(k & 1)) h_dma(3, k >> 1);
    if (k >= 3 && k <= 9 && (k & 1)) { if (k == 3) VMW(7); wd_rd(1, (k - 3) >> 1); }
    if (k == 7) hr = ldg(dw, hro, 0);
    if (k == 8) { tm2 = ldg(dw, s2o, 32 * 512); tm3 = ldg(dw, s2o, 33 * 512); }
    if (k >= 10 && k <= 16 && !(k & 1)) wd_dma(3, (k - 10) >> 1);
    if (k >= 18 && k <= 27 && k % 3 == 0) wd_dma(4, (k - 18) / 3);
    if (k == 13) { VMW(8); RBAR(); if (tid == 0) flag3(0); }
    if (k == 23) { vmw<6>(tm2); PIN(tm3); PIN(hr); RBAR(); }
    if (k == 25) tm_fix(tm2, 32 * 512);
    if (k == 26) tm_fix(tm3, 33 * 512);
  });
  center(accA, __uint_as_float(tm2.x));
  geglu_step(1, accB, 28);
  tm = __builtin_bit_cast(f32x4, tm2);
  SB();
  const u32 gb = WS_G + (u32)((2 * ri * 2 + rq2) * 56 + 7 * w) * 1024;
  constexpr int NS = 6 + 2;
  u32x4 Hf[NS];
  auto hl = [&](int t) AI { Hf[t % NS] = ldg(dw, lo16, gb + gofs(t / 7) + (t % 7) * 1024); };
  chain(3, accB, [&](int k) AI {
    if (k == 2) redo1(2, accA, tm2.x);
    geglu_step(2, accA, k);
    if (k >= 5 && k < 13) { if (k == 5) VMW(5); wd_rd(2 + (k - 5) / 4, (k - 5) & 3); }
    if (k == 10) fl = flag_ld(dw, f3o);
    if (k >= 12 && k <= 18 && !(k & 1)) wd_dma(5, (k - 12) >> 1);
    if (k == 13) VMW(3);
    if (k >= 13 && k < 21 && (k & 1)) wd_rd(4, (k - 13) >> 1);
    if (k >= 19 && k <= 22) wd_dma(6, k - 19);
    if (k == 23) { VMWP(8, fl); if (RARE(!__all(fl == tag))) poll(ws, f3o, true, tag); }
    if (k >= 23) hl(k - 23);
  });
  center(accB, __uint_as_float(tm3.x));
  geglu_step(2, accA, 28);
  tm = __builtin_bit_cast(f32x4, tm3);
  redo1(3, accB, tm3.x);
#pragma unroll
  for (int e = 0; e < 8; ++e) {
    const float a = accB[e], u = accB[e + 8];
    gu[e] = (a * u) * __builtin_amdgcn_rcpf(__builtin_amdgcn_exp2f(a * (tm.w * (a * a) + tm.z)) * tm.y + tm.y);
  }
#pragma unroll
  for (int e = 0; e < 4; ++e) od[e] = pk(gu[2 * e], gu[2 * e + 1]);
  geglu_step(3, accB, 28);
  __builtin_amdgcn_s_setprio(0);
  SB();
  f32x4 acc[2][4];
  const rsrc_t ro = mk(gout, S_ * D_ * 2);
  auto pbuf = [](int g) AI { return g == 3 ? L_T : g == 1 ? L_Q : L_P; };
  const u32 pa = (u32)(w * 4096 + i16 * 256), tf = 2 * w + hh;
  const u32 fa = opq(L_P + tf * 256 + (((tk >> 1) ^ (tf & 7)) << 4) + (tk & 1) * 8), ft = opq(fa + (L_T - L_P));
  const u32 oa = opq((u32)(((64 * ri + 16 * rq2 + tf) * D_ + 64 * n2 + 2 * tk) * 2));
  u32 pq[4];
#pragma unroll
  for (int nt = 0; nt < 4; ++nt) pq[nt] = opq(L_P + pa + (((4 * nt + G) ^ (i16 & 7)) << 4));
  auto put = [&](int g, int nt) AI {
    f32x4& v = acc[g & 1][nt];
    if (w == 4 * (g & 1) + nt) { const u32x2 r = res[g >> 1]; v.x += bflo(r.x); v.y = op(v.y + bfhi(r.x)); v.z += bflo(r.y); v.w = op(v.w + bfhi(r.y)); }
    *(f32x4*)(lds + (g == 3 ? opq(pq[nt] + (L_T - L_P)) : pq[nt] + (pbuf(g) - L_P))) = v;
  };
  float2 p[8];
  auto fin_rd = [&](int g) AI {
#pragma unroll
    for (int n = 0; n < 8; ++n) { const u32x2 x = *(const u32x2*)(lds + (g == 3 ? ft : fa + (pbuf(g) - L_P)) + n * 4096); p[n].x = __uint_as_float(x.x); p[n].y = __uint_as_float(x.y); }
  };
  auto fin_st = [&](int g) AI {
    const float a = ((p[0].x + p[1].x) + (p[2].x + p[3].x)) + ((p[4].x + p[5].x) + (p[6].x + p[7].x));
    const float c = ((p[0].y + p[1].y) + (p[2].y + p[3].y)) + ((p[4].y + p[5].y) + (p[6].y + p[7].y));
    __builtin_amdgcn_raw_buffer_store_b32(pk(a, c), ro, oa, (32 * (g >> 1) + (g & 1)) * (32 * D_ * 2), 2);
  };
  auto redo = [&](int g) AI {
    if (RARE(__any(acc[g & 1][0].x != acc[g & 1][0].x))) {
      u32x4 v[7];
#pragma unroll 1
      for (int it = 0; it < 4000; ++it) {
        u32 m = 0;
#pragma unroll
        for (int k = 0; k < 7; ++k) v[k] = ldg(dw, lo16, gb + gofs(g) + k * 1024);
#pragma unroll
        for (int k = 0; k < 7; ++k) { vmw<0>(v[k]); m = max(m, max4(v[k])); }
        if (!__any(m == EMPTY)) break;
      }
#pragma unroll
      for (int k = 0; k < 7; ++k)
#pragma unroll
        for (int nt = 0; nt < 4; ++nt) acc[g & 1][nt] = mfma16(Wd[k][nt], v[k], k == 0 ? f32x4{} : acc[g & 1][nt]);
    }
  };
#pragma unroll
  for (int h = 5; h < 6; ++h) hl(h);
  if (RARE(__any(max4(hr) == EMPTY))) {
#pragma unroll 1
    for (int it = 0; it < 4000; ++it) { hr = ldg(dw, hro, 0); vmw<0>(hr); if (!__any(max4(hr) == EMPTY)) break; }
  }
  *(u32x4*)(lds + L_SP + w * 1024 + lo16) = hr;
  { const u32 a = L_SP + w * 1024 + (4 * G + q4) * 32 + p4 * 8; res[0] = trd(lds, a); res[1] = trd(lds, a + 512); }
  VMW(6 + 2);
  wd_read(5);
  SB();
  sfor<28>([&](auto tc) AI {
    constexpr int t = decltype(tc)::value, g = t / 7, k = t % 7;
    constexpr auto last = [](int u) { return u < 28 - 6 ? u + 6 : 27; };
    constexpr int n = last(t) - t + (t < 5 ? 2 : 0) + (t > 14 && t - 6 <= 14 ? 1 : 0) + (t > 21 ? 1 : 0);
    if (t < 28 - 6) hl(t + 6);
    vmw<n>(Hf[t % NS]);
#pragma unroll
    for (int nt = 0; nt < 4; ++nt) acc[g & 1][nt] = mfma16(Wd[k][nt], Hf[t % NS], k == 0 ? f32x4{} : acc[g & 1][nt]);
    SB();
    if (t < 4) wd_rd(6, t);
    if (t == 15 || t == 22) redo(g - 1);
    if (g > 0 && k >= 1 && k <= 4) put(g - 1, k - 1);
    if (g > 0 && k == 5) { bar(); fin_rd(g - 1); }
    if (t == 14 || t == 21) fin_st(g - 2);
    SB();
  });
  fin_st(2);
  redo(3);
  put(3, 0); put(3, 1); put(3, 2); put(3, 3);
  bar();
  fin_rd(3); fin_st(3);
}

}

extern "C" void kernel_launch(void* const* d_in, const int* in_sizes, int n_in, void* d_out, int out_size, void* d_ws, size_t ws_size,
                              hipStream_t stream) {
  h3_ffn<<<256, 512, 0, stream>>>((const char*)d_in[0], (const char*)d_in[1], (const char*)d_in[2], (const char*)d_in[3], (const char*)d_in[4],
                                  (const char*)d_in[5], (const char*)d_in[7], (const char*)d_in[8], (const char*)d_in[9], (char*)d_out, (char*)d_ws);
}
```

```cpp
#include <hip/hip_runtime.h>
#include <stdint.h>

namespace {

#define LAS __attribute__((address_space(3)))
#define GAS __attribute__((address_space(1)))
#define DI __device__ __forceinline__
#define AI __attribute__((always_inline))
#define RARE(x) __builtin_expect(!!(x), 0)
typedef uint32_t u32;
typedef unsigned long long u64;
typedef u32 u32x2 __attribute__((ext_vector_type(2)));
typedef u32 u32x4 __attribute__((ext_vector_type(4)));
typedef short s16x4 __attribute__((ext_vector_type(4)));
typedef short s16x8 __attribute__((ext_vector_type(8)));
typedef float f32x4 __attribute__((ext_vector_type(4)));
typedef float f32x16 __attribute__((ext_vector_type(16)));
typedef __bf16 bf2 __attribute__((ext_vector_type(2)));
typedef __amdgpu_buffer_rsrc_t rsrc_t;
#define RLX_AGENT __ATOMIC_RELAXED, __HIP_MEMORY_SCOPE_AGENT

constexpr int S_ = 2048, D_ = 448, F_ = 1792;
constexpr u32 TB = 448 * 64;
constexpr u32 WS_E  = 0;
constexpr u32 WS_F1 = 128;
constexpr u32 WS_F3 = WS_F1 + 256 * 128;
constexpr u32 WS_F2 = WS_F3 + 896 * 128;
constexpr u32 WS_S2 = 256 * 1024 + 16384;
constexpr u32 WS_ST = 256 * 1024;
constexpr u32 WS_U  = WS_S2 + 32768;
constexpr u32 WS_X  = WS_U + 64 * TB;
constexpr u32 WS_H  = WS_X + 64 * TB;
constexpr u32 WS_G  = WS_H + 64 * TB;
constexpr u32 WS_END = WS_G + 64 * 112 * 1024;
constexpr int SC1 = 16;
constexpr u32 OOB = 0xFFFFFF00u;
constexpr u32 ONES = 0x3f803f80u;
constexpr u32 EMPTY = 0xffffffffu;

constexpr u32 L_K = 0, L_V = 9216, L_X = 17408, L_SK = 25600;
constexpr u32 L_RED = 0, L_FC = 32768, FCS = 4160, FCH = 4 * FCS;
constexpr u32 L_U = L_FC + 2 * FCH, UCH = 4608, L_RING = L_U + 2 * UCH;
constexpr u32 L_H0 = 32768, L_H1 = L_H0 + TB, L_H2 = 0, L_H3 = L_H1 + TB + 32768, L_SP = 155648;
constexpr u32 L_P = TB, L_Q = L_P + 32768, L_R = L_Q + 32768, L_R2 = L_H3 + TB;
constexpr u32 L_T = L_H3;
constexpr u32 LDS_BYTES = 163840;

DI float bflo(u32 x) { return __uint_as_float(x << 16); }
DI float bfhi(u32 x) { return __uint_as_float(x & 0xffff0000u); }
DI u32 pk(float lo, float hi) { bf2 p; p.x = (__bf16)lo; p.y = (__bf16)hi; return __builtin_bit_cast(u32, p); }
DI rsrc_t mk(const void* p, u32 bytes) { return __builtin_amdgcn_make_buffer_rsrc((void*)p, 0, bytes, 0x00020000); }
DI u32x2 trd(const char* lds, u32 a) { return __builtin_bit_cast(u32x2, __builtin_amdgcn_ds_read_tr16_b64_v4i16((LAS s16x4*)(lds + a))); }
DI u32x4 cat(u32x2 a, u32x2 b) { return u32x4{a.x, a.y, b.x, b.y}; }
DI u32x4 srd(const void* p, u32 bytes) { const u64 a = (u64)p; return u32x4{(u32)a, (u32)(a >> 32) & 0xffffu, bytes, 0x00020000u}; }
template <int AUX = 0> DI void dma(u32x4 r, u32 ldsoff, u32 voff, u32 soff) {
  if (AUX) asm volatile("s_mov_b32 m0, %0\n\ts_nop 0\n\tbuffer_load_dwordx4 %1, %2, %3 offen sc1 lds" :: "s"(ldsoff), "v"(voff), "s"(r), "s"(soff) : "memory");
  else asm volatile("s_mov_b32 m0, %0\n\ts_nop 0\n\tbuffer_load_dwordx4 %1, %2, %3 offen lds" :: "s"(ldsoff), "v"(voff), "s"(r), "s"(soff) : "memory");
}
DI void dma_at(u32 ldsoff) { asm volatile("s_mov_b32 m0, %0\n\ts_nop 0" :: "s"(ldsoff) : "memory"); }
template <int OFF, int AUX> DI void dma_go(u32x4 r, u32 voff, u32 soff) {
  if (AUX) asm volatile("buffer_load_dwordx4 %0, %1, %2 offen offset:%3 sc1 lds" :: "v"(voff), "s"(r), "s"(soff), "n"(OFF) : "memory");
  else asm volatile("buffer_load_dwordx4 %0, %1, %2 offen offset:%3 lds" :: "v"(voff), "s"(r), "s"(soff), "n"(OFF) : "memory");
}
template <int AUX> DI void dma_i(int i, u32x4 r, u32 voff, u32 soff) {
  if (i == 0) dma_go<0, AUX>(r, voff, soff); else if (i == 1) dma_go<1024, AUX>(r, voff, soff); else if (i == 2) dma_go<2048, AUX>(r, voff, soff); else dma_go<3072, AUX>(r, voff, soff);
}
DI float op(float x) { asm("" : "+v"(x)); return x; }
DI u32 opq(u32 x) { asm volatile("" : "+v"(x)); return x; }
#define PIN(x) asm volatile("" : "+v"(x))
DI f32x4 mfma16(u32x4 a, u32x4 b, f32x4 c) { return __builtin_amdgcn_mfma_f32_16x16x32_bf16(__builtin_bit_cast(s16x8, a), __builtin_bit_cast(s16x8, b), c, 0, 0, 0); }
DI f32x16 mfma32(u32x4 a, u32x4 b, f32x16 c) { return __builtin_amdgcn_mfma_f32_32x32x16_bf16(__builtin_bit_cast(s16x8, a), __builtin_bit_cast(s16x8, b), c, 0, 0, 0); }
DI f32x4 mfma4(u32x2 a, u32x2 b, f32x4 c) { return __builtin_amdgcn_mfma_f32_4x4x4bf16_1k(__builtin_bit_cast(s16x4, a), __builtin_bit_cast(s16x4, b), c, 0, 0, 0); }
DI float dot2(u32 a, u32 b, float c) { return __builtin_amdgcn_fdot2_f32_bf16(__builtin_bit_cast(bf2, a), __builtin_bit_cast(bf2, b), c, false); }
#define VMW(n) asm volatile("s_waitcnt vmcnt(%0)" :: "n"(n) : "memory")
#define LG0() asm volatile("s_waitcnt lgkmcnt(0)" ::: "memory")
#define SB() __builtin_amdgcn_sched_barrier(0)
DI void bar() { asm volatile("s_waitcnt lgkmcnt(0)\n\ts_barrier" ::: "memory"); }
#define RBAR() asm volatile("s_barrier" ::: "memory")
DI float hsum(float v) {
  const auto r = __builtin_amdgcn_permlane32_swap(__float_as_uint(v), __float_as_uint(v), false, false);
  return __uint_as_float(r[0]) + __uint_as_float(r[1]);
}
template <int SLEEP = 1> DI void poll(char* ws, u32 off, bool act, u64 tag) {
  bool ok = !act;
#pragma unroll 1
  for (int it = 0; it < 20000; ++it) {
    u64 x = tag;
    if (!ok) x = __hip_atomic_load((GAS u64*)(ws + off), RLX_AGENT);
    ok = (x == tag);
    if (__all(ok)) break;
    __builtin_amdgcn_s_sleep(SLEEP);
  }
  asm volatile("" ::: "memory");
}
DI u32 max4(u32x4 v) { return max(max(v.x, v.y), max(v.z, v.w)); }
DI u64 flag_ld(u32x4 r, u32 voff) { u64 v; asm volatile("buffer_load_dwordx2 %0, %1, %2, 0 offen sc1" : "=v"(v) : "v"(voff), "s"(r) : "memory"); return v; }
DI u32x4 ldg(u32x4 r, u32 voff, u32 soff) { u32x4 v; asm volatile("buffer_load_dwordx4 %0, %1, %2, %3 offen sc1" : "=v"(v) : "v"(voff), "s"(r), "s"(soff) : "memory"); return v; }
template <int N> DI void vmw(u32x4& x) { asm volatile("s_waitcnt vmcnt(%1)" : "+v"(x) : "n"(N) : "memory"); }
#define VMWP(n, x) asm volatile("s_waitcnt vmcnt(" #n ")" : "+v"(x) :: "memory")
DI void setflag(char* ws, u32 off, u64 tag) { __hip_atomic_store((GAS u64*)(ws + off), tag, RLX_AGENT); }


DI float xsum(float a, float c) {
  const auto r = __builtin_amdgcn_permlane32_swap(__float_as_uint(a), __float_as_uint(c), false, false);
  return __uint_as_float(r[0]) + __uint_as_float(r[1]);
}
DI f32x4 ln2_terms(float t) {
  const auto r = __builtin_amdgcn_permlane32_swap(__float_as_uint(t), __float_as_uint(t), false, false);
  const float mean = __uint_as_float(r[0]) * (1.f / D_);
  const float V = fmaxf(__uint_as_float(r[1]) * (1.f / D_) - mean * mean, 0.f) + 1e-5f;
  const float rs = __builtin_amdgcn_rsqf(V);
  const float K1 = -2.f * 0.7978845608028654f * 1.4426950408889634f * rs;
  const float lo = -mean - bflo(pk(-mean, 0.f));
  return f32x4{__uint_as_float(pk(-mean, lo)), V, K1, (K1 * rs) * (0.044715f * rs)};
}
template <int N> struct IC { static constexpr int value = N; };
template <int N, int I = 0, class F> DI void sfor(F&& f) { if constexpr (I < N) { f(IC<I>{}); sfor<N, I + 1>(f); } }
struct W1 { u32x4 rg, ru; u32 vg, vu, ra, lb, rot; };
DI u32 wrap(u32 x, u32 n) { return x >= n ? x - n : x; }
template <int T> DI void w1_g(const W1& c, u32 base) { dma(c.rg, c.lb + base, c.vg, wrap(T + c.rot, 14) * (32 * F_ * 2)); }
template <int T> DI void w1_u(const W1& c, u32 base) { dma(c.ru, c.lb + base + 8192, c.vu, wrap(T + c.rot, 14) * (32 * F_ * 2)); }
template <int T> DI void w1_issue(const W1& c, u32 base) { w1_g<T>(c, base); w1_u<T>(c, base); }
template <int T> DI void w1_read(const char* lds, u32 a, u32x4 (&W)[28]) {
#pragma unroll
  for (int kk = 0; kk < 2; ++kk) W[2 * T + kk] = cat(trd(lds, a + kk * 4096), trd(lds, a + kk * 4096 + 1024));
}
DI float x8sum(float v) {
  v += __builtin_amdgcn_update_dpp(0.f, v, 0xb1, 0xf, 0xf, true);
  v += __builtin_amdgcn_update_dpp(0.f, v, 0x4e, 0xf, 0xf, true);
  v += __builtin_amdgcn_update_dpp(0.f, v, 0x141, 0xf, 0xf, true);
  return v;
}

__global__ __launch_bounds__(512, 2) void h3_ffn(const char* __restrict__ gq, const char* __restrict__ gk, const char* __restrict__ gv,
                                                 const char* __restrict__ gsk, const char* __restrict__ gf, const char* __restrict__ gD,
                                                 const char* __restrict__ gwg, const char* __restrict__ gwu, const char* __restrict__ gwd,
                                                 char* __restrict__ gout, char* ws) {
  __shared__ __attribute__((aligned(16))) char lds[LDS_BYTES];
  const int tid = threadIdx.x, l = tid & 63, w = __builtin_amdgcn_readfirstlane(tid >> 6);
  const int b = blockIdx.x, xc = b & 7, sl = b >> 3;
  const int tb64 = 4 * xc + (sl >> 3), role = sl & 7;
  const int sc = (sl >> 3) * 7 + role;
  asm volatile("" :: "s"(gq), "s"(gk), "s"(gv), "s"(gsk), "s"(gf), "s"(gD), "s"(gwg), "s"(gwu), "s"(gwd), "s"(gout), "s"(ws));
  u64 pc0; asm volatile("s_getpc_b64 %0" : "=s"(pc0));
  u32 ipf = 0;
  if (role == 7 && w >= 4) asm volatile("global_load_dword %0, %1, off" : "=v"(ipf) : "v"((pc0 & ~255ull) + (u64)min((u32)(tid - 256) * 128u, (u32)28672)) : "memory");
  SB();
  const rsrc_t rw = mk(ws, WS_END);
  const int G = l >> 4, i16 = l & 15, q4 = i16 >> 2, p4 = l & 3, r8 = l >> 3;
  auto mktag = [&](u64 e0) AI { return ((u64)__builtin_amdgcn_readfirstlane((u32)e0) | ((u64)__builtin_amdgcn_readfirstlane((u32)(e0 >> 32)) << 32)) * 6364136223846793005ull + 1442695040888963407ull; };
  if (RARE(role == 7)) {
    const rsrc_t rq = mk(gq, S_ * D_ * 2);
    const int row = 64 * tb64 + 8 * w + r8;
    u32x4 xs[7];
#pragma unroll
    for (int i = 0; i < 7; ++i) xs[i] = __builtin_amdgcn_raw_buffer_load_b128(rq, (u32)(row * 896 + (l & 7) * 16 + i * 128), 0, 0);
    const u64 e0 = __hip_atomic_load((GAS u64*)(ws + WS_E), RLX_AGENT);
    __builtin_amdgcn_raw_buffer_store_b128(u32x4{EMPTY, EMPTY, EMPTY, EMPTY}, rw, tid < 64 ? WS_S2 + (u32)((tb64 + (tid & 32)) * 512 + (tid & 31) * 16) : OOB, 0, SC1);
    SB();
    u32 tlb = 0;
    if (w == 7) {
      const u32 n = (WS_END + 1048576 - 1) / 1048576, m2 = (S_ * D_ * 2 + 1048576 - 1) / 1048576;
      const u32 i = min((u32)l, n + 4 * m2 - 1);
      const char* p = i < n ? ws + (size_t)i * 1048576 + 64 : (i < n + m2 ? (const char*)gout : i < n + 2 * m2 ? gwd : i < n + 3 * m2 ? gwg : gwu) + ((i - n) % m2) * 1048576;
      asm volatile("global_load_dword %0, %1, off" : "=v"(tlb) : "v"(p) : "memory");
    }
    SB();
    const u64 tag = mktag(e0);
    float s[4] = {};
#pragma unroll
    for (int i = 0; i < 7; ++i)
#pragma unroll
      for (int e = 0; e < 4; ++e) s[e] += bflo(xs[i][e]) + bfhi(xs[i][e]);
    const float mean = x8sum((s[0] + s[1]) + (s[2] + s[3])) * (1.f / D_);
    float v[4] = {};
#pragma unroll
    for (int i = 0; i < 7; ++i)
#pragma unroll
      for (int e = 0; e < 4; ++e) { const float d0 = bflo(xs[i][e]) - mean, d1 = bfhi(xs[i][e]) - mean; v[e] += d0 * d0; v[e] += d1 * d1; }
    const float rstd = __builtin_amdgcn_rsqf(x8sum((v[0] + v[1]) + (v[2] + v[3])) * (1.f / D_) + 1e-5f);
    if ((l & 7) == 0) *(u32x2*)(lds + ((r8 >> 1) & 3) * 128 + w * 16 + (r8 & 1) * 8) = u32x2{__float_as_uint(mean), __float_as_uint(rstd)};
    bar();
    if (w == 0) {
      __builtin_amdgcn_raw_buffer_store_b128(*(const u32x4*)(lds + (l & 31) * 16), rw, l < 32 ? WS_ST + (u32)((tb64 >> 2) * 2048 + (l >> 3) * 512 + (tb64 & 3) * 128 + (l & 7) * 16) : OOB, 0, SC1);
      VMW(0);
    }
    if (tid == 0) setflag(ws, WS_F1 + (tb64 * 8 + 7) * 128, tag);
    bar();
#pragma unroll
    for (int i = 0; i < 7; ++i) {
      const u32 o = (u32)(i * 8192 + tid * 16);
      __builtin_amdgcn_raw_buffer_store_b128(u32x4{EMPTY, EMPTY, EMPTY, EMPTY}, rw, o < TB ? o : o + 31 * TB, WS_H + (u32)tb64 * TB, SC1);
    }
    VMW(0); PIN(tlb); PIN(ipf); bar();
    if (tid == 0) setflag(ws, WS_F2 + tb64 * 128, tag);
    {
      const rsrc_t rdw = mk(gwd, D_ * F_ * 2);
      const u32 t = __builtin_amdgcn_raw_buffer_load_b32(rdw, tid < 392 ? (u32)((tb64 * 392 + tid) * 128) : OOB, 0, 0);
      asm volatile("" :: "v"(t));
    }
    const u32x4 dw = srd(ws, WS_END);
    const u32 lb = (u32)(size_t)(LAS char*)lds;
    __builtin_amdgcn_s_sleep(8);
#pragma unroll 1
    for (int half = 0; half < 2; ++half) {
      const int tb = 32 * half + tb64;
#pragma unroll 1
      for (int it = 0; it < 4000; ++it) {
#pragma unroll
        for (int k = 0; k < 4; ++k) if (k < 3 || l < 32) dma<SC1>(dw, lb + 32768 + w * 3584 + k * 1024, l * 16, WS_H + (u32)(tb * TB + w * 3584 + k * 1024));
        VMW(0);
        u32 m = 0;
#pragma unroll
        for (int k = 0; k < 4; ++k) m = max(m, max4(*(const u32x4*)(lds + 32768 + w * 3584 + (k < 3 ? k * 1024 + l * 16 : 3072 + (l & 31) * 16))));
        if (!__any(m == EMPTY)) break;
        __builtin_amdgcn_s_sleep(3);
      }
      float s1 = 0.f, s2 = 0.f;
#pragma unroll
      for (int i = 0; i < 7; ++i) {
        const u32x2 t = trd(lds, 32768 + (u32)((56 * w + 4 * (G >> 1) + q4) * 64 + (G & 1) * 32 + p4 * 8) + i * 512);
        s1 = dot2(t.x, ONES, s1); s2 = dot2(t.x, t.x, s2); s1 = dot2(t.y, ONES, s1); s2 = dot2(t.y, t.y, s2);
      }
      *(float*)(lds + 20480 + half * 2048 + l * 32 + w * 4) = xsum(s1, s2);
      bar();
      if (w == 0) {
        const f32x4 a = *(const f32x4*)(lds + 20480 + half * 2048 + l * 32), c = *(const f32x4*)(lds + 20480 + half * 2048 + l * 32 + 16);
        __builtin_amdgcn_raw_buffer_store_b128(__builtin_bit_cast(u32x4, ln2_terms(((a.x + a.y) + (a.z + a.w)) + ((c.x + c.y) + (c.z + c.w)))), rw, l < 32 ? WS_S2 + (u32)(tb * 512 + l * 16) : OOB, 0, SC1);
      }
    }
    return;
  }
  const int cp = xc * 28 + sc, cch = w & 1, vp = w >> 1;
  const int ech = (l >> 2) & 1;
  const u32 eo = (u32)((8 * w + r8) * TB + (2 * cp + ech) * 64 + p4 * 16);
  u32 pg = 0;
  if (w == 2) {
    const int j = l & 7;
    const size_t ro = (size_t)tb64 * (64 * 896) + role * 128;
    const char* p = j == 0 ? gk + ro : j == 1 ? gv + ro : j == 2 ? gq + ro : j == 3 ? gf + (size_t)cp * 8192 : j == 4 ? gwg : j == 5 ? gwu : j == 6 ? (const char*)ws + WS_U + (size_t)tb64 * (2 * TB) : (const char*)ws + 64;
    asm volatile("global_load_dword %0, %1, off" : "=v"(pg) : "v"(p) : "memory");
  }
  u32x4 fhi; u32x2 flo; u32 Dh; u64 e0;
  const u32 lb = (u32)(size_t)(LAS char*)lds;
  const u32x4 dw = srd(ws, WS_END);
  {
    const u32x4 rk = srd(gk, S_ * D_ * 2), rv = srd(gv, S_ * D_ * 2);
    const int c8 = l & 7, s0 = 64 * tb64;
    const int swv = ((r8 >> 1) & 1) | ((w & 1) << 1), swk = swv ^ 2;
    const u32 g0 = (u32)((s0 + 8 * w + r8) * 896 + role * 128);
    dma(rk, lb + L_K + 1024 + w * 1024, g0 + ((c8 ^ (swk << 1)) << 4), 0);
    dma(rv, lb + L_V + w * 1024, g0 + ((c8 ^ (swv << 1)) << 4), 0);
    SB();
    if (w == 0) dma(rk, lb + L_K, g0 - 8 * 896 + ((c8 ^ (swv << 1)) << 4), 0);
    if (w == 1) dma(srd(gsk, D_ * 8), lb + L_SK, (u32)(512 * role + 16 * l), 0);
    dma(srd(gq, S_ * D_ * 2), lb + L_X + w * 1024, g0 + ((c8 ^ (swv << 1)) << 4), 0);
    const u32x4 rf = srd(gf, S_ * D_ * 2);
    const int fq = vp * 64 + l;
    const u32 fo = (u32)((2 * cp + cch) * 4096 + 16 * fq);
    asm volatile("buffer_load_dwordx4 %0, %2, %4, 0 offen\n\tbuffer_load_dwordx2 %1, %3, %4, 0 offen" : "=&v"(fhi), "=&v"(flo) : "v"(fo), "v"(fq > 0 ? fo - 8 : OOB), "s"(rf) : "memory");
    asm volatile("buffer_load_ushort %0, %1, %2, 0 offen" : "=v"(Dh) : "v"((u32)((2 * cp + ((l >> 2) & 1)) * 2)), "s"(srd(gD, D_ * 2)) : "memory");
    e0 = flag_ld(dw, WS_E);
    SB();
  }
  const int gx = (xc >> 1) * 28 + sc, gi = 8 * (xc & 1) + (gx & 7), gj = gx >> 3;
  W1 w1;
  {
    const u32 v0 = (u32)(((4 * w + G) * F_ + 128 * gj) * 2);
    w1.rg = srd(gwg, D_ * F_ * 2); w1.ru = srd(gwu, D_ * F_ * 2); w1.lb = lb + w * 1024;
    w1.rot = (7 * (xc & 1) + ((7 * (gx & 3)) >> 2));
    w1.vg = v0 + ((i16 ^ (4 * G)) << 4); w1.vu = v0 + ((i16 ^ (4 * G + 2)) << 4);
    w1.ra = (u32)((G & 1) * 8192 + (8 * (G >> 1) + q4) * 256 + ((w ^ (2 * q4 + (G & 1))) * 32) + p4 * 8);
  }
  w1_issue<0>(w1, L_FC); w1_issue<1>(w1, L_FC + 16384);
  VMW(9); PIN(pg); bar();
  {
    const int tbl = w >> 2, chg = w & 3;
    const u32 rb = (u32)((32 * tbl + 8 * G + q4) * 128 + p4 * 8);
    const u32 sv = (u32)((chg ^ ((q4 >> 1) | ((G & 1) << 1))) * 32), sk2 = sv ^ 64;
    const u32x2 km = trd(lds, L_K + rb + 512 + sv), k0 = trd(lds, L_K + rb + 1024 + sk2), k1 = trd(lds, L_K + rb + 1536 + sk2);
    const u32x2 v0 = trd(lds, L_V + rb + sv), v1 = trd(lds, L_V + rb + 512 + sv);
    const u32x2 skv = *(const u32x2*)(lds + L_SK + (16 * chg + i16) * 8);
    const float kk[11] = {bfhi(km.x), bflo(km.y), bfhi(km.y), bflo(k0.x), bfhi(k0.x), bflo(k0.y), bfhi(k0.y), bflo(k1.x), bfhi(k1.x), bflo(k1.y), bfhi(k1.y)};
    const float vv[8] = {bflo(v0.x), bfhi(v0.x), bflo(v0.y), bfhi(v0.y), bflo(v1.x), bfhi(v1.x), bflo(v1.y), bfhi(v1.y)};
    const float s0 = bflo(skv.x), s1 = bfhi(skv.x), s2 = bflo(skv.y), s3 = bfhi(skv.y);
    float u[8];
#pragma unroll
    for (int e = 0; e < 8; ++e) u[e] = (kk[e + 3] * s0 + kk[e + 2] * s1 + kk[e + 1] * s2 + kk[e] * s3) * vv[e];
    const u32 o = (u32)((2 * tb64 + tbl) * TB + (64 * role + 16 * chg + i16) * 64 + G * 16);
    __builtin_amdgcn_raw_buffer_store_b128(u32x4{pk(u[0], u[1]), pk(u[2], u[3]), pk(u[4], u[5]), pk(u[6], u[7])}, rw, WS_U + o, 0, SC1);
    SB();
    VMW(9); bar();
    const u32x2 x0 = trd(lds, L_X + rb + sv), x1 = trd(lds, L_X + rb + 512 + sv);
    __builtin_amdgcn_raw_buffer_store_b128(cat(x0, x1), rw, WS_X + o, 0, SC1);
    VMW(6); PIN(fhi); PIN(flo); PIN(Dh); PIN(e0);
    SB();
  }
  const u64 tag = mktag(e0);
  const float Dv = bflo(Dh);
  SB();
  w1_issue<2>(w1, L_RING); w1_issue<3>(w1, L_RING + 16384);
  VMW(4); bar();
  if (tid == 0) setflag(ws, WS_F1 + (tb64 * 8 + role) * 128, tag);
  const int hh = l >> 5, tk = l & 31;
  const u32 gso = WS_G + (u32)(((2 * gi * 2 + (tk >> 4)) * 56 + 4 * gj + (w >> 1)) * 1024 + (2 * (w & 1) + hh) * 256 + i16 * 16);
  auto gofs = [](int g) AI { return (u32)((32 * (g >> 1) + (g & 1)) * 2 * 56 * 1024); };
  u32x4 W[28];
  f32x4 cs = {};
  const u32 sel = i16 == (G & 1) ? ONES : 0u;
  auto colsum = [&](int k0, int k1) AI {
#pragma unroll
    for (int k = k0; k < k1; ++k) cs = mfma16(u32x4{sel, sel, sel, sel}, W[k], cs);
  };
  const u32 ra0 = opq(w1.ra), ra1 = opq(w1.ra + L_RING), ra2 = opq(w1.ra + L_RING + 49152);
  w1_read<0>(lds, ra0 + L_FC, W); w1_read<1>(lds, ra0 + L_FC + 16384, W);
  w1_issue<7>(w1, L_RED); w1_issue<8>(w1, L_RED + 16384);
  bar();
  {
    const u32 a0 = __builtin_amdgcn_alignbit(flo.y, flo.x, 16), a1 = __builtin_amdgcn_alignbit(fhi.x, flo.y, 16), a2 = __builtin_amdgcn_alignbit(fhi.y, fhi.x, 16),
              a3 = __builtin_amdgcn_alignbit(fhi.z, fhi.y, 16), a4 = __builtin_amdgcn_alignbit(fhi.w, fhi.z, 16);
    char* fc = lds + L_FC + cch * FCH + (vp * 64 + l) * 16 + 32;
    *(u32x4*)(fc) = fhi;
    *(u32x4*)(fc + FCS) = u32x4{a1, a2, a3, a4};
    *(u32x4*)(fc + 2 * FCS) = u32x4{flo.y, fhi.x, fhi.y, fhi.z};
    *(u32x4*)(fc + 3 * FCS) = u32x4{a0, a1, a2, a3};
    if (tid >= 128 && tid < 144) *(u32x4*)(lds + L_FC + (tid & 7) * FCS + ((tid >> 3) & 1) * 16) = u32x4{0, 0, 0, 0};
    if (tid >= 192 && tid < 252) { const int z = tid - 192; *(u32x4*)(lds + L_U + (z / 30) * UCH + (z % 30) * 16) = u32x4{0, 0, 0, 0}; }
  }
  SB();
  colsum(0, 4);
  w1_issue<4>(w1, L_RING + 32768); w1_issue<5>(w1, L_RING + 49152); w1_issue<6>(w1, L_RING + 65536);
  SB();

  const u32 lo16 = l * 16;
  const u32 bta = (u32)((8 * (G >> 1) + q4) * 64 + (G & 1) * 32 + p4 * 8);
  auto hbase = [](int g) AI { return g == 0 ? L_H0 : g == 1 ? L_H1 : g == 2 ? L_H2 : L_H3; };
  const u32 gsv = opq(gso);
  u32 ba = opq(hbase(0) + bta);
  const int ri = 2 * xc + sc / 14, rq2 = (sc / 7) & 1, n2 = sc % 7;
  u32x4 rd = srd(gwd, D_ * F_ * 2); asm volatile("" : "+s"(rd));
  const u32 wdv = opq((u32)(((224 * w + r8) * D_ + 64 * n2) * 2 + (((l & 7) ^ (((r8 >> 1) & 3) << 1)) << 4)));
  const u32 blkR = w < 7 ? L_R + w * 4096 : L_R2;
  u32 wda[4], wdr[4];
#pragma unroll
  for (int nt = 0; nt < 4; ++nt) {
    const u32 a = (16 * (G >> 1) + 4 * (G & 1) + q4) * 128 + ((nt ^ (2 * (G & 1) + (q4 >> 1))) * 32) + p4 * 8;
    wda[nt] = opq(a + L_P + w * 4096); wdr[nt] = opq(a + blkR);
  }
  const int j0 = (14 * w) >> 3, jn = ((14 * w + 13) >> 3) - j0;
  const u32 f3o = opq(WS_F3 + (u32)(ri * 56 + j0 + ((l & 3) < jn ? (l & 3) : jn)) * 128);

  const u32 hro = opq(WS_H + (u32)((32 * hh + (w >> 2) + 2 * ri) * TB + (64 * n2 + 16 * (w & 3) + ((l >> 1) & 15)) * 64 + rq2 * 32 + (l & 1) * 16));
  const u32 s2o = opq(WS_S2 + (u32)(2 * gi * 512 + tk * 16));
  const u32 hp = (u32)(w < 4 ? 4 * w : 4 + 3 * w) * 1024;
  u32 hs[4];
#pragma unroll
  for (int i = 0; i < 4; ++i) { hs[i] = WS_H + 2 * gi * TB + (wrap((hp >> 10) + i + 2 * w1.rot, 28) - i) * 1024; asm volatile("" : "+s"(hs[i])); }
  SB();
  poll(ws, WS_F1 + (u32)(32 * w + 8 * (l & 3) + ((l & 4) ? 7 : (cp >> 5))) * 128, l < 8, tag);
  u32x4 uu = ldg(dw, eo, WS_U), ex = ldg(dw, eo, WS_X);
  u32x4 est[4];
#pragma unroll
  for (int i = 0; i < 4; ++i) est[i] = ldg(dw, (u32)(r8 * 64 + p4 * 16), WS_ST + (u32)(w * 2048 + i * 512));
  bar();
  w1_read<7>(lds, ra0 + L_RED, W); w1_read<8>(lds, ra0 + L_RED + 16384, W);
  w1_read<2>(lds, ra1, W); w1_read<3>(lds, ra1 + 16384, W); w1_read<4>(lds, ra1 + 32768, W); w1_read<5>(lds, ra2, W); w1_read<6>(lds, ra2 + 16384, W);
  const u32 aA = opq(L_FC + cch * FCH + q4 * FCS + 64 - 16 * G + 8 * p4 - 8 + 128 * vp);
  const u32 aB = opq(L_U + cch * UCH + 32 * i16 + 16 * G + 448 - 128 * vp - 64);
  const u32 rwr = opq(L_RED + w * 4096 + G * 256 + ((i16 ^ ((G >> 1) << 3)) << 4));
  const u32 rr = opq(L_RED + ech * 4096 + (w & 3) * 1024 + (p4 & 1) * 512 + (((2 * r8 + (p4 >> 1)) ^ ((p4 & 1) << 3)) << 4));
  colsum(4, 18);
  PIN(cs);
  SB();
  vmw<5>(uu);
  *(u32x4*)(lds + L_U + ech * UCH + 480 + (8 * w + r8) * 64 + p4 * 16) = uu;
  bar();
  auto rdA = [&](int v, int m) AI { const u32 o = 64 * v + 512 * m; return cat(trd(lds, aA + o + 8), trd(lds, aA + o)); };
  auto rdB = [&](int v, int m) AI { return *(const u32x4*)(lds + aB + 64 * (1 - v) + 512 * m); };
  float ex8[8], eq8[8], eu8[8];
  auto terms = [&]() AI {
#pragma unroll
    for (int i = 0; i < 4; ++i) {
      ex8[2 * i] = bflo(ex[i]); ex8[2 * i + 1] = bfhi(ex[i]);
      eu8[2 * i] = bflo(uu[i]) * Dv; eu8[2 * i + 1] = op(bfhi(uu[i]) * Dv);
      eq8[2 * i] = (ex8[2 * i] - __uint_as_float(est[i].x)) * __uint_as_float(est[i].y);
      eq8[2 * i + 1] = op(op(ex8[2 * i + 1] - __uint_as_float(est[i].z)) * __uint_as_float(est[i].w));
    }
#pragma unroll
    for (int i = 0; i < 8; ++i) { PIN(ex8[i]); PIN(eq8[i]); PIN(eu8[i]); }
  };
  auto epilogue = [&]() AI {
    f32x4 p[8];
#pragma unroll
    for (int n = 0; n < 4; ++n) { p[2 * n] = *(const f32x4*)(lds + rr + n * 8192); p[2 * n + 1] = *(const f32x4*)(lds + rr + n * 8192 + 256); }
    float h[8], y0[8], y1[8];
#pragma unroll
    for (int i = 0; i < 8; ++i) y0[i] = op(eu8[i] + p[i >> 2][i & 3]);
    SB();
#pragma unroll
    for (int i = 0; i < 8; ++i) y0[i] = op(y0[i] + p[2 + (i >> 2)][i & 3]);
    SB();
#pragma unroll
    for (int i = 0; i < 8; ++i) y1[i] = op(p[4 + (i >> 2)][i & 3] + p[6 + (i >> 2)][i & 3]);
    SB();
#pragma unroll
    for (int i = 0; i < 8; ++i) y0[i] = op(y0[i] + y1[i]);
    SB();
#pragma unroll
    for (int i = 0; i < 8; ++i) h[i] = op(ex8[i] + eq8[i] * y0[i]);
    SB();
    __builtin_amdgcn_raw_buffer_store_b128(u32x4{pk(h[0], h[1]), pk(h[2], h[3]), pk(h[4], h[5]), pk(h[6], h[7])}, rw, WS_H + eo, 0, SC1);
  };
  u64 fl;
  const u32 f2o = WS_F2 + (u32)(8 * (w & 3) + (l & 7)) * 128;
  {
    f32x4 acc[4];
    u32x4 A[2][4], B[2][4];
#pragma unroll
    for (int v = 0; v < 2; ++v) {
      A[v][0] = rdA(v, 0); SB();
#pragma unroll
      for (int m = 0; m < 4; ++m) { B[v][m] = rdB(v, m); SB(); }
#pragma unroll
      for (int m = 1; m < 4; ++m) { A[v][m] = rdA(v, m); SB(); }
      if (v == 0) {
        vmw<0>(ex); PIN(est[0]); PIN(est[1]); PIN(est[2]); PIN(est[3]);
        fl = flag_ld(dw, f2o);
        terms();
        SB();
      }
    }
#pragma unroll
    for (int v = 0; v < 2; ++v)
#pragma unroll
      for (int m = 0; m < 4; ++m) {
#pragma unroll
        for (int c = m; c < 4; ++c) acc[c] = mfma16(A[v][m], B[v][c - m], (v == 0 && m == 0) ? f32x4{} : acc[c]);
        SB();
        if (v == 0 && m == 0) w1_g<9>(w1, L_RING);
        if (v == 0 && m == 2) w1_u<9>(w1, L_RING);
        if (v == 1 && m == 0) w1_g<10>(w1, L_RING + 16384);
        SB();
      }
#pragma unroll
    for (int c = 0; c < 4; ++c) *(f32x4*)(lds + rwr + c * 1024) = acc[c];
  }
  VMWP(3, fl);
  if (RARE(!__all(fl == tag))) poll<2>(ws, f2o, true, tag);
  bar();
  if (w < 4) epilogue();
  SB();
  {
    f32x4 acc[4];
    u32x4 A[2][8], B[2][8];
    auto rd = [&](int v, int m) AI {
      if (m == 0) {
        A[v][0] = rdA(v, 0);
#pragma unroll
        for (int d = 4; d < 8; ++d) B[v][d] = rdB(v, d);
      } else { A[v][m] = rdA(v, m); if (m <= 4) B[v][4 - m] = rdB(v, 4 - m); }
      SB();
    };
    rd(0, 0); rd(0, 1); rd(0, 2); rd(0, 3);
#pragma unroll
    for (int v = 0; v < 2; ++v) {
#pragma unroll
      for (int m = 0; m < 8; ++m) {
#pragma unroll
        for (int c = 4; c < 8; ++c) if (m <= c) acc[c - 4] = mfma16(A[v][m], B[v][c - m], (v == 0 && m == 0) ? f32x4{} : acc[c - 4]);
        SB();
        if (m < 4) rd(v, m + 4); else if (v == 0) rd(1, m - 4);
        if (v == 0) {
          if (m == 0) w1_u<10>(w1, L_RING + 16384);
          if (m == 1) w1_g<11>(w1, L_RING + 32768);
          if (m == 2) w1_u<11>(w1, L_RING + 32768);
          if (m == 3) w1_g<12>(w1, L_RING + 49152);
          if (m == 4) w1_u<12>(w1, L_RING + 49152);
          if (m == 6) w1_g<13>(w1, L_RING + 65536);
          if (m == 7) w1_u<13>(w1, L_RING + 65536);
        }
        SB();
      }
    }
    VMW(0); bar();
#pragma unroll
    for (int c = 0; c < 4; ++c) *(f32x4*)(lds + rwr + c * 1024) = acc[c];
  }
  if (w < 4) {
#pragma unroll
    for (int k = 0; k < 7; ++k) dma<SC1>(dw, lb + L_H0 + (7 * w + k) * 1024, lo16, WS_H + (u32)(2 * gi * TB) + wrap(7 * w + k + 2 * w1.rot, 28) * 1024);
  }
  w1_read<9>(lds, ra1, W); w1_read<10>(lds, ra1 + 16384, W); w1_read<11>(lds, ra1 + 32768, W); w1_read<12>(lds, ra2, W); w1_read<13>(lds, ra2 + 16384, W);
  bar();
  if (w >= 4) epilogue();
  SB();

  u32 csh, csl;
  auto colfin = [&]() AI {
    colsum(18, 28);
    const auto sw = __builtin_amdgcn_permlane16_swap(__float_as_uint(cs.x), __float_as_uint(cs.y), false, false);
    const float t = __uint_as_float(sw[0]);
    const u32 hi = pk(t, t);
    csh = hh ? 0u : hi; csl = hh ? 0u : pk(t - bflo(hi), 0.f);
    PIN(csh); PIN(csl);
  };
  f32x4 tm; float gu[8], gt[8]; u32 od[4];
  auto center = [&](f32x16& acc, float m) AI {
    const u32 d = __float_as_uint(m);
    acc = mfma32(u32x4{csh, csl, 0, 0}, u32x4{d, d & 0xffffu, 0, 0}, acc);
  };
  auto geglu_step = [&](int g, f32x16& acc, int i) AI {
#pragma unroll
    for (int e = 0; e < 8; ++e) {
      const int s = i - (2 + (18 * e) / 7);
      if (s == 0) { gt[e] = acc[e] * acc[e]; PIN(gt[e]); }
      if (s == 1) { gt[e] = tm.w * gt[e] + tm.z; gu[e] = acc[e] * acc[e + 8]; PIN(gt[e]); PIN(gu[e]); }
      if (s == 2) { gt[e] = acc[e] * gt[e]; PIN(gt[e]); }
      if (s == 3) { gt[e] = __builtin_amdgcn_exp2f(gt[e]); PIN(gt[e]); }
      if (s == 4) { gt[e] = gt[e] * tm.y + tm.y; PIN(gt[e]); }
      if (s == 5) { gt[e] = __builtin_amdgcn_rcpf(gt[e]); PIN(gt[e]); }
      if (s == 6) { gu[e] = gu[e] * gt[e]; PIN(gu[e]); }
      if (s == 7 && (e & 1)) { od[e >> 1] = pk(gu[e - 1], gu[e]); PIN(od[e >> 1]); }
    }
    if (i == 28) {
      __builtin_amdgcn_raw_buffer_store_b128(u32x4{od[0], od[1], od[2], od[3]}, rw, gsv, gofs(g), SC1);
    }
  };
  constexpr int PF = 4;
  u32x4 B[PF];
  auto chain = [&](int g, f32x16& acc, auto side) AI {
    const u32 bn = opq(hbase(g < 3 ? g + 1 : g) + bta);
    sfor<28>([&](auto kc) AI {
      constexpr int k = decltype(kc)::value;
      if (!(k & 1)) { asm volatile("" :: "v"(B[(k + 1) % PF])); SB(); }
      acc = mfma32(W[k], B[k % PF], k == 0 ? f32x16{} : acc);
      SB();
      side(k);
      if (k + PF < 28) B[k % PF] = cat(trd(lds, ba + (k + PF) * 1024), trd(lds, ba + (k + PF) * 1024 + 256));
      else if (g < 3) B[k % PF] = cat(trd(lds, bn + (k + PF - 28) * 1024), trd(lds, bn + (k + PF - 28) * 1024 + 256));
      SB();
    });
    ba = bn;
  };
  auto flag3 = [&](int g) AI { setflag(ws, WS_F3 + (u32)(((gi * 4 + g) * 14) + gj) * 128, tag); };
  u32x4 Wd[7][4];
  auto wblk = [](int k) AI { return (k == 1 || k == 4) ? 1 : (k == 3 || k == 6) ? 2 : 0; };
  auto wd_dma = [&](int k, int i) AI {
    if (i == 0) dma_at(lb + (wblk(k) == 0 ? blkR : (wblk(k) == 2 ? L_Q : L_P) + w * 4096));
    dma_i<0>(i, rd, wdv, 4 * k * (8 * D_ * 2) + i * (8 * D_ * 2 - 1024));
  };
  auto wd_rd = [&](int k, int nt) AI {
    const u32 a = wblk(k) == 0 ? wdr[nt] : wda[nt] + (wblk(k) == 2 ? L_Q - L_P : 0);
    Wd[k][nt] = cat(trd(lds, a), trd(lds, a + 1024));
  };
  auto wd_read = [&](int k) AI { wd_rd(k, 0); wd_rd(k, 1); wd_rd(k, 2); wd_rd(k, 3); };

  f32x16 accA, accB;
  u32x4 tm0, tm1, tm2, tm3, hr;
  u32x2 res[2];
  auto htb = [&](int g) AI { return WS_H + (u32)((32 * (g >> 1) + 2 * gi + (g & 1)) * TB); };
  auto h_dma = [&](int g, int i) AI {
    if (i == 0) dma_at(lb + hbase(g) + hp);
    if (i < 3 || w < 4) dma_i<SC1>(i, dw, lo16, hs[i] + (32 * (g >> 1) + (g & 1)) * TB);
  };
  auto redo1 = [&](int g, f32x16& acc, u32 m) AI {
    if (RARE(__any(acc[0] != acc[0]))) {
      const u32 sc0 = L_SP + w * 1024;
      acc = f32x16{};
      const rsrc_t rg = mk(gwg, D_ * F_ * 2), ru = mk(gwu, D_ * F_ * 2);
#pragma unroll 1
      for (int k = 0; k < 28; ++k) {
        const u32 wo = (u32)(((16 * k + ((l >> 1) & 15)) * F_ + 128 * gj + 16 * w) * 2 + (l & 1) * 16);
        const u32x4 wg = __builtin_amdgcn_raw_buffer_load_b128(rg, wo, 0, 0), wu = __builtin_amdgcn_raw_buffer_load_b128(ru, wo, 0, 0);
        *(u32x4*)(lds + sc0 + lo16) = l < 32 ? wg : wu;
        const u32 wa = sc0 + (G & 1) * 512 + (8 * (G >> 1) + q4) * 32 + p4 * 8;
        const u32x4 a = cat(trd(lds, wa), trd(lds, wa + 128));
        u32x4 v;
#pragma unroll 1
        for (int it = 0; it < 400; ++it) { v = ldg(dw, lo16, htb(g) + k * 1024); vmw<0>(v); if (!__any(max4(v) == EMPTY)) break; }
        *(u32x4*)(lds + sc0 + lo16) = v;
        acc = mfma32(a, cat(trd(lds, sc0 + bta), trd(lds, sc0 + bta + 256)), acc);
      }
      center(acc, __uint_as_float(m));
      SB();
#pragma unroll
      for (int i = 0; i < 12; ++i) __builtin_amdgcn_s_setprio(1);
      SB();
    }
  };
  auto tm_fix = [&](u32x4& t, u32 so) AI {
    if (RARE(__any(max4(t) == EMPTY))) {
#pragma unroll 1
      for (int it = 0; it < 4000; ++it) { t = ldg(dw, s2o, so); vmw<0>(t); if (!__any(max4(t) == EMPTY)) break; }
    }
  };
  SB();
  colfin();
  SB();
  if (w < 4) {
    const u32 ha = opq(L_H0 + 7 * w * 1024 + lo16);
#pragma unroll 1
    for (int it = 0; it < 4000; ++it) {
      VMW(0);
      u32 m = 0;
#pragma unroll
      for (int k = 0; k < 7; ++k) { const u32x4 v = *(const u32x4*)(lds + ha + k * 1024); m = max(m, max4(v)); }
      if (!__any(m == EMPTY)) break;
#pragma unroll
      for (int k = 0; k < 7; ++k) dma<SC1>(dw, lb + L_H0 + (7 * w + k) * 1024, lo16, WS_H + (u32)(2 * gi * TB) + wrap(7 * w + k + 2 * w1.rot, 28) * 1024);
    }
    VMW(0);
  }
  __builtin_amdgcn_s_setprio(1);
  bar();
#pragma unroll
  for (int k = 0; k < PF; ++k) B[k] = cat(trd(lds, ba + k * 1024), trd(lds, ba + k * 1024 + 256));
  SB();
  chain(0, accA, [&](int k) AI {
    if (k <= 6 && !(k & 1)) h_dma(1, k >> 1);
    if (k >= 8 && k <= 14 && !(k & 1)) wd_dma(0, (k - 8) >> 1);
    if (k == 15) tm0 = ldg(dw, s2o, 0);
    if (k == 18 || k == 20 || k == 22) __builtin_amdgcn_raw_buffer_store_b128(u32x4{EMPTY, EMPTY, EMPTY, EMPTY}, rw, gsv, gofs((k - 16) >> 1), SC1);
    if (k == 23) { VMW(8); RBAR(); if (tid == 0 && b == 0) setflag(ws, WS_E, tag); }
    if (k == 27) { vmw<3>(tm0); tm_fix(tm0, 0); }
  });
  center(accA, __uint_as_float(tm0.x));
  tm = __builtin_bit_cast(f32x4, tm0);
  SB();
  chain(1, accB, [&](int k) AI {
    if (k == 1) RBAR();
    geglu_step(0, accA, k);
    if (k == 2) tm1 = ldg(dw, s2o, 512);
    if (k == 3 || k == 5) wd_dma(1, (k - 3) >> 1);
    if (k >= 7 && k <= 10) h_dma(2, k - 7);
    if (k == 12) dma_at(lb + L_P + w * 4096);
    if (k == 13 || k == 15) wd_dma(1, (k - 9) >> 1);
    if (k >= 12 && k <= 18 && !(k & 1)) wd_rd(0, (k - 12) >> 1);
    if (k == 17) { vmw<7>(tm1); tm_fix(tm1, 512); }
    if (k >= 21 && (k & 1)) wd_dma(2, (k - 21) >> 1);
    if (k == 23) { VMW(4); RBAR(); }
  });
  center(accB, __uint_as_float(tm1.x));
  geglu_step(0, accA, 28);
  tm = __builtin_bit_cast(f32x4, tm1);
  SB();
  chain(2, accA, [&](int k) AI {
    if (k == 1) RBAR();
    if (k == 2) redo1(1, accB, tm1.x);
    geglu_step(1, accB, k);
    if (k <= 6 && !(k & 1)) h_dma(3, k >> 1);
    if (k >= 3 && k <= 9 && (k & 1)) { if (k == 3) VMW(7); wd_rd(1, (k - 3) >> 1); }
    if (k == 7) hr = ldg(dw, hro, 0);
    if (k == 8) { tm2 = ldg(dw, s2o, 32 * 512); tm3 = ldg(dw, s2o, 33 * 512); }
    if (k >= 10 && k <= 16 && !(k & 1)) wd_dma(3, (k - 10) >> 1);
    if (k >= 18 && k <= 27 && k % 3 == 0) wd_dma(4, (k - 18) / 3);
    if (k == 13) { VMW(8); RBAR(); if (tid == 0) flag3(0); }
    if (k == 23) { vmw<6>(tm2); PIN(tm3); PIN(hr); RBAR(); }
    if (k == 25) tm_fix(tm2, 32 * 512);
    if (k == 26) tm_fix(tm3, 33 * 512);
  });
  center(accA, __uint_as_float(tm2.x));
  geglu_step(1, accB, 28);
  tm = __builtin_bit_cast(f32x4, tm2);
  SB();
  const u32 gb = WS_G + (u32)((2 * ri * 2 + rq2) * 56 + 7 * w) * 1024;
  constexpr int NS = 6 + 2;
  u32x4 Hf[NS];
  auto hl = [&](int t) AI { Hf[t % NS] = ldg(dw, lo16, gb + gofs(t / 7) + (t % 7) * 1024); };
  chain(3, accB, [&](int k) AI {
    if (k == 2) redo1(2, accA, tm2.x);
    geglu_step(2, accA, k);
    if (k >= 5 && k < 13) { if (k == 5) VMW(5); wd_rd(2 + (k - 5) / 4, (k - 5) & 3); }
    if (k == 10) fl = flag_ld(dw, f3o);
    if (k >= 12 && k <= 18 && !(k & 1)) wd_dma(5, (k - 12) >> 1);
    if (k == 13) VMW(3);
    if (k >= 13 && k < 21 && (k & 1)) wd_rd(4, (k - 13) >> 1);
    if (k >= 19 && k <= 22) wd_dma(6, k - 19);
    if (k == 23) { VMWP(8, fl); if (RARE(!__all(fl == tag))) poll(ws, f3o, true, tag); }
    if (k >= 23) hl(k - 23);
  });
  center(accB, __uint_as_float(tm3.x));
  geglu_step(2, accA, 28);
  tm = __builtin_bit_cast(f32x4, tm3);
  redo1(3, accB, tm3.x);
#pragma unroll
  for (int e = 0; e < 8; ++e) {
    const float a = accB[e], u = accB[e + 8];
    gu[e] = (a * u) * __builtin_amdgcn_rcpf(__builtin_amdgcn_exp2f(a * (tm.w * (a * a) + tm.z)) * tm.y + tm.y);
  }
#pragma unroll
  for (int e = 0; e < 4; ++e) od[e] = pk(gu[2 * e], gu[2 * e + 1]);
  geglu_step(3, accB, 28);
  __builtin_amdgcn_s_setprio(0);
  SB();
  f32x4 acc[2][4];
  const rsrc_t ro = mk(gout, S_ * D_ * 2);
  auto pbuf = [](int g) AI { return g == 3 ? L_T : g == 1 ? L_Q : L_P; };
  const u32 pa = (u32)(w * 4096 + i16 * 256), tf = 2 * w + hh;
  const u32 fa = opq(L_P + tf * 256 + (((tk >> 1) ^ (tf & 7)) << 4) + (tk & 1) * 8), ft = opq(fa + (L_T - L_P));
  const u32 oa = opq((u32)(((64 * ri + 16 * rq2 + tf) * D_ + 64 * n2 + 2 * tk) * 2));
  u32 pq[4];
#pragma unroll
  for (int nt = 0; nt < 4; ++nt) pq[nt] = opq(L_P + pa + (((4 * nt + G) ^ (i16 & 7)) << 4));
  auto put = [&](int g, int nt) AI {
    f32x4& v = acc[g & 1][nt];
    if (w == 4 * (g & 1) + nt) { const u32x2 r = res[g >> 1]; v.x += bflo(r.x); v.y = op(v.y + bfhi(r.x)); v.z += bflo(r.y); v.w = op(v.w + bfhi(r.y)); }
    *(f32x4*)(lds + (g == 3 ? opq(pq[nt] + (L_T - L_P)) : pq[nt] + (pbuf(g) - L_P))) = v;
  };
  float2 p[8];
  auto fin_rd = [&](int g) AI {
#pragma unroll
    for (int n = 0; n < 8; ++n) { const u32x2 x = *(const u32x2*)(lds + (g == 3 ? ft : fa + (pbuf(g) - L_P)) + n * 4096); p[n].x = __uint_as_float(x.x); p[n].y = __uint_as_float(x.y); }
  };
  auto fin_st = [&](int g) AI {
    const float a = ((p[0].x + p[1].x) + (p[2].x + p[3].x)) + ((p[4].x + p[5].x) + (p[6].x + p[7].x));
    const float c = ((p[0].y + p[1].y) + (p[2].y + p[3].y)) + ((p[4].y + p[5].y) + (p[6].y + p[7].y));
    __builtin_amdgcn_raw_buffer_store_b32(pk(a, c), ro, oa, (32 * (g >> 1) + (g & 1)) * (32 * D_ * 2), 2);
  };
  auto redo = [&](int g) AI {
    if (RARE(__any(acc[g & 1][0].x != acc[g & 1][0].x))) {
      u32x4 v[7];
#pragma unroll 1
      for (int it = 0; it < 4000; ++it) {
        u32 m = 0;
#pragma unroll
        for (int k = 0; k < 7; ++k) v[k] = ldg(dw, lo16, gb + gofs(g) + k * 1024);
#pragma unroll
        for (int k = 0; k < 7; ++k) { vmw<0>(v[k]); m = max(m, max4(v[k])); }
        if (!__any(m == EMPTY)) break;
      }
#pragma unroll
      for (int k = 0; k < 7; ++k)
#pragma unroll
        for (int nt = 0; nt < 4; ++nt) acc[g & 1][nt] = mfma16(Wd[k][nt], v[k], k == 0 ? f32x4{} : acc[g & 1][nt]);
      SB();
#pragma unroll
      for (int i = 0; i < 8; ++i) __builtin_amdgcn_s_setprio(0);
      SB();
    }
  };
#pragma unroll
  for (int h = 5; h < 6; ++h) hl(h);
  if (RARE(__any(max4(hr) == EMPTY))) {
#pragma unroll 1
    for (int it = 0; it < 4000; ++it) { hr = ldg(dw, hro, 0); vmw<0>(hr); if (!__any(max4(hr) == EMPTY)) break; }
  }
  *(u32x4*)(lds + L_SP + w * 1024 + lo16) = hr;
  { const u32 a = L_SP + w * 1024 + (4 * G + q4) * 32 + p4 * 8; res[0] = trd(lds, a); res[1] = trd(lds, a + 512); }
  VMW(6 + 2);
  wd_read(5);
  SB();
  sfor<28>([&](auto tc) AI {
    constexpr int t = decltype(tc)::value, g = t / 7, k = t % 7;
    constexpr auto last = [](int u) { return u < 28 - 6 ? u + 6 : 27; };
    constexpr int n = last(t) - t + (t < 5 ? 2 : 0) + (t > 14 && t - 6 <= 14 ? 1 : 0) + (t > 21 ? 1 : 0);
    if (t < 28 - 6) hl(t + 6);
    vmw<n>(Hf[t % NS]);
#pragma unroll
    for (int nt = 0; nt < 4; ++nt) acc[g & 1][nt] = mfma16(Wd[k][nt], Hf[t % NS], k == 0 ? f32x4{} : acc[g & 1][nt]);
    SB();
    if (t < 4) wd_rd(6, t);
    if (t == 15 || t == 22) redo(g - 1);
    if (g > 0 && k >= 1 && k <= 4) put(g - 1, k - 1);
    if (g > 0 && k == 5) { bar(); fin_rd(g - 1); }
    if (t == 14 || t == 21) fin_st(g - 2);
    SB();
  });
  fin_st(2);
  redo(3);
  put(3, 0); put(3, 1); put(3, 2); put(3, 3);
  bar();
  fin_rd(3); fin_st(3);
}

}

extern "C" void kernel_launch(void* const* d_in, const int* in_sizes, int n_in, void* d_out, int out_size, void* d_ws, size_t ws_size,
                              hipStream_t stream) {
  h3_ffn<<<256, 512, 0, stream>>>((const char*)d_in[0], (const char*)d_in[1], (const char*)d_in[2], (const char*)d_in[3], (const char*)d_in[4],
                                  (const char*)d_in[5], (const char*)d_in[7], (const char*)d_in[8], (const char*)d_in[9], (char*)d_out, (char*)d_ws);
}
```
